```python
import torch

def _softmax(x, axis=-1):
    return torch.softmax(x, dim=axis)
def _gelu(x):
    return torch.nn.functional.gelu(x, approximate="tanh")
def _silu(x):
    return torch.nn.functional.silu(x)
def _sigmoid(x):
    return torch.sigmoid(x)
def _layernorm(x, axis=-1, eps=1e-5):
    m = x.mean(dim=axis, keepdim=True)
    v = x.var(dim=axis, keepdim=True, unbiased=False)
    return (x - m) / torch.sqrt(v + eps)
def _scatter_add(tgt, idx, src, axis=0):
    return tgt.clone().index_add_(axis, idx, src)
def _segment_sum(src, seg, n):
    out = torch.zeros((n,) + src.shape[1:], dtype=src.dtype)
    return out.index_add_(0, seg, src)
def _pad(x, pw):
    flat = tuple(v for pair in reversed(pw) for v in pair)
    return torch.nn.functional.pad(x, flat)

kernel_name = 'interleave_layers__ssm_conv_s4d__glu_gated_geglu__prompt'

def setup_inputs():

    _w = (lambda a: (lambda t: t.to(torch.bfloat16) if t.is_floating_point() else t)(torch.as_tensor(a)))
    def _A_setup():
        Bsz, L, D, N = 1, 2048, 512, 128
        return {
            "u": _w((lambda *s: torch.randn(*s, dtype=torch.bfloat16) * 0.05)(Bsz, L, D)),
            "log_A_real": _w((lambda *s: torch.randn(*s, dtype=torch.bfloat16) * 3.2)(D, N) * 0.5),
            "A_imag": _w((lambda *s: torch.randn(*s, dtype=torch.bfloat16) * 0.05)(D, N) * 3.14),
            "C_real": _w((lambda *s: torch.randn(*s, dtype=torch.bfloat16) * 0.05)(D, N) / N ** 0.5),
            "C_imag": _w((lambda *s: torch.randn(*s, dtype=torch.bfloat16) * 0.05)(D, N) / N ** 0.5),
            "log_dt": _w((lambda *s: torch.randn(*s, dtype=torch.bfloat16) * 0.05)(D) * 0.5 - 4.0),
            "D_skip": _w((lambda *s: torch.randn(*s, dtype=torch.bfloat16) * 0.05)(D)),
        }
    _A_in = _A_setup()

    def _B_setup():
        B, S, Dm, Df = 1, 2048, 512, 1376
        return {
            "x": _w((lambda *s: torch.randn(*s, dtype=torch.bfloat16) * 0.05)(B, S, Dm)),
            "w_gate": _w((lambda *s: torch.randn(*s, dtype=torch.bfloat16) * 0.8)(Dm, Df) * (Dm ** -0.5)),
            "w_up": _w((lambda *s: torch.randn(*s, dtype=torch.bfloat16) * 0.8)(Dm, Df) * (Dm ** -0.5)),
            "w_down": _w((lambda *s: torch.randn(*s, dtype=torch.bfloat16) * 0.8)(Df, Dm) * (Df ** -0.5)),
        }
    _B_in = _B_setup()

    return {
        "A_u": _A_in["u"],
        "A_log_A_real": _A_in["log_A_real"],
        "A_A_imag": _A_in["A_imag"],
        "A_C_real": _A_in["C_real"],
        "A_C_imag": _A_in["C_imag"],
        "A_log_dt": _A_in["log_dt"],
        "A_D_skip": _A_in["D_skip"],
        "B_x": _B_in["x"],
        "B_w_gate": _B_in["w_gate"],
        "B_w_up": _B_in["w_up"],
        "B_w_down": _B_in["w_down"],
    }

def reference(A_u, A_log_A_real, A_A_imag, A_C_real, A_C_imag, A_log_dt, A_D_skip, B_x, B_w_gate, B_w_up, B_w_down):
    def _A_ref(u, log_A_real, A_imag, C_real, C_imag, log_dt, D_skip):
        L = u.shape[1]
        dt = torch.exp(log_dt)
        A = -torch.exp(log_A_real) + 1j * A_imag
        C = (C_real + 1j * C_imag) * dt[:, None]
        pos = (lambda x, dt: x.to(dt))(torch.arange(L), log_dt.dtype)
        P = torch.exp((A * dt[:, None])[:, :, None] * pos[None, None, :])
        K = 2.0 * torch.einsum("dn,dnl->dl", C, P).real
        L2 = 2 * L
        u_t = (lambda x, *d: x.permute(*d))(u, 0, 2, 1)
        Kf = (lambda x, *a, **k: torch.fft.rfft(x.float(), *a, **k))(K, L2)
        Uf = (lambda x, *a, **k: torch.fft.rfft(x.float(), *a, **k))(u_t, L2)
        y = (lambda x, *a, **k: torch.fft.irfft(x, *a, **k).to(torch.bfloat16))(Uf * Kf[None, :, :], L2)[:, :, :L]
        y = (lambda x, *d: x.permute(*d))(y, 0, 2, 1)
        return y + u * D_skip[None, None, :]

    def _B_ref(x, w_gate, w_up, w_down):
        g = torch.einsum("bsd,df->bsf", x, w_gate)
        u = torch.einsum("bsd,df->bsf", x, w_up)
        h = _gelu(g) * u
        return torch.einsum("bsf,fd->bsd", h, w_down)

    x = A_u
    x = x + _A_ref(_layernorm(x, axis=-1), A_log_A_real, A_A_imag, A_C_real, A_C_imag, A_log_dt, A_D_skip)
    x = x + _B_ref(_layernorm(x, axis=-1), B_w_gate, B_w_up, B_w_down)
    x = x + _A_ref(_layernorm(x, axis=-1), A_log_A_real, A_A_imag, A_C_real, A_C_imag, A_log_dt, A_D_skip)
    x = x + _B_ref(_layernorm(x, axis=-1), B_w_gate, B_w_up, B_w_down)
    return x
```

```cpp
#include <hip/hip_runtime.h>
#include <hip/hip_bf16.h>

typedef unsigned int u32;
typedef unsigned short u16;
typedef u32 u32x4 __attribute__((ext_vector_type(4)));
typedef u32 u32x2 __attribute__((ext_vector_type(2)));
typedef float f32x4 __attribute__((ext_vector_type(4)));
typedef float f32x2 __attribute__((ext_vector_type(2)));
typedef short s16x4 __attribute__((ext_vector_type(4)));
typedef __bf16 bf16x8 __attribute__((ext_vector_type(8)));
typedef __bf16 bf16x2 __attribute__((ext_vector_type(2)));
#define LAS __attribute__((address_space(3)))
#define GAS __attribute__((address_space(1)))
#define RLX_AGENT __ATOMIC_RELAXED, __HIP_MEMORY_SCOPE_AGENT
#define DEV __device__ __forceinline__

namespace {

constexpr int SEQ = 2048, DM = 512, NS = 128, DF = 1376;
constexpr int NBLK = 256;
constexpr int KS2 = 44;

constexpr u32 OFF_EPOCH = 0;
constexpr u32 OFF_FLAG = 4096;
constexpr u32 OFF_GFLAG = OFF_FLAG + 4 * 8192;
constexpr u32 OFF_ST1 = OFF_GFLAG + 4096;
constexpr u32 OFF_ST3 = OFF_ST1 + 16384;
constexpr u32 OFF_WGU = OFF_ST3 + 32768;
constexpr u32 OFF_WD = OFF_WGU + 16 * 11 * 16 * 1024;
constexpr u32 OFF_XC = OFF_WD + 32 * KS2 * 1024;
constexpr u32 OFF_X2 = OFF_XC + 2 * 2097152;
constexpr u32 OFF_H = OFF_X2 + 2097152;
constexpr u32 H_BYTES = 128 * KS2 * 1024;
constexpr u32 OFF_DBG = OFF_H + 2 * H_BYTES;
constexpr u32 WS_BYTES = OFF_DBG + 256 * 32 * 8;

constexpr int KC_COPY = 4160, KC_HALF = 4 * KC_COPY + 32, KC_CH = 33344;
constexpr int L_KC = 0;
constexpr int L_RING = 2 * KC_CH;
constexpr int L_MISC = L_RING + 5 * 16384;
constexpr int L_RES = L_MISC;
constexpr int L_ST = L_MISC + 8192;
constexpr int LDS_BYTES = 163840;
constexpr int L_U = L_RING;
constexpr int U_CH = 4608;
constexpr int L_P = L_RING + 16384;
constexpr int L_IMG1 = L_RING + 16384;
constexpr int L_IMG2 = L_RING + 32768;
constexpr int L_PAR = L_RING + 49152;
constexpr int L_KX = L_RING + 57344 + 1024;
constexpr int L_KT = L_RING + 65536;
constexpr int KT_CH = 772 * 4;

struct Params {
  const u16 *x0, *logA, *Aim, *Cre, *Cim, *logdt, *Dskip, *wg, *wu, *wd;
  u16* out;
  char* ws;
};

DEV float bf_lo(u32 v) { return __builtin_bit_cast(float, v << 16); }
DEV float bf_hi(u32 v) { return __builtin_bit_cast(float, v & 0xffff0000u); }
DEV float bf2f(u16 v) { return __builtin_bit_cast(float, (u32)v << 16); }
DEV u32 pk_bf16(float lo, float hi) {
  f32x2 f = {lo, hi};
  return __builtin_bit_cast(u32, __builtin_convertvector(f, bf16x2));
}
DEV float rnd_bf16(float v) { return bf_lo(pk_bf16(v, 0.f)); }
DEV f32x4 mfma_bf16(u32x4 a, u32x4 b, f32x4 c) {
  return __builtin_amdgcn_mfma_f32_16x16x32_bf16(__builtin_bit_cast(bf16x8, a), __builtin_bit_cast(bf16x8, b), c, 0, 0, 0);
}
DEV u32x2 tr_read(LAS char* p) {
  return __builtin_bit_cast(u32x2, __builtin_amdgcn_ds_read_tr16_b64_v4i16((LAS s16x4*)p));
}
DEV u32x4 tr_frag(LAS char* p) {
  u32x2 a = tr_read(p), b = tr_read(p + 512);
  u32x4 r = {a.x, a.y, b.x, b.y};
  return r;
}
template <int CTRL> DEV float dpp_f(float v) {
  return __builtin_bit_cast(float, __builtin_amdgcn_mov_dpp(__builtin_bit_cast(int, v), CTRL, 0xf, 0xf, true));
}
template <int CTRL> DEV u32 dpp_u(u32 v) { return (u32)__builtin_amdgcn_mov_dpp((int)v, CTRL, 0xf, 0xf, true); }
DEV void swap16(u32& a, u32& b) { auto r = __builtin_amdgcn_permlane16_swap(a, b, false, false); a = r[0]; b = r[1]; }
DEV void swap32(u32& a, u32& b) { auto r = __builtin_amdgcn_permlane32_swap(a, b, false, false); a = r[0]; b = r[1]; }
DEV void swap16f(float& a, float& b) { u32 x = __builtin_bit_cast(u32, a), y = __builtin_bit_cast(u32, b); swap16(x, y); a = __builtin_bit_cast(float, x); b = __builtin_bit_cast(float, y); }
DEV void swap32f(float& a, float& b) { u32 x = __builtin_bit_cast(u32, a), y = __builtin_bit_cast(u32, b); swap32(x, y); a = __builtin_bit_cast(float, x); b = __builtin_bit_cast(float, y); }
DEV float wave_sum(float v) {
  v += dpp_f<0xB1>(v);
  v += dpp_f<0x4E>(v);
  v += dpp_f<0x141>(v);
  v += dpp_f<0x140>(v);
  float a = v, b = v;
  swap16f(a, b); v = a + b;
  a = v; b = v;
  swap32f(a, b); v = a + b;
  return v;
}
#define BARRIER() __builtin_amdgcn_s_barrier()
#define LGKM0() asm volatile("s_waitcnt lgkmcnt(0)" ::: "memory")
#define SYNC_LDS() do { LGKM0(); BARRIER(); asm volatile("" ::: "memory"); } while (0)
template <int N> DEV void vm_wait() { asm volatile("s_waitcnt vmcnt(%0)" ::"i"(N) : "memory"); }

typedef __amdgpu_buffer_rsrc_t rsrc_t;
DEV u32x4 ld16(rsrc_t r, u32 voff, u32 soff) { return __builtin_amdgcn_raw_buffer_load_b128(r, voff, soff, 16); }
DEV void st16(rsrc_t r, u32 voff, u32 soff, u32x4 v) { __builtin_amdgcn_raw_buffer_store_b128(v, r, voff, soff, 16); }
DEV void st8(rsrc_t r, u32 voff, u32 soff, u32x2 v) { __builtin_amdgcn_raw_buffer_store_b64(v, r, voff, soff, 16); }

DEV void dma4(const char* base, u32 voffA, u32 voffB, u32 lds) {
  u32 keep;
  asm volatile(
      "s_mov_b32 %0, m0\n\ts_mov_b32 m0, %4\n\ts_nop 0\n\t"
      "global_load_lds_dwordx4 %1, %3 sc1\n\t"
      "global_load_lds_dwordx4 %1, %3 offset:1024 sc1\n\t"
      "global_load_lds_dwordx4 %2, %3 offset:2048 sc1\n\t"
      "global_load_lds_dwordx4 %2, %3 offset:3072 sc1\n\t"
      "s_mov_b32 m0, %0"
      : "=&s"(keep) : "v"(voffA), "v"(voffB), "s"(base), "s"(lds) : "memory");
}
DEV void dma2(const char* base, u32 voff, u32 lds) {
  u32 keep;
  asm volatile(
      "s_mov_b32 %0, m0\n\ts_mov_b32 m0, %3\n\ts_nop 0\n\t"
      "global_load_lds_dwordx4 %1, %2 sc1\n\t"
      "global_load_lds_dwordx4 %1, %2 offset:1024 sc1\n\t"
      "s_mov_b32 m0, %0"
      : "=&s"(keep) : "v"(voff), "s"(base), "s"(lds) : "memory");
}

constexpr u32 SPIN_CAP = 1u << 16;
constexpr int SYNC_WAVE = 3;

DEV void signal_all(char* ws, int edge, int b, int w, int lane, u32 epoch) {
  vm_wait<0>();
  BARRIER();
  if (w == SYNC_WAVE && lane < 8)
    __hip_atomic_store((GAS u32*)(ws + OFF_FLAG + edge * 8192 + lane * 1024 + b * 4), epoch, RLX_AGENT);
}
DEV void wait_all(rsrc_t rs, int edge, int b, int w, int lane, u32 epoch) {
  if (w == SYNC_WAVE) {
    for (u32 sp = 0; sp < SPIN_CAP; ++sp) {
      u32x4 f = ld16(rs, lane * 16, OFF_FLAG + edge * 8192 + (b & 7) * 1024);
      bool ok = f.x == epoch && f.y == epoch && f.z == epoch && f.w == epoch;
      if (__all(ok)) break;
      __builtin_amdgcn_s_sleep(1);
      asm volatile("" ::: "memory");
    }
  }
  asm volatile("" ::: "memory");
  BARRIER();
  asm volatile("" ::: "memory");
}
DEV void signal_grp(char* ws, int edge, int grp, int member, int w, int lane, u32 epoch) {
  vm_wait<0>();
  BARRIER();
  if (w == SYNC_WAVE && lane == 0)
    __hip_atomic_store((GAS u32*)(ws + OFF_GFLAG + edge * 2048 + grp * 128 + member * 4), epoch, RLX_AGENT);
}
DEV void wait_grp(char* ws, int edge, int grp, int w, int lane, u32 epoch) {
  if (w == SYNC_WAVE) {
    for (u32 sp = 0; sp < SPIN_CAP; ++sp) {
      u32 f = __hip_atomic_load((GAS u32*)(ws + OFF_GFLAG + edge * 2048 + grp * 128 + (lane & 15) * 4), RLX_AGENT);
      if (__all(f == epoch)) break;
      __builtin_amdgcn_s_sleep(1);
    }
  }
  asm volatile("" ::: "memory");
  BARRIER();
  asm volatile("" ::: "memory");
}

#define STAMP(i) do { } while (0)

constexpr int wd_cnt(int s) { return s < 2 ? 0 : (s < 4 ? 4 : 3); }
constexpr int wd_start(int s) { int n = 0; for (int i = 0; i < s; ++i) n += wd_cnt(i); return n; }
constexpr int g1_wait_n(int c) {
  int n = 0;
  int top = 2 * c - 1;
  if (c <= 3) {
    n += (3 - c) * 4 + 2;
    for (int s = 0; s < top; ++s) { if ((s & 1) && (s + 1) / 2 + 3 <= 7) n += 4; n += wd_cnt(s); }
  } else {
    int s0 = 2 * (c - 3) - 1;
    n += wd_cnt(s0);
    for (int s = s0 + 1; s < top; ++s) { if ((s & 1) && (s + 1) / 2 + 3 <= 7) n += 4; n += wd_cnt(s); }
  }
  return n;
}

template <int S>
DEV void g1_step(LAS char* ring, u32 ldsr, const char* xc, u32 vA, u32 vB, int w, rsrc_t rs, u32 wd_off,
                 const u32x4 (&W)[3][16], u32x4 (&WD)[KS2], f32x4 (&acc)[3][8], u32x4 (&Bc)[8]) {
  if constexpr ((S & 1) && S < 15) {
    constexpr int c = (S + 1) / 2;
    vm_wait<g1_wait_n(c)>();
    BARRIER();
    if constexpr (c + 3 <= 7) dma4(xc + (c + 3) * 2048, vA, vB, ldsr + ((c + 3) % 5) * 16384);
    else asm volatile("" ::: "memory");
  }
  u32x4 Bn[8];
#pragma unroll
  for (int ct = 0; ct < 8; ++ct) {
    if constexpr (S < 15) {
      constexpr int c = (S + 1) / 2, ks = (S + 1) & 1;
      Bn[ct] = tr_frag(ring + (c % 5) * 16384 + ct * 2048 + ks * 1024);
    }
    if (ct < wd_cnt(S)) {
      const int n = wd_start(S) + ct;
      WD[n] = ld16(rs, vA + (n & 3) * 1024, wd_off + (n >> 2) * 4096);
    }
    acc[0][ct] = mfma_bf16(W[0][S], Bc[ct], acc[0][ct]);
    acc[1][ct] = mfma_bf16(W[1][S], Bc[ct], acc[1][ct]);
    __builtin_amdgcn_sched_barrier(0);
  }
  if (w == 3) {
#pragma unroll
    for (int ct = 0; ct < 8; ++ct) acc[2][ct] = mfma_bf16(Bc[ct], Bc[ct], acc[2][ct]);
  } else {
#pragma unroll
    for (int ct = 0; ct < 8; ++ct) acc[2][ct] = mfma_bf16(W[2][S], Bc[ct], acc[2][ct]);
  }
  if constexpr (S < 15) {
#pragma unroll
    for (int ct = 0; ct < 8; ++ct) Bc[ct] = Bn[ct];
    g1_step<S + 1>(ring, ldsr, xc, vA, vB, w, rs, wd_off, W, WD, acc, Bc);
  }
}

template <int C>
DEV void g2_chunk(LAS char* lds, u32 lds0, const char* hsrc, u32 vH, int w, int lane, const u32x4 (&WD)[KS2], f32x4 (&acc)[4]) {
  constexpr int rem = (10 - C) < 3 ? (10 - C) : 3;
  vm_wait<rem * 4>();
  BARRIER();
  if constexpr (C + 4 <= 10) dma4(hsrc + (C + 4) * 4096, vH, vH, lds0 + L_RING + ((C + 4) % 5) * 16384 + w * 4096);
  else asm volatile("" ::: "memory");
#pragma unroll
  for (int ks = 0; ks < 4; ++ks) {
#pragma unroll
    for (int ct = 0; ct < 4; ++ct) {
      u32x4 B = *(LAS u32x4*)(lds + L_RING + (C % 5) * 16384 + ct * 4096 + ks * 1024 + lane * 16);
      acc[ct] = mfma_bf16(WD[C * 4 + ks], B, acc[ct]);
    }
  }
  LGKM0();
  if constexpr (C < 10) g2_chunk<C + 1>(lds, lds0, hsrc, vH, w, lane, WD, acc);
}

__global__ void __launch_bounds__(256, 1) mega_kernel(Params p) {
  __shared__ __attribute__((aligned(16))) char smem[LDS_BYTES];
  LAS char* lds = (LAS char*)smem;
  const u32 lds0 = (u32)(size_t)lds;
  const int w_0 = __builtin_amdgcn_readfirstlane(threadIdx.x >> 6);
  int b, tid, lane, w;
  int cp, cb, rb, db, rt, q, l15, kch, khalf;
#define REFRESH() do { tid = threadIdx.x; b = blockIdx.x; w = w_0; asm volatile("" : "+v"(tid)); asm volatile("" : "+s"(b)); asm volatile("" : "+s"(w)); lane = tid & 63; q = lane >> 4; l15 = lane & 15; \
    const int xcd_ = b & 7, rr_ = b >> 3; cp = xcd_ * 32 + rr_; \
    cb = (xcd_ & 3) * 4 + (rr_ & 3); rb = (xcd_ >> 2) * 8 + (rr_ >> 2); \
    db = (xcd_ & 1) * 4 + (rr_ & 3); rt = (xcd_ >> 1) * 8 + (rr_ >> 2); kch = w >> 1; khalf = w & 1; } while (0)
  REFRESH();
  const rsrc_t rs = __builtin_amdgcn_make_buffer_rsrc(p.ws, 0, WS_BYTES, 0x00020000);
  STAMP(0);

  const u32 epoch = __hip_atomic_load((GAS u32*)(p.ws + OFF_EPOCH), RLX_AGENT) + 1u;

  u32 xg[8];
  {
    const u32x4 r0 = *(const u32x4*)(p.x0 + (size_t)(8 * b + 2 * w) * DM + lane * 8);
    const u32x4 r1 = *(const u32x4*)(p.x0 + (size_t)(8 * b + 2 * w + 1) * DM + lane * 8);
    const int cbR = b >> 4, sR = b & 15;
    u32 wv[11];
#pragma unroll
    for (int i = 0; i < 11; ++i) {
      const int idx = tid + 256 * i, row = idx / 88, rem = idx - row * 88, mat = rem >= 44, P = rem - mat * 44;
      const u16* src = (mat ? p.wu : p.wg) + (size_t)(32 * sR + row) * DF + 86 * cbR + 2 * (P < 43 ? P : 0);
      wv[i] = *(const u32*)src;
    }
    u32x4 dv[2];
    const int s2 = (b < 176 ? b : 0) >> 2, dq = b & 3;
#pragma unroll
    for (int i = 0; i < 2; ++i) {
      const int idx = tid + 256 * i, krow = idx >> 4, c16 = idx & 15, U = 4 * s2 + (krow >> 3), cbU = U / 11, tau = U - cbU * 11;
      const int fl = 8 * tau + (krow & 7);
      dv[i] = *(const u32x4*)(p.wd + (size_t)(86 * cbU + (fl < 86 ? fl : 0)) * DM + 128 * dq + 8 * c16);
    }
    const int dch = 2 * cp + kch, pn = dch * NS + 64 * khalf + lane;
    const u16 pA = p.logA[pn], pI = p.Aim[pn], pCr = p.Cre[pn], pCi = p.Cim[pn], pdt = p.logdt[dch];

    float s0 = 0.f, q0 = 0.f, s1 = 0.f, q1 = 0.f;
#pragma unroll
    for (int i = 0; i < 4; ++i) {
      float a = bf_lo(r0[i]), c = bf_hi(r0[i]);
      s0 += a + c; q0 += a * a + c * c;
      a = bf_lo(r1[i]); c = bf_hi(r1[i]);
      s1 += a + c; q1 += a * a + c * c;
    }
    s0 = wave_sum(s0); q0 = wave_sum(q0); s1 = wave_sum(s1); q1 = wave_sum(q1);
    const float m0 = s0 * (1.f / DM), m1 = s1 * (1.f / DM);
    const float v0 = fmaxf(q0 * (1.f / DM) - m0 * m0, 0.f), v1 = fmaxf(q1 * (1.f / DM) - m1 * m1, 0.f);
    if (lane == 0) {
      f32x4 o = {m0, __builtin_amdgcn_rsqf(v0 + 1e-5f), m1, __builtin_amdgcn_rsqf(v1 + 1e-5f)};
      st16(rs, 0, OFF_ST1 + (8 * b + 2 * w) * 8, __builtin_bit_cast(u32x4, o));
    }
    if (tid < 8) { const u32x4 z = {0u, 0u, 0u, 0u}; st16(rs, tid * 16, OFF_ST3 + b * 128, z); }

#pragma unroll
    for (int i = 0; i < 11; ++i) {
      const int idx = tid + 256 * i, row = idx / 88, rem = idx - row * 88, mat = rem >= 44, P = rem - mat * 44;
      asm volatile("" : "+v"(wv[i]));
      *(LAS u32*)(lds + L_IMG1 + (P >> 2) * 1056 + row * 32 + (2 * (P & 3) + mat) * 4) = P < 43 ? wv[i] : (mat ? 0u : 0x00003F80u);
    }
#pragma unroll
    for (int i = 0; i < 2; ++i) {
      const int idx = tid + 256 * i, krow = idx >> 4, c16 = idx & 15, kg = krow >> 3, j = krow & 7;
      const int ir = (j < 4) ? 4 * kg + j : 12 + 4 * kg + j;
      const int U = 4 * s2 + kg, tau = U - (U / 11) * 11;
      const u32x4 z = {0u, 0u, 0u, 0u};
      asm volatile("" : "+v"(dv[i]));
      *(LAS u32x4*)(lds + L_IMG2 + (c16 >> 1) * 1056 + ir * 32 + (c16 & 1) * 16) = (8 * tau + j < 86) ? dv[i] : z;
    }
    {
      const float dt = rnd_bf16(expf(bf2f(pdt)));
      const float ar = -rnd_bf16(expf(bf2f(pA))) * dt;
      const float ai = bf2f(pI) * dt;
      const float i2p_hi = 0.15915494f, i2p_lo = 5.1981e-9f * 0.0f + (float)(0.15915494309189535 - (double)0.15915494f);
      const float f = ai * i2p_hi;
      const float fhi = __builtin_bit_cast(float, __builtin_bit_cast(u32, f) & 0xffffff00u);
      const float flo = __builtin_fmaf(ai, i2p_hi, -fhi) + ai * i2p_lo;
      f32x4 o = {ar * 1.44269504f, fhi, flo, 2.f * dt * bf2f(pCr)};
      *(LAS f32x4*)(lds + L_PAR + w * 2048 + lane * 32) = o;
      *(LAS float*)(lds + L_PAR + w * 2048 + lane * 32 + 16) = 2.f * dt * bf2f(pCi);
    }
    SYNC_LDS();
#pragma unroll
    for (int i = 0; i < 3; ++i) {
      const int tau = 3 * w + i;
      if (tau < 11) st16(rs, lane * 16, OFF_WGU + ((cbR * 11 + tau) * 16 + sR) * 1024, tr_frag(lds + L_IMG1 + tau * 1056 + lane * 8));
    }
    if (b < 176) {
#pragma unroll
      for (int i = 0; i < 2; ++i) {
        const int dt = 2 * w + i;
        st16(rs, lane * 16, OFF_WD + ((dq * 8 + dt) * KS2 + s2) * 1024, tr_frag(lds + L_IMG2 + dt * 1056 + lane * 8));
      }
    }
  }
  STAMP(1);
  signal_all(p.ws, 0, b, w, lane, epoch);
  STAMP(2);
#pragma unroll
  for (int i = 0; i < 8; ++i) xg[i] = *(const u32*)(p.x0 + (size_t)(8 * tid + i) * DM + 2 * cp);
  const u32 dsk = *(const u32*)(p.Dskip + 2 * cp);

  {
    const float pa0 = (float)(128 * (l15 >> 3) + (l15 & 7));
    const float pa1 = (float)(l15 < 8 ? 256 + 128 * (l15 >> 2) + 2 * (l15 & 3) : 512 + 128 * ((l15 - 8) >> 1) + 4 * (l15 & 1));
    const float pa2 = (float)(1024 + 128 * l15);
    const float pb = (float)(8 * l15);
    f32x4 k0 = {0.f, 0.f, 0.f, 0.f}, k1 = k0, k2 = k0;
#pragma unroll 2
    for (int st = 0; st < 16; ++st) {
      LAS char* pp = lds + L_PAR + w * 2048 + (4 * st + q) * 32;
      const f32x4 c = *(LAS f32x4*)pp;
      const float ci = *(LAS float*)(pp + 16);
      float xr[3], xi[3];
      const float pa[3] = {pa0, pa1, pa2};
#pragma unroll
      for (int i = 0; i < 3; ++i) {
        const float mag = __builtin_amdgcn_exp2f(c.x * pa[i]);
        const float rev = __builtin_fmaf(c.z, pa[i], __builtin_amdgcn_fractf(c.y * pa[i]));
        xr[i] = mag * __builtin_amdgcn_cosf(rev);
        xi[i] = mag * __builtin_amdgcn_sinf(rev);
      }
      const float mag = __builtin_amdgcn_exp2f(c.x * pb);
      const float rev = __builtin_fmaf(c.z, pb, __builtin_amdgcn_fractf(c.y * pb));
      const float cs = mag * __builtin_amdgcn_cosf(rev), sn = mag * __builtin_amdgcn_sinf(rev);
      const float yr = c.w * cs - ci * sn, yi = -(c.w * sn + ci * cs);
      k0 = __builtin_amdgcn_mfma_f32_16x16x4f32(xr[0], yr, k0, 0, 0, 0);
      k1 = __builtin_amdgcn_mfma_f32_16x16x4f32(xr[1], yr, k1, 0, 0, 0);
      k2 = __builtin_amdgcn_mfma_f32_16x16x4f32(xr[2], yr, k2, 0, 0, 0);
      k0 = __builtin_amdgcn_mfma_f32_16x16x4f32(xi[0], yi, k0, 0, 0, 0);
      k1 = __builtin_amdgcn_mfma_f32_16x16x4f32(xi[1], yi, k1, 0, 0, 0);
      k2 = __builtin_amdgcn_mfma_f32_16x16x4f32(xi[2], yi, k2, 0, 0, 0);
    }
    LAS char* kx = lds + L_KX + kch * 3072 + lane * 16;
    if (khalf) {
      *(LAS f32x4*)kx = k0; *(LAS f32x4*)(kx + 1024) = k1; *(LAS f32x4*)(kx + 2048) = k2;
    }
    SYNC_LDS();
    if (!khalf) {
      k0 += *(LAS f32x4*)kx; k1 += *(LAS f32x4*)(kx + 1024); k2 += *(LAS f32x4*)(kx + 2048);
      LAS float* T = (LAS float*)(lds + L_KT + kch * KT_CH);
#pragma unroll
      for (int j = 0; j < 4; ++j) {
        const int r0 = 4 * q + j;
        T[128 * (r0 >> 3) + (r0 & 7) + 8 * l15] = k0[j];
        T[q < 2 ? 256 + 64 * q + j + 4 * l15 : 384 + 32 * (2 * (q - 2) + (j >> 1)) + (j & 1) + 2 * l15] = k1[j];
        T[512 + 16 * r0 + l15] = k2[j];
      }
      if (lane == 0) T[768] = 0.f;
    }
    SYNC_LDS();
    int ci[15];
#pragma unroll
    for (int i = 0; i < 15; ++i) {
      const int lag = 2047 - 8 * tid - i;
      const int bits = (int)(pk_bf16((float)lag, 0.f) & 0xffffu) - (134 << 7) + 128;
      ci[i] = lag < 0 ? 768 : (lag < 128 ? lag : bits);
    }
#pragma unroll
    for (int ch = 0; ch < 2; ++ch) {
      LAS float* T = (LAS float*)(lds + L_KT + ch * KT_CH);
      float V[15];
#pragma unroll
      for (int i = 0; i < 15; ++i) V[i] = T[ci[i]];
      u32 pkd[14];
#pragma unroll
      for (int i = 0; i < 14; ++i) pkd[i] = pk_bf16(V[i], V[i + 1]);
#pragma unroll
      for (int s = 0; s < 8; ++s) {
        const u32x4 o = {pkd[7 - s], pkd[9 - s], pkd[11 - s], pkd[13 - s]};
        *(LAS u32x4*)(lds + L_KC + ch * KC_CH + (s & 3) * KC_COPY + (s >> 2) * KC_HALF + tid * 16) = o;
      }
    }
    if (tid < 16) {
      const u32x4 z = {0u, 0u, 0u, 0u};
      *(LAS u32x4*)(lds + L_KC + (tid >> 3) * KC_CH + (tid & 3) * KC_COPY + ((tid >> 2) & 1) * KC_HALF + 4096) = z;
    }
  }
  STAMP(3);

#pragma unroll 1
  for (int layer = 0; layer < 2; ++layer) {
    REFRESH();
    wait_all(rs, layer * 2, b, w, lane, epoch);
    if (layer == 0 && b == 0 && tid == 0) __hip_atomic_store((GAS u32*)(p.ws + OFF_EPOCH), epoch, RLX_AGENT);
    STAMP(4 + layer * 10);
    float mean[8], rstd[8];
    if (layer == 0) {
#pragma unroll
      for (int i = 0; i < 4; ++i) {
        const f32x4 v = __builtin_bit_cast(f32x4, ld16(rs, tid * 64 + i * 16, OFF_ST1));
        mean[2 * i] = v.x; rstd[2 * i] = v.y; mean[2 * i + 1] = v.z; rstd[2 * i + 1] = v.w;
      }
    } else {
      const u32x4 xa = ld16(rs, tid * 32, OFF_X2 + cp * 8192), xb = ld16(rs, tid * 32 + 16, OFF_X2 + cp * 8192);
      xg[0] = xa.x; xg[1] = xa.y; xg[2] = xa.z; xg[3] = xa.w; xg[4] = xb.x; xg[5] = xb.y; xg[6] = xb.z; xg[7] = xb.w;
#pragma unroll
      for (int i = 0; i < 8; ++i) {
        const u32x4 v = ld16(rs, tid * 128 + i * 16, OFF_ST3);
        const u32x2 lo = {v.x, v.y}, hi = {v.z, v.w};
        const double m = __builtin_bit_cast(double, lo) * (1.0 / DM);
        const double var = __builtin_bit_cast(double, hi) * (1.0 / DM) - m * m;
        mean[i] = (float)m;
        rstd[i] = __builtin_amdgcn_rsqf(fmaxf((float)var, 0.f) + 1e-5f);
      }
    }
    float base0[8], base1[8];
    {
      u32x4 u0, u1;
      const float d0 = bf_lo(dsk), d1 = bf_hi(dsk);
#pragma unroll
      for (int i = 0; i < 4; ++i) {
        const float a0 = bf_lo(xg[2 * i]), a1 = bf_hi(xg[2 * i]), c0 = bf_lo(xg[2 * i + 1]), c1 = bf_hi(xg[2 * i + 1]);
        u0[i] = pk_bf16((a0 - mean[2 * i]) * rstd[2 * i], (c0 - mean[2 * i + 1]) * rstd[2 * i + 1]);
        u1[i] = pk_bf16((a1 - mean[2 * i]) * rstd[2 * i], (c1 - mean[2 * i + 1]) * rstd[2 * i + 1]);
        base0[2 * i] = a0 + bf_lo(u0[i]) * d0; base0[2 * i + 1] = c0 + bf_hi(u0[i]) * d0;
        base1[2 * i] = a1 + bf_lo(u1[i]) * d1; base1[2 * i + 1] = c1 + bf_hi(u1[i]) * d1;
      }
      *(LAS u32x4*)(lds + L_U + 512 + tid * 16) = u0;
      *(LAS u32x4*)(lds + L_U + U_CH + 512 + tid * 16) = u1;
      if (tid < 64) { const u32x4 z = {0u, 0u, 0u, 0u}; *(LAS u32x4*)(lds + L_U + (tid >> 5) * U_CH + (tid & 31) * 16) = z; }
    }
    SYNC_LDS();
    STAMP(5 + layer * 10);
    u32x4 W[3][16];
    {
      f32x4 acc[8];
#pragma unroll
      for (int c = 0; c < 8; ++c) acc[c] = f32x4{0.f, 0.f, 0.f, 0.f};
      LAS char* pa = lds + L_KC + kch * KC_CH + (l15 & 3) * KC_COPY + ((l15 >> 2) & 1) * KC_HALF +
                     2 * (2040 - 16 * (8 * khalf + 1) - 8 * (l15 >> 3) + 8 * q) - 7 * 512;
      LAS char* pb = lds + L_U + kch * U_CH + 2 * (256 + 16 * (l15 - 8 * khalf - 1) + 8 * q);
#pragma unroll
      for (int it = 0; it < 4; ++it) {
        u32x4 A[8], B[8];
#pragma unroll
        for (int m = 0; m < 8; ++m) {
          A[m] = *(LAS u32x4*)(pa - 64 * it + (7 - m) * 512);
          B[m] = *(LAS u32x4*)(pb - 64 * it + m * 512);
        }
#pragma unroll
        for (int i = 0; i < 6; ++i) {
          const int n = it * 6 + i, tl = n % 3, s = n / 3;
          W[tl][s] = ld16(rs, lane * 16 + (s & 3) * 1024, OFF_WGU + (cb * 11 + 3 * w + ((tl == 2 && w == 3) ? 0 : tl)) * 16384 + (s >> 2) * 4096);
        }
#pragma unroll
        for (int c = 0; c < 8; ++c)
#pragma unroll
          for (int cc = 0; cc <= c; ++cc) acc[c] = mfma_bf16(A[c - cc], B[cc], acc[c]);
      }
#pragma unroll
      for (int c = 0; c < 8; ++c)
        *(LAS f32x4*)(lds + L_P + w * 8192 + c * 1024 + (q * 16 + (l15 ^ ((q >> 1) * 8))) * 16) = acc[c];
    }
    SYNC_LDS();
    {
      const int c = tid >> 5, i = (tid >> 1) & 15, qp = tid & 1;
      LAS char* pr = lds + L_P + c * 1024 + (qp * 32 + (i ^ (qp * 8))) * 16;
      u32x4 o[2];
#pragma unroll
      for (int ch = 0; ch < 2; ++ch) {
        const f32x4 a0 = *(LAS f32x4*)(pr + ch * 16384), a1 = *(LAS f32x4*)(pr + ch * 16384 + 256);
        const f32x4 b0 = *(LAS f32x4*)(pr + ch * 16384 + 8192), b1 = *(LAS f32x4*)(pr + ch * 16384 + 8192 + 256);
        const float* bs = ch ? base1 : base0;
        o[ch].x = pk_bf16(a0.x + b0.x + bs[0], a0.y + b0.y + bs[1]);
        o[ch].y = pk_bf16(a0.z + b0.z + bs[2], a0.w + b0.w + bs[3]);
        o[ch].z = pk_bf16(a1.x + b1.x + bs[4], a1.y + b1.y + bs[5]);
        o[ch].w = pk_bf16(a1.z + b1.z + bs[6], a1.w + b1.w + bs[7]);
      }
      const bool up = (lane & 2) != 0;
      u32x4 snd, s1, s2;
#pragma unroll
      for (int k = 0; k < 4; ++k) {
        snd[k] = dpp_u<0x4E>(up ? o[0][k] : o[1][k]);
        s1[k] = up ? snd[k] : o[0][k];
        s2[k] = up ? o[1][k] : snd[k];
      }
      const u32 voff = (tid >> 2) * 32768 + (tid & 3) * 16;
      const u32 soff = OFF_XC + layer * 2097152 + cp * 64;
      st16(rs, voff, soff, s1);
      st16(rs, voff + 16384, soff, s2);
    }
    STAMP(6 + layer * 10);
    signal_all(p.ws, layer * 2 + 1, b, w, lane, epoch);
    STAMP(7 + layer * 10);

    REFRESH();
#pragma unroll
    for (int n = 24; n < 48; ++n) {
      const int tl = n % 3, s = n / 3;
      W[tl][s] = ld16(rs, lane * 16 + (s & 3) * 1024, OFF_WGU + (cb * 11 + 3 * w + ((tl == 2 && w == 3) ? 0 : tl)) * 16384 + (s >> 2) * 4096);
    }
    f32x4 wsum[3];
    {
      const u32x4 ones = {0x3F803F80u, 0x3F803F80u, 0x3F803F80u, 0x3F803F80u};
#pragma unroll
      for (int i = 0; i < 3; ++i) wsum[i] = f32x4{0.f, 0.f, 0.f, 0.f};
#pragma unroll
      for (int s = 0; s < 16; ++s)
#pragma unroll
        for (int i = 0; i < 3; ++i) wsum[i] = mfma_bf16(W[i][s], ones, wsum[i]);
#pragma unroll
      for (int i = 0; i < 3; ++i) asm volatile("" : "+v"(wsum[i]));
    }
    wait_all(rs, layer * 2 + 1, b, w, lane, epoch);
    STAMP(8 + layer * 10);
    u32x4 WD[KS2];
    {
      const char* xc = p.ws + OFF_XC + layer * 2097152 + (size_t)(rb * 8 + 2 * w) * 16384;
      const u32 vA = lane * 16, vB = lane * 16 + 16384 - 2048;
#pragma unroll
      for (int c = 0; c < 4; ++c) dma4(xc + c * 2048, vA, vB, lds0 + L_RING + c * 16384 + w * 4096);
      dma2(p.ws + OFF_XC + layer * 2097152 + (size_t)(rt * 4 + w) * 16384 + db * 2048, vA, lds0 + L_RES + w * 2048);
      f32x4 acc[3][8];
#pragma unroll
      for (int i = 0; i < 3; ++i)
#pragma unroll
        for (int c = 0; c < 8; ++c) acc[i][c] = f32x4{0.f, 0.f, 0.f, 0.f};
      vm_wait<14>();
      BARRIER();
      asm volatile("" ::: "memory");
      u32x4 Bc[8];
#pragma unroll
      for (int ct = 0; ct < 8; ++ct) Bc[ct] = tr_frag(lds + L_RING + ct * 2048 + lane * 8);
      const u32 wd_off = OFF_WD + (db * 4 + w) * KS2 * 1024;
      g1_step<0>(lds + L_RING + lane * 8, lds0 + L_RING + w * 4096, xc, vA, vB, w, rs, wd_off, W, WD, acc, Bc);
      if (w == 3) {
#pragma unroll
        for (int ct = 0; ct < 8; ++ct) {
          if (q == 3) *(LAS float*)(lds + L_ST + (ct * 16 + l15) * 8) = acc[1][ct][0];
          const int j = l15 & 3;
          const float sq = j == 0 ? acc[2][ct][0] : (j == 1 ? acc[2][ct][1] : (j == 2 ? acc[2][ct][2] : acc[2][ct][3]));
          if (q == (l15 >> 2)) *(LAS float*)(lds + L_ST + (ct * 16 + l15) * 8 + 4) = sq;
        }
      }
      SYNC_LDS();
      STAMP(9 + layer * 10);
      REFRESH();
      float mu[8], c1[8], c2[8], vr[8];
#pragma unroll
      for (int ct = 0; ct < 8; ++ct) {
        const f32x2 s = *(LAS f32x2*)(lds + L_ST + (ct * 16 + l15) * 8);
        mu[ct] = s.x * (1.f / DM);
        vr[ct] = fmaxf(s.y * (1.f / DM) - mu[ct] * mu[ct], 0.f) + 1e-5f;
        const float r = __builtin_amdgcn_rsqf(vr[ct]);
        c1[ct] = (-2.f * 0.7978845608f * 1.44269504f) * r;
        c2[ct] = (-2.f * 0.7978845608f * 1.44269504f * 0.044715f) * r * r * r;
      }
      const u32 hsoff = OFF_H + layer * H_BYTES;
#pragma unroll
      for (int i = 0; i < 3; ++i) {
        if (i == 2 && w == 3) break;
        u32 hv[8];
#pragma unroll
        for (int ct = 0; ct < 8; ++ct) {
          float h[2];
#pragma unroll
          for (int e = 0; e < 2; ++e) {
            const float g = __builtin_fmaf(-mu[ct], wsum[i][e], acc[i][ct][e]);
            const float u = __builtin_fmaf(-mu[ct], wsum[i][2 + e], acc[i][ct][2 + e]);
            const float ex = __builtin_amdgcn_exp2f(g * __builtin_fmaf(g * g, c2[ct], c1[ct]));
            h[e] = g * u * __builtin_amdgcn_rcpf(__builtin_fmaf(ex, vr[ct], vr[ct]));
          }
          hv[ct] = pk_bf16(h[0], h[1]);
        }
        const int U = cb * 11 + 3 * w + i;
#pragma unroll
        for (int hf = 0; hf < 2; ++hf) {
          swap32(hv[4 * hf], hv[4 * hf + 2]); swap32(hv[4 * hf + 1], hv[4 * hf + 3]);
          swap16(hv[4 * hf], hv[4 * hf + 1]); swap16(hv[4 * hf + 2], hv[4 * hf + 3]);
          const u32x4 o = {hv[4 * hf], hv[4 * hf + 1], hv[4 * hf + 2], hv[4 * hf + 3]};
          st16(rs, q * (KS2 * 1024) + l15 * 16, hsoff + ((rb * 8 + 4 * hf) * KS2 + (U >> 2)) * 1024 + (U & 3) * 256, o);
        }
      }
    }
    STAMP(10 + layer * 10);
    signal_grp(p.ws, layer, rb, cb, w, lane, epoch);
    wait_grp(p.ws, layer, rt >> 1, w, lane, epoch);
    STAMP(11 + layer * 10);

    REFRESH();
    {
      const char* hsrc = p.ws + OFF_H + layer * H_BYTES + (size_t)(rt * 4 + w) * (KS2 * 1024);
      const u32 vH = lane * 16;
#pragma unroll
      for (int c = 0; c < 4; ++c) dma4(hsrc + c * 4096, vH, vH, lds0 + L_RING + c * 16384 + w * 4096);
      f32x4 acc[4];
#pragma unroll
      for (int ct = 0; ct < 4; ++ct) {
        const u32x2 r = tr_read(lds + L_RES + ct * 2048 + w * 512 + lane * 8);
        acc[ct] = f32x4{bf_lo(r.x), bf_hi(r.x), bf_lo(r.y), bf_hi(r.y)};
      }
      g2_chunk<0>(lds, lds0, hsrc, vH, w, lane, WD, acc);
      STAMP(12 + layer * 10);
      u32 pl[4], ph[4];
#pragma unroll
      for (int ct = 0; ct < 4; ++ct) { pl[ct] = pk_bf16(acc[ct][0], acc[ct][1]); ph[ct] = pk_bf16(acc[ct][2], acc[ct][3]); }
      if (layer == 0) {
        float sm[4], sq[4];
#pragma unroll
        for (int ct = 0; ct < 4; ++ct) {
          const float a = bf_lo(pl[ct]), c = bf_hi(pl[ct]), d = bf_lo(ph[ct]), e = bf_hi(ph[ct]);
          sm[ct] = (a + c) + (d + e);
          sq[ct] = (a * a + c * c) + (d * d + e * e);
        }
        swap32f(sm[0], sm[2]); swap32f(sm[1], sm[3]); swap32f(sq[0], sq[2]); swap32f(sq[1], sq[3]);
        float sA = sm[0] + sm[2], sB = sm[1] + sm[3], qA = sq[0] + sq[2], qB = sq[1] + sq[3];
        swap16f(sA, sB); swap16f(qA, qB);
        const f32x2 o = {sA + sB, qA + qB};
        *(LAS f32x2*)(lds + L_ST + (w * 64 + lane) * 8) = o;
        const bool o1 = (lane & 1) != 0, o2 = (lane & 2) != 0;
#pragma unroll
        for (int z = 0; z < 2; ++z) {
          u32* v = z ? ph : pl;
          u32 t0 = dpp_u<0xB1>(o1 ? v[0] : v[1]), t1 = dpp_u<0xB1>(o1 ? v[2] : v[3]);
          const u32 a0 = o1 ? t0 : v[0], a1 = o1 ? v[1] : t0, a2 = o1 ? t1 : v[2], a3 = o1 ? v[3] : t1;
          t0 = dpp_u<0x4E>(o2 ? a0 : a2); t1 = dpp_u<0x4E>(o2 ? a1 : a3);
          const u32x4 o4 = {o2 ? t0 : a0, o2 ? t1 : a1, o2 ? a2 : t0, o2 ? a3 : t1};
          st16(rs, (2 * q + z) * 8192 + (lane & 3) * 64 + (l15 >> 2) * 16, OFF_X2 + (db * 32 + w * 8) * 8192 + rt * 256, o4);
        }
        SYNC_LDS();
        if (tid < 128) {
          LAS float* s = (LAS float*)(lds + L_ST) + tid;
          const float v = (s[0] + s[128]) + (s[256] + s[384]);
          __hip_atomic_fetch_add((GAS double*)(p.ws + OFF_ST3 + rt * 1024 + tid * 8), (double)v, RLX_AGENT);
        }
        signal_all(p.ws, 2, b, w, lane, epoch);
      } else {
#pragma unroll
        for (int ct = 0; ct < 4; ++ct) {
          const u32x2 o = {pl[ct], ph[ct]};
          *(u32x2*)(p.out + (size_t)(rt * 64 + ct * 16 + l15) * DM + db * 64 + w * 16 + 4 * q) = o;
        }
      }
      STAMP(13 + layer * 10);
    }
  }
}

}

extern "C" void kernel_launch(void* const* d_in, const int* in_sizes, int n_in, void* d_out, int out_size, void* d_ws,
                              size_t ws_size, hipStream_t stream) {
  Params p;
  p.x0 = (const u16*)d_in[0];
  p.logA = (const u16*)d_in[1];
  p.Aim = (const u16*)d_in[2];
  p.Cre = (const u16*)d_in[3];
  p.Cim = (const u16*)d_in[4];
  p.logdt = (const u16*)d_in[5];
  p.Dskip = (const u16*)d_in[6];
  p.wg = (const u16*)d_in[8];
  p.wu = (const u16*)d_in[9];
  p.wd = (const u16*)d_in[10];
  p.out = (u16*)d_out;
  p.ws = (char*)d_ws;
  mega_kernel<<<NBLK, 256, 0, stream>>>(p);
}
```

```cpp
#include <hip/hip_runtime.h>
#include <hip/hip_bf16.h>
#include <utility>

typedef unsigned int u32;
typedef unsigned short u16;
typedef u32 u32x4 __attribute__((ext_vector_type(4)));
typedef u32 u32x2 __attribute__((ext_vector_type(2)));
typedef float f32x4 __attribute__((ext_vector_type(4)));
typedef float f32x2 __attribute__((ext_vector_type(2)));
typedef short s16x4 __attribute__((ext_vector_type(4)));
typedef __bf16 bf16x8 __attribute__((ext_vector_type(8)));
typedef __bf16 bf16x2 __attribute__((ext_vector_type(2)));
typedef _Float16 f16x8 __attribute__((ext_vector_type(8)));
#define LAS __attribute__((address_space(3)))
#define GAS __attribute__((address_space(1)))
#define RLX_AGENT __ATOMIC_RELAXED, __HIP_MEMORY_SCOPE_AGENT
#define DEV __device__ __forceinline__

namespace {

constexpr int SEQ = 2048, DM = 512, NS = 128, DF = 1376;
constexpr int NBLK = 256;
constexpr int KS2 = 44;

constexpr u32 OFF_EPOCH = 0;
constexpr u32 OFF_FLAG = 4096;
constexpr u32 OFF_GFLAG = OFF_FLAG + 8 * 8192;
constexpr u32 OFF_ST1 = OFF_GFLAG + 8192;
constexpr u32 OFF_ST3 = OFF_ST1 + 16384;
constexpr u32 OFF_WGU = OFF_ST3 + 32768;
constexpr u32 OFF_WD = OFF_WGU + 16 * 11 * 16 * 1024;
constexpr u32 OFF_XC = OFF_WD + 32 * KS2 * 1024;
constexpr u32 OFF_XP = OFF_XC + 2 * 2097152;
constexpr u32 OFF_H = OFF_XP + 2 * 2097152;
constexpr u32 H_BYTES = 128 * KS2 * 1024;
constexpr u32 OFF_DBG = OFF_H + 2 * H_BYTES;
constexpr u32 WS_BYTES = OFF_DBG + 256 * 64 * 16;
constexpr int E_P0 = 0, E_CONV = 1  , E_DOWN = 3  ;

constexpr int KC_COPY = 4160, KC_HALF = 4 * KC_COPY + 32, KC_CH = 33344;
constexpr int L_KC = 0;
constexpr int L_RING = 2 * KC_CH;
constexpr int L_RES = L_RING + 5 * 16384;
constexpr int L_ST = L_RES + 8192;
constexpr int LDS_BYTES = 163840;
constexpr int L_U = L_RING;
constexpr int U_CH = 4608;
constexpr int L_P = L_RING + 16384;
constexpr int TB_ROW = 272, TB_HALF = 57 * TB_ROW, TB_WAVE = 2 * TB_HALF;
constexpr int L_IMG1 = 4 * TB_WAVE;
constexpr int L_IMG2 = L_IMG1 + 11 * 1056;
constexpr int L_XT = L_IMG2 + 8 * 1056;
constexpr int L_KX = L_XT + 32 * 272;
constexpr int L_KT = L_IMG1;
constexpr int KT_CH = 772 * 4;
static_assert(L_KX + 6144 <= LDS_BYTES, "lds");

struct Params {
  const u16 *x0, *logA, *Aim, *Cre, *Cim, *logdt, *Dskip, *wg, *wu, *wd;
  u16* out;
  char* ws;
};

DEV float bf_lo(u32 v) { return __builtin_bit_cast(float, v << 16); }
DEV float bf_hi(u32 v) { return __builtin_bit_cast(float, v & 0xffff0000u); }
DEV float bf2f(u16 v) { return __builtin_bit_cast(float, (u32)v << 16); }
DEV u32 pk_bf16(float lo, float hi) {
  f32x2 f = {lo, hi};
  return __builtin_bit_cast(u32, __builtin_convertvector(f, bf16x2));
}
DEV float rnd_bf16(float v) { return bf_lo(pk_bf16(v, 0.f)); }
DEV f32x4 mfma_bf16(u32x4 a, u32x4 b, f32x4 c) {
  return __builtin_amdgcn_mfma_f32_16x16x32_bf16(__builtin_bit_cast(bf16x8, a), __builtin_bit_cast(bf16x8, b), c, 0, 0, 0);
}
DEV f32x4 mfma_f16(u32x4 a, u32x4 b, f32x4 c) {
  return __builtin_amdgcn_mfma_f32_16x16x32_f16(__builtin_bit_cast(f16x8, a), __builtin_bit_cast(f16x8, b), c, 0, 0, 0);
}
DEV u32x2 tr_read(LAS char* p) {
  return __builtin_bit_cast(u32x2, __builtin_amdgcn_ds_read_tr16_b64_v4i16((LAS s16x4*)p));
}
DEV u32x4 tr_frag(LAS char* p) {
  u32x2 a = tr_read(p), b = tr_read(p + 512);
  u32x4 r = {a.x, a.y, b.x, b.y};
  return r;
}
DEV u32x4 lds16(LAS char* p) { return *(LAS u32x4*)p; }
template <int CTRL> DEV float dpp_f(float v) {
  return __builtin_bit_cast(float, __builtin_amdgcn_mov_dpp(__builtin_bit_cast(int, v), CTRL, 0xf, 0xf, true));
}
template <int CTRL> DEV u32 dpp_u(u32 v) { return (u32)__builtin_amdgcn_mov_dpp((int)v, CTRL, 0xf, 0xf, true); }
DEV void swap16(u32& a, u32& b) { auto r = __builtin_amdgcn_permlane16_swap(a, b, false, false); a = r[0]; b = r[1]; }
DEV void swap32(u32& a, u32& b) { auto r = __builtin_amdgcn_permlane32_swap(a, b, false, false); a = r[0]; b = r[1]; }
DEV void swap16f(float& a, float& b) { u32 x = __builtin_bit_cast(u32, a), y = __builtin_bit_cast(u32, b); swap16(x, y); a = __builtin_bit_cast(float, x); b = __builtin_bit_cast(float, y); }
DEV void swap32f(float& a, float& b) { u32 x = __builtin_bit_cast(u32, a), y = __builtin_bit_cast(u32, b); swap32(x, y); a = __builtin_bit_cast(float, x); b = __builtin_bit_cast(float, y); }
DEV float wave_sum(float v) {
  v += dpp_f<0xB1>(v);
  v += dpp_f<0x4E>(v);
  v += dpp_f<0x141>(v);
  v += dpp_f<0x140>(v);
  float a = v, b = v;
  swap16f(a, b); v = a + b;
  a = v; b = v;
  swap32f(a, b); v = a + b;
  return v;
}
DEV float wave_max(float v) {
  v = fmaxf(v, dpp_f<0xB1>(v)); v = fmaxf(v, dpp_f<0x4E>(v)); v = fmaxf(v, dpp_f<0x141>(v)); v = fmaxf(v, dpp_f<0x140>(v));
  float a = v, b = v;
  swap16f(a, b); v = fmaxf(a, b);
  a = v; b = v;
  swap32f(a, b); v = fmaxf(a, b);
  return v;
}
DEV f32x2 cmul(f32x2 a, f32x2 b) {
  const f32x2 t = a.yy * f32x2{-b.y, b.x};
  return __builtin_elementwise_fma(a.xx, b, t);
}
#define ACLOB "a0","a1","a2","a3","a4","a5","a6","a7","a8","a9","a10","a11","a12","a13","a14","a15","a16","a17","a18","a19","a20","a21","a22","a23","a24","a25","a26","a27","a28","a29","a30","a31","a32","a33","a34","a35","a36","a37","a38","a39","a40","a41","a42","a43","a44","a45","a46","a47","a48","a49","a50","a51","a52","a53","a54","a55","a56","a57","a58","a59","a60","a61","a62","a63","a64","a65","a66","a67","a68","a69","a70","a71","a72","a73","a74","a75","a76","a77","a78","a79","a80","a81","a82","a83","a84","a85","a86","a87","a88","a89","a90","a91","a92","a93","a94","a95","a96","a97","a98","a99","a100","a101","a102","a103","a104","a105","a106","a107","a108","a109","a110","a111","a112","a113","a114","a115","a116","a117","a118","a119","a120","a121","a122","a123","a124","a125","a126","a127","a128","a129","a130","a131","a132","a133","a134","a135","a136","a137","a138","a139","a140","a141","a142","a143","a144","a145","a146","a147","a148","a149","a150","a151","a152","a153","a154","a155","a156","a157","a158","a159","a160","a161","a162","a163","a164","a165","a166","a167","a168","a169","a170","a171","a172","a173","a174","a175","a176","a177","a178","a179","a180","a181","a182","a183","a184","a185","a186","a187","a188","a189","a190","a191","a192","a193","a194","a195","a196","a197","a198","a199","a200","a201","a202","a203","a204","a205","a206","a207","a208","a209","a210","a211","a212","a213","a214","a215","a216","a217","a218","a219","a220","a221","a222","a223","a224","a225","a226","a227","a228","a229","a230","a231","a232","a233","a234","a235","a236","a237","a238","a239","a240","a241","a242","a243","a244","a245","a246","a247","a248","a249","a250","a251","a252","a253","a254","a255"
#define BARRIER() __builtin_amdgcn_s_barrier()
#define FENCE() asm volatile("" ::: "memory")
#define PIN() __builtin_amdgcn_sched_barrier(0)
#define LGKM0() asm volatile("s_waitcnt lgkmcnt(0)" ::: "memory", ACLOB)
#define SYNC_LDS() do { LGKM0(); BARRIER(); FENCE(); } while (0)
template <int N> DEV void vm_wait() { asm volatile("s_waitcnt vmcnt(%0)" ::"i"(N) : "memory", ACLOB); }

typedef __amdgpu_buffer_rsrc_t rsrc_t;
DEV rsrc_t mk_rsrc(const void* p, u32 bytes) { return __builtin_amdgcn_make_buffer_rsrc((void*)p, 0, bytes, 0x00020000); }
DEV u32x4 ld16(rsrc_t r, u32 voff, u32 soff) { return __builtin_amdgcn_raw_buffer_load_b128(r, voff, soff, 16); }
DEV u32x4 ld16p(rsrc_t r, u32 voff, u32 soff) { return __builtin_amdgcn_raw_buffer_load_b128(r, voff, soff, 0); }
DEV void st16(rsrc_t r, u32 voff, u32 soff, u32x4 v) { __builtin_amdgcn_raw_buffer_store_b128(v, r, voff, soff, 16); }

template <int... Is, class F> DEV void sfor_impl(std::integer_sequence<int, Is...>, F&& f) { (f(std::integral_constant<int, Is>{}), ...); }
template <int N, class F> DEV void sfor(F&& f) { sfor_impl(std::make_integer_sequence<int, N>{}, f); }
template <bool FIRST> DEV void mm_vv(f32x4& acc, const u32x4& a, const u32x4& b) {
  if constexpr (FIRST) asm volatile("v_mfma_f32_16x16x32_bf16 %0, %1, %2, 0" : "=&v"(acc) : "v"(a), "v"(b));
  else asm volatile("v_mfma_f32_16x16x32_bf16 %0, %1, %2, %0" : "+v"(acc) : "v"(a), "v"(b));
}
template <bool FIRST, int A> DEV void mm_av(f32x4& acc, const u32x4& b) {
  if constexpr (FIRST) asm volatile("v_mfma_f32_16x16x32_bf16 %0, a[%c2:%c3], %1, 0" : "=&v"(acc) : "v"(b), "i"(A), "i"(A + 3));
  else asm volatile("v_mfma_f32_16x16x32_bf16 %0, a[%c2:%c3], %1, %0" : "+v"(acc) : "v"(b), "i"(A), "i"(A + 3));
}
template <int A, int OFF> DEV void ld16_a(rsrc_t r, u32 voff, u32 soff) {
  asm volatile("buffer_load_dwordx4 a[%c3:%c4], %0, %1, %2 offen offset:%c5 sc1" ::"v"(voff), "s"(r), "s"(soff), "i"(A), "i"(A + 3), "i"(OFF) : "memory");
}
#define MM_DONE4(a) asm volatile("s_nop 7" : "+v"((a)[0]), "+v"((a)[1]), "+v"((a)[2]), "+v"((a)[3]))

template <int OFF> DEV void dma1(const char* base, u32 voff, u32 lds) {
  asm volatile("s_mov_b32 m0, %2\n\ts_nop 0\n\tglobal_load_lds_dwordx4 %0, %1 offset:%3 sc1" ::"v"(voff), "s"(base), "s"(lds), "i"(OFF) : "memory");
}
DEV void dma4(const char* base, u32 voff, u32 lds) {
  asm volatile(
      "s_mov_b32 m0, %2\n\ts_nop 0\n\t"
      "global_load_lds_dwordx4 %0, %1 sc1\n\t"
      "global_load_lds_dwordx4 %0, %1 offset:1024 sc1\n\t"
      "global_load_lds_dwordx4 %0, %1 offset:2048 sc1\n\t"
      "global_load_lds_dwordx4 %0, %1 offset:3072 sc1" ::"v"(voff), "s"(base), "s"(lds) : "memory");
}

constexpr u32 SPIN_CAP = 1u << 16;
constexpr int SYNC_WAVE = 3;

struct Ctx {
  char* ws;
  rsrc_t rs;
  LAS char* lds;
  u32 lds0, epoch;
  int b, tid, lane, w, q, l15, cp, cb, rb, db, rt;
};

DEV void flag_all(const Ctx& c, int edge) {
  if (c.w == SYNC_WAVE && c.lane < 8)
    __hip_atomic_store((GAS u32*)(c.ws + OFF_FLAG + edge * 8192 + c.lane * 1024 + c.b * 4), c.epoch, RLX_AGENT);
}
DEV void flag_grp(const Ctx& c, int edge) {
  if (c.w == SYNC_WAVE && c.lane == 0)
    __hip_atomic_store((GAS u32*)(c.ws + OFF_GFLAG + edge * 2048 + c.rb * 128 + c.cb * 4), c.epoch, RLX_AGENT);
}
DEV u32x4 peek_all(const Ctx& c, int edge) { return ld16(c.rs, c.lane * 16, OFF_FLAG + edge * 8192 + (c.b & 7) * 1024); }
DEV u32x4 peek_grp(const Ctx& c, int edge) { return ld16(c.rs, (c.lane & 3) * 16, OFF_GFLAG + edge * 2048 + (c.rt >> 1) * 128); }
DEV bool flags_ok(const Ctx& c, u32x4 f) { return __all(f.x == c.epoch && f.y == c.epoch && f.z == c.epoch && f.w == c.epoch); }
template <bool GRP> DEV void spin(const Ctx& c, int edge) {
#pragma unroll 1
  for (u32 sp = 0; sp < SPIN_CAP; ++sp) {
    if (flags_ok(c, GRP ? peek_grp(c, edge) : peek_all(c, edge))) break;
    __builtin_amdgcn_s_sleep(1);
    FENCE();
  }
}
template <bool GRP> DEV void wait_edge(const Ctx& c, int edge) {
  if (c.w == SYNC_WAVE) spin<GRP>(c, edge);
  FENCE(); BARRIER(); FENCE();
}
template <bool GRP> DEV void check_edge(const Ctx& c, int edge, u32x4 f) {
  if (!flags_ok(c, f)) spin<GRP>(c, edge);
  FENCE();
}

#define STAMP(i) do { } while (0)

DEV void refresh(Ctx& c, int w0) {
  int tid = threadIdx.x, b = blockIdx.x, w = w0;
  asm volatile("" : "+v"(tid)); asm volatile("" : "+s"(b)); asm volatile("" : "+s"(w));
  c.tid = tid; c.b = b; c.w = w; c.lane = tid & 63; c.q = c.lane >> 4; c.l15 = tid & 15;
  const int xcd = b & 7, rr = b >> 3;
  c.cp = xcd * 32 + rr;
  c.cb = (xcd & 3) * 4 + (rr & 3); c.rb = (xcd >> 2) * 8 + (rr >> 2);
  c.db = (xcd & 1) * 4 + (rr & 3); c.rt = (xcd >> 1) * 8 + (rr >> 2);
}

constexpr int AW(int tile, int s) { return (tile * 16 + s) * 4; }
template <int N> DEV void ld_wgu(const Ctx& c) {
  constexpr int tl = N % 3, s = N / 3;
  const u32 v = c.lane * 16 + ((tl == 2 && c.w == 3) ? 0x40000000u : 0u);
  ld16_a<AW(tl, s), (s & 3) * 1024>(c.rs, v, OFF_WGU + (c.cb * 11 + 3 * c.w + tl) * 16384 + (s >> 2) * 4096);
}
constexpr int NWDV = KS2 - 16;
template <int N> DEV void ld_wd(const Ctx& c, u32x4 (&WD)[NWDV]) {
  constexpr int j = N >> 1;
  const int kh = c.w >> 1;
  const u32 base = OFF_WD + (c.db * 4 + 2 * (c.w & 1) + (N & 1)) * (KS2 * 1024);
  if constexpr (N < 16) ld16_a<192 + 4 * N, (j & 3) * 1024>(c.rs, c.lane * 16, base + ((j >> 2) * 8 + 4 * kh) * 1024);
  else if constexpr (j < 20) WD[N - 16] = ld16(c.rs, c.lane * 16 + (j & 3) * 1024, base + ((j >> 2) * 8 + 4 * kh) * 1024);
  else WD[N - 16] = ld16(c.rs, c.lane * 16 + (j & 1) * 1024, base + (40 + 2 * kh) * 1024);
}

struct ConvIn { u32x4 x; u32x4 st[4]; };
DEV void ld16_v(u32x4& d, rsrc_t r, u32 voff, u32 soff) {
  asm volatile("buffer_load_dwordx4 %0, %1, %2, %3 offen sc1" : "=v"(d) : "v"(voff), "s"(r), "s"(soff) : "memory");
}
template <int LAYER> DEV void conv_load(const Ctx& c, int half, ConvIn& in) {
  if (LAYER == 0) {
    ld16_v(in.x, c.rs, c.tid * 16, OFF_XP + c.cp * 8192 + half * 4096);
#pragma unroll
    for (int i = 0; i < 2; ++i) ld16_v(in.st[i], c.rs, c.tid * 16, OFF_ST1 + half * 8192 + i * 4096);
  } else {
    in.x = ld16(c.rs, c.tid * 16, OFF_XP + 2097152 + c.cp * 8192 + half * 4096);
#pragma unroll
    for (int i = 0; i < 4; ++i) in.st[i] = ld16(c.rs, c.tid * 16, OFF_ST3 + half * 16384 + i * 4096);
  }
}
template <int LAYER> DEV void conv_norm(const Ctx& c, int half, const ConvIn& in, u32 dsk, float (&base)[2][4]) {
  float mean[4], rstd[4];
  if (LAYER == 0) {
#pragma unroll
    for (int i = 0; i < 2; ++i) {
      const f32x4 v = __builtin_bit_cast(f32x4, in.st[i]);
      mean[2 * i] = v.x; rstd[2 * i] = v.y; mean[2 * i + 1] = v.z; rstd[2 * i + 1] = v.w;
    }
  } else {
#pragma unroll
    for (int i = 0; i < 4; ++i) {
      const u32x2 lo = {in.st[i].x, in.st[i].y}, hi = {in.st[i].z, in.st[i].w};
      const float m = (float)__builtin_bit_cast(double, lo) * (1.f / DM);
      const float e2 = (float)__builtin_bit_cast(double, hi) * (1.f / DM);
      mean[i] = m;
      rstd[i] = __builtin_amdgcn_rsqf(fmaxf(__builtin_fmaf(-m, m, e2), 0.f) + 1e-5f);
    }
  }
  const float d0 = bf_lo(dsk), d1 = bf_hi(dsk);
  u32x2 u0, u1;
#pragma unroll
  for (int i = 0; i < 2; ++i) {
    const float a0 = bf_lo(in.x[2 * i]), a1 = bf_hi(in.x[2 * i]), c0 = bf_lo(in.x[2 * i + 1]), c1 = bf_hi(in.x[2 * i + 1]);
    u0[i] = pk_bf16((a0 - mean[2 * i]) * rstd[2 * i], (c0 - mean[2 * i + 1]) * rstd[2 * i + 1]);
    u1[i] = pk_bf16((a1 - mean[2 * i]) * rstd[2 * i], (c1 - mean[2 * i + 1]) * rstd[2 * i + 1]);
    base[0][2 * i] = a0 + bf_lo(u0[i]) * d0; base[0][2 * i + 1] = c0 + bf_hi(u0[i]) * d0;
    base[1][2 * i] = a1 + bf_lo(u1[i]) * d1; base[1][2 * i + 1] = c1 + bf_hi(u1[i]) * d1;
  }
  *(LAS u32x2*)(c.lds + L_U + 512 + half * 2048 + c.tid * 8) = u0;
  *(LAS u32x2*)(c.lds + L_U + U_CH + 512 + half * 2048 + c.tid * 8) = u1;
}
constexpr int tri_j(int m) { return m < 4 ? 0 : (m < 7 ? 1 : (m < 9 ? 2 : 3)); }
constexpr int tri_i(int m) { return m < 4 ? m : (m < 7 ? m - 3 : (m < 9 ? m - 5 : 3)); }
template <int B0, bool ZERO, int WN0, int WCNT>
DEV void conv_tri(const Ctx& c, LAS char* pa, LAS char* pb, u32x4 (&Ac)[4], f32x4 (&acc)[4]) {
  u32x4 Bc[4];
#pragma unroll
  for (int j = 0; j < 4; ++j) Bc[j] = lds16(pb + (B0 + j) * 512);
  PIN();
  sfor<4>([&](auto IT) {
    constexpr int it = IT;
    u32x4 An[4], Bn[4];
    sfor<10>([&](auto M) {
      constexpr int m = M, i = tri_i(m), j = tri_j(m);
      if constexpr (it < 3 && m < 8) {
        if constexpr (m & 1) Bn[m >> 1] = lds16(pb - 64 * (it + 1) + (B0 + (m >> 1)) * 512);
        else An[m >> 1] = lds16(pa - 64 * (it + 1) + (7 - (m >> 1)) * 512);
      }
      if constexpr (m >= 8 && it * 2 + m - 8 < WCNT) ld_wgu<WN0 + it * 2 + m - 8>(c);
      mm_vv<ZERO && it == 0 && j == 0>(acc[i], Ac[i - j], Bc[j]);
      PIN();
    });
    if constexpr (it < 3) {
#pragma unroll
      for (int j = 0; j < 4; ++j) { Ac[j] = An[j]; Bc[j] = Bn[j]; }
    }
  });
  MM_DONE4(acc);
}
template <int WN0, int WCNT>
DEV void conv_cross(const Ctx& c, LAS char* pa, LAS char* pb, f32x4 (&acc)[4]) {
  u32x4 Ac[7], Bc[4];
#pragma unroll
  for (int k = 0; k < 4; ++k) { Ac[3 + k] = lds16(pa + (3 - k) * 512); if (k == 0) Bc[0] = lds16(pb); }
#pragma unroll
  for (int k = 1; k < 4; ++k) { Bc[k] = lds16(pb + k * 512); Ac[3 - k] = lds16(pa + (3 + k) * 512); }
  PIN();
  sfor<4>([&](auto IT) {
    constexpr int it = IT;
    u32x4 An[7], Bn[4];
    sfor<16>([&](auto M) {
      constexpr int m = M, j = m >> 2, i = m & 3;
      if constexpr (it < 3 && m < 11) {
        if constexpr (m < 7) An[6 - m] = lds16(pa - 64 * (it + 1) + m * 512);
        else Bn[m - 7] = lds16(pb - 64 * (it + 1) + (m - 7) * 512);
      }
      if constexpr (m >= 11 && (m & 1) && it * 3 + (m - 11) / 2 < WCNT) ld_wgu<WN0 + it * 3 + (m - 11) / 2>(c);
      mm_vv<it == 0 && j == 0>(acc[i], Ac[3 + i - j], Bc[j]);
      PIN();
    });
    if constexpr (it < 3) {
#pragma unroll
      for (int k = 0; k < 7; ++k) Ac[k] = An[k];
#pragma unroll
      for (int k = 0; k < 4; ++k) Bc[k] = Bn[k];
    }
  });
  MM_DONE4(acc);
}
DEV void conv_finish(const Ctx& c, int layer, int half, const f32x4 (&acc)[4], const float (&base)[2][4]) {
#pragma unroll
  for (int i = 0; i < 4; ++i) *(LAS f32x4*)(c.lds + L_P + c.w * 4096 + i * 1024 + (c.q * 16 + (c.l15 ^ (c.q * 4))) * 16) = acc[i];
  SYNC_LDS();
  const int qq = c.tid & 3, ii = (c.tid >> 2) & 15;
  LAS char* pr = c.lds + L_P + c.w * 1024 + (qq * 16 + (ii ^ (qq * 4))) * 16;
  u32x2 o[2];
#pragma unroll
  for (int ch = 0; ch < 2; ++ch) {
    const f32x4 a = *(LAS f32x4*)(pr + ch * 8192), bb = *(LAS f32x4*)(pr + ch * 8192 + 4096);
    o[ch].x = pk_bf16(a.x + bb.x + base[ch][0], a.y + bb.y + base[ch][1]);
    o[ch].y = pk_bf16(a.z + bb.z + base[ch][2], a.w + bb.w + base[ch][3]);
  }
  const bool odd = (c.tid & 1) != 0;
  const u32 r0 = dpp_u<0xB1>(odd ? o[0].x : o[1].x), r1 = dpp_u<0xB1>(odd ? o[0].y : o[1].y);
  const u32x4 v = {odd ? r0 : o[0].x, odd ? r1 : o[0].y, odd ? o[1].x : r0, odd ? o[1].y : r1};
  st16(c.rs, (c.tid >> 2) * 16384 + (c.tid & 1) * 32 + (c.tid & 2) * 8, OFF_XC + layer * 2097152 + half * 1048576 + c.cp * 64, v);
}

template <int LAYER> DEV void conv_phase(Ctx& c, int w0, u32 dsk) {
  refresh(c, w0);
  const int kch = c.w >> 1, khalf = c.w & 1;
  LAS char* pa = c.lds + L_KC + kch * KC_CH + (c.l15 & 3) * KC_COPY + ((c.l15 >> 2) & 1) * KC_HALF +
                 2 * (2040 - 16 * (8 * khalf + 1) - 8 * (c.l15 >> 3) + 8 * c.q) - 7 * 512;
  LAS char* pb = c.lds + L_U + kch * U_CH + 2 * (256 + 16 * (c.l15 - 8 * khalf - 1) + 8 * c.q);
  LGKM0();
  wait_edge<false>(c, LAYER == 0 ? E_P0 : E_DOWN);
  if (LAYER == 0 && c.b == 0 && c.tid == 0) __hip_atomic_store((GAS u32*)(c.ws + OFF_EPOCH), c.epoch, RLX_AGENT);
  STAMP(4 + LAYER * 20);
  ConvIn inA, inB;
  float baseA[2][4], baseB[2][4];
  conv_load<LAYER>(c, 0, inA);
  if (LAYER == 0) conv_load<LAYER>(c, 1, inB);
  PIN();
  if constexpr (LAYER == 0) sfor<16>([&](auto N) { ld_wgu<N>(c); });
  u32x4 Ac[4];
#pragma unroll
  for (int j = 0; j < 4; ++j) Ac[j] = lds16(pa + (7 - j) * 512);
  {
    u32 z = 0u;
    asm volatile("" : "+v"(z));
    *(LAS u32*)(c.lds + L_U + (c.tid >> 7) * U_CH + (c.tid & 127) * 4) = z;
  }
  PIN();
  if constexpr (LAYER == 0)
    asm volatile("s_waitcnt vmcnt(16)" : "+v"(inA.x), "+v"(inA.st[0]), "+v"(inA.st[1]), "+v"(inB.x), "+v"(inB.st[0]), "+v"(inB.st[1])::"memory");
  conv_norm<LAYER>(c, 0, inA, dsk, baseA);
  if (LAYER == 0) conv_norm<LAYER>(c, 1, inB, dsk, baseB);
  SYNC_LDS();
  STAMP(5 + LAYER * 20);
  f32x4 acc[4];
  conv_tri<0, true, 0, 0>(c, pa, pb, Ac, acc);
  conv_finish(c, LAYER, 0, acc, baseA);
  STAMP(6 + LAYER * 20);
  conv_cross<16, LAYER == 0 ? 12 : 0>(c, pa, pb, acc);
#pragma unroll
  for (int j = 0; j < 4; ++j) Ac[j] = lds16(pa + (7 - j) * 512);
  vm_wait<LAYER == 0 ? 12 : 0>();
  BARRIER();
  flag_all(c, E_CONV + 4 * LAYER);
  STAMP(7 + LAYER * 20);
  if (LAYER == 1) {
    wait_edge<false>(c, E_DOWN + 1);
    conv_load<LAYER>(c, 1, inB);
    conv_norm<LAYER>(c, 1, inB, dsk, baseB);
    SYNC_LDS();
  }
  STAMP(8 + LAYER * 20);
  conv_tri<4, false, 28, LAYER == 0 ? 8 : 0>(c, pa, pb, Ac, acc);
  conv_finish(c, LAYER, 1, acc, baseB);
  if constexpr (LAYER == 0) sfor<12>([&](auto N) { ld_wgu<36 + N>(c); });
  vm_wait<LAYER == 0 ? 12 : 0>();
  BARRIER();
  flag_all(c, E_CONV + 4 * LAYER + 1);
  STAMP(9 + LAYER * 20);
}

struct G1 {
  const char* xa;
  const char* res;
  LAS char* ring;
  u32 ldsw;
  u32 v16;
  int layer;
};
template <int HALF, bool STATS, int S>
DEV void g1_step(const Ctx& c, const G1& g, u32x4 (&WD)[NWDV], f32x4 (&acc)[3][4], u32x4 (&Bc)[4], u32x4& fl) {
  if constexpr (HALF == 0) {
    if constexpr (S == 3) { vm_wait<15>(); BARRIER(); FENCE(); }
    if constexpr (S == 7) { vm_wait<23>(); BARRIER(); FENCE(); }
    if constexpr (S == 11) { vm_wait<0>(); check_edge<false>(c, E_CONV + 4 * g.layer + 1, fl); BARRIER(); FENCE(); }
  } else {
    if constexpr (S == 3) { vm_wait<9>(); BARRIER(); FENCE(); if constexpr (STATS) flag_grp(c, 2 * g.layer); }
  }
  u32x4 Bn[4];
  sfor<12>([&](auto M) {
    constexpr int m = M, ct = m / 3, k = m % 3;
    if constexpr (S < 15 && k < 2) {
      constexpr int slot = (HALF * 4 + (S + 1) / 4) % 5;
      const u32x2 r = tr_read(g.ring + slot * 16384 + ct * 4096 + ((S + 1) & 3) * 1024 + k * 512);
      if constexpr (k == 0) { Bn[ct].x = r.x; Bn[ct].y = r.y; } else { Bn[ct].z = r.x; Bn[ct].w = r.y; }
    }
    if constexpr (k == 2) {
      if constexpr (HALF == 0) {
        if constexpr (S >= 1 && S <= 8 && ct < 3) ld_wd<3 * (S - 1) + ct>(c, WD);
        if constexpr (S == 9 && ct == 0) fl = peek_all(c, E_CONV + 4 * g.layer + 1);
        if constexpr (S >= 11 && S <= 13) dma1<ct * 1024>(g.xa + 1048576 + (S - 11) * 4096, g.v16, g.ldsw + ((S - 11 + 4) % 5) * 16384);
        if constexpr (S == 14 && ct == 0) dma1<0>(g.res + 1048576, g.v16, c.lds0 + L_RES + 4096 + c.w * 1024);
      } else {
        if constexpr (S <= 6 && ct < 3 && 24 + 3 * S + ct < KS2) ld_wd<24 + 3 * S + ct>(c, WD);
        if constexpr (S == 13 && ct == 0) fl = peek_grp(c, 2 * g.layer);
      }
    }
    if constexpr (STATS && k == 2) mm_vv<S == 0>(acc[k][ct], Bc[ct], Bc[ct]);
    else mm_av<S == 0, AW(k, S)>(acc[k][ct], Bc[ct]);
    PIN();
  });
  if constexpr (S < 15) {
#pragma unroll
    for (int ct = 0; ct < 4; ++ct) Bc[ct] = Bn[ct];
    g1_step<HALF, STATS, S + 1>(c, g, WD, acc, Bc, fl);
  } else {
    MM_DONE4(acc[2]); asm volatile("" : "+v"(acc[0][3]), "+v"(acc[1][3]), "+v"(acc[0][2]), "+v"(acc[1][2]), "+v"(acc[0][1]), "+v"(acc[1][1]));
  }
}
template <int HALF>
DEV void g1_main(const Ctx& c, const G1& g, u32x4 (&WD)[NWDV], f32x4 (&acc)[3][4], u32x4 (&Bc)[4], u32x4& fl) {
  if (c.w == 3) {
    g1_step<HALF, true, 0>(c, g, WD, acc, Bc, fl);
    const int j = c.l15 & 3;
    const bool hasq = c.q == (c.l15 >> 2);
#pragma unroll
    for (int ct = 0; ct < 4; ++ct) {
      const float s01 = (j & 1) ? acc[2][ct][1] : acc[2][ct][0], s23 = (j & 1) ? acc[2][ct][3] : acc[2][ct][2];
      const float sq = (j & 2) ? s23 : s01;
      LAS char* d = c.lds + L_ST + (ct * 16 + c.l15) * 8;
      *(LAS float*)(c.q == 3 ? d : c.lds + L_ST + 1024 + c.lane * 4) = acc[1][ct][0];
      *(LAS float*)(hasq ? d + 4 : c.lds + L_ST + 1024 + c.lane * 4) = sq;
    }
  } else {
    g1_step<HALF, false, 0>(c, g, WD, acc, Bc, fl);
  }
}
template <int HALF>
DEV void g1_epilogue(const Ctx& c, const G1& g, const f32x4 (&acc)[3][4], const f32x4 (&wsum)[3], const char* hsrc) {
  float mu[4], c1[4], c2[4], vr[4];
#pragma unroll
  for (int ct = 0; ct < 4; ++ct) {
    const f32x2 s = *(LAS f32x2*)(c.lds + L_ST + (ct * 16 + c.l15) * 8);
    mu[ct] = s.x * (-1.f / DM);
    vr[ct] = fmaxf(__builtin_fmaf(-mu[ct], mu[ct], s.y * (1.f / DM)), 0.f) + 1e-5f;
    const float r = __builtin_amdgcn_rsqf(vr[ct]);
    c1[ct] = (-2.f * 0.7978845608f * 1.44269504f) * r;
    c2[ct] = (-2.f * 0.7978845608f * 1.44269504f * 0.044715f) * r * r * r;
  }
#pragma unroll
  for (int i = 0; i < 3; ++i) {
    if constexpr (HALF == 1) {
      if (i == 0) dma4(hsrc, g.v16, g.ldsw);
      dma4(hsrc + (i + 1) * 8192, g.v16, g.ldsw + (i + 1) * 16384);
    }
    if (i == 2 && c.w == 3) break;
    u32 hv[4];
#pragma unroll
    for (int ct = 0; ct < 4; ++ct) {
      const f32x2 m2 = {mu[ct], mu[ct]};
      const f32x2 gg = __builtin_elementwise_fma(m2, wsum[i].xy, acc[i][ct].xy);
      const f32x2 uu = __builtin_elementwise_fma(m2, wsum[i].zw, acc[i][ct].zw);
      const f32x2 pl = __builtin_elementwise_fma(gg * gg, f32x2{c2[ct], c2[ct]}, f32x2{c1[ct], c1[ct]});
      const f32x2 ar = gg * pl;
      const f32x2 ex = {__builtin_amdgcn_exp2f(ar.x), __builtin_amdgcn_exp2f(ar.y)};
      const f32x2 v2 = {vr[ct], vr[ct]};
      const f32x2 dn = __builtin_elementwise_fma(ex, v2, v2);
      const f32x2 h = (gg * uu) * f32x2{__builtin_amdgcn_rcpf(dn.x), __builtin_amdgcn_rcpf(dn.y)};
      hv[ct] = pk_bf16(h.x, h.y);
    }
    swap32(hv[0], hv[2]); swap32(hv[1], hv[3]);
    swap16(hv[0], hv[1]); swap16(hv[2], hv[3]);
    const u32x4 o = {hv[0], hv[1], hv[2], hv[3]};
    const int U = c.cb * 11 + 3 * c.w + i;
    st16(c.rs, c.q * (KS2 * 1024) + c.l15 * 16, OFF_H + g.layer * H_BYTES + ((HALF * 64 + c.rb * 4) * KS2 + (U >> 2)) * 1024 + (U & 3) * 256, o);
    FENCE();
  }
}

template <int HALF, int C>
DEV void g2_chunk(const Ctx& c, int layer, LAS char* rd, const char* hsrc, u32 v16, u32 ldsw, const u32x4 (&WD)[NWDV], f32x4 (&acc)[4], u32x4 (&Bc)[8]) {
  if constexpr (C < 5) {
    constexpr int n = C == 0 ? 8 : (C == 1 ? 8 : (C == 2 ? 6 : (C == 3 ? 2 : 0)));
    vm_wait<(HALF == 0 && C == 1) ? 4 : n>();
    LGKM0();
    BARRIER(); FENCE();
    if constexpr (HALF == 0 && C == 1) flag_grp(c, 2 * layer + 1);
    if constexpr (C == 0) dma4(hsrc + 4 * 8192, v16, ldsw + 4 * 16384);
    if constexpr (C == 1) {
      const char* s5 = hsrc + 5 * 8192 - (c.w >> 1) * 2048;
      dma1<0>(s5, v16, ldsw); dma1<1024>(s5, v16, ldsw);
    }
  }
  constexpr int NK = C < 5 ? 4 : 2, NKN = C < 4 ? 4 : 2;
  u32x4 Bn[8];
  sfor<NK * 4>([&](auto M) {
    constexpr int m = M, ks = m >> 2, ct = (m >> 1) & 1, dt = m & 1, n = (C * 4 + ks) * 2 + dt;
    if constexpr (C < 5 && m < NKN * 2) Bn[m] = lds16(rd + ((C + 1) % 5) * 16384 + (m & 1) * 4096 + (m >> 1) * 1024);
    if constexpr (n < 16) mm_av<C == 0 && ks == 0, 192 + 4 * n>(acc[dt * 2 + ct], Bc[ks * 2 + ct]);
    else mm_vv<false>(acc[dt * 2 + ct], WD[n - 16], Bc[ks * 2 + ct]);
    PIN();
  });
  if constexpr (C < 5) {
#pragma unroll
    for (int m = 0; m < NKN * 2; ++m) Bc[m] = Bn[m];
    g2_chunk<HALF, C + 1>(c, layer, rd, hsrc, v16, ldsw, WD, acc, Bc);
  }
}
template <int HALF>
DEV void g2_unit(const Ctx& c, const Params& p, int layer, const char* hsrc, const u32x4 (&WD)[NWDV]) {
  const u32 v16 = c.lane * 16, ldsw = c.lds0 + L_RING + c.w * 4096;
  const int kh = c.w >> 1, dfin = 2 * (c.w & 1) + kh;
  LAS char* rd = c.lds + L_RING + kh * 8192 + c.lane * 16;
  if constexpr (HALF == 1) {
#pragma unroll
    for (int i = 0; i < 4; ++i) dma4(hsrc + i * 8192, v16, ldsw + i * 16384);
  }
  u32x2 rres[2];
#pragma unroll
  for (int ct = 0; ct < 2; ++ct) rres[ct] = tr_read(c.lds + L_RES + HALF * 4096 + ct * 2048 + dfin * 512 + c.lane * 8);
  f32x4 acc[4];
  vm_wait<12>();
  BARRIER(); FENCE();
  u32x4 Bc[8];
#pragma unroll
  for (int m = 0; m < 8; ++m) Bc[m] = lds16(rd + (m & 1) * 4096 + (m >> 1) * 1024);
  g2_chunk<HALF, 0>(c, layer, rd, hsrc, v16, ldsw, WD, acc, Bc);
  MM_DONE4(acc);
  STAMP(16 + 2 * HALF + layer * 20);
  f32x4 o[2];
#pragma unroll
  for (int ct = 0; ct < 2; ++ct) {
    *(LAS f32x4*)(c.lds + L_RING + 2 * 16384 + c.w * 2048 + ct * 1024 + c.lane * 16) = kh ? acc[ct] : acc[2 + ct];
    o[ct] = (kh ? acc[2 + ct] : acc[ct]) + f32x4{bf_lo(rres[ct].x), bf_hi(rres[ct].x), bf_lo(rres[ct].y), bf_hi(rres[ct].y)};
  }
  SYNC_LDS();
  u32 v[4];
#pragma unroll
  for (int ct = 0; ct < 2; ++ct) {
    o[ct] += *(LAS f32x4*)(c.lds + L_RING + 2 * 16384 + (c.w ^ 2) * 2048 + ct * 1024 + c.lane * 16);
    v[2 * ct] = pk_bf16(o[ct][0], o[ct][1]); v[2 * ct + 1] = pk_bf16(o[ct][2], o[ct][3]);
  }
  if (layer == 0) {
    float st[4];
#pragma unroll
    for (int ct = 0; ct < 2; ++ct) {
      const float a = bf_lo(v[2 * ct]), bb = bf_hi(v[2 * ct]), d = bf_lo(v[2 * ct + 1]), e = bf_hi(v[2 * ct + 1]);
      st[2 * ct] = (a + bb) + (d + e);
      st[2 * ct + 1] = (a * a + bb * bb) + (d * d + e * e);
    }
    swap32f(st[0], st[2]); swap32f(st[1], st[3]);
    float sA = st[0] + st[2], sB = st[1] + st[3];
    swap16f(sA, sB);
    *(LAS float*)(c.lds + L_ST + c.tid * 4) = sA + sB;
    const bool o1 = (c.lane & 1) != 0, o2 = (c.lane & 2) != 0;
    u32 t0 = dpp_u<0xB1>(o1 ? v[0] : v[1]), t1 = dpp_u<0xB1>(o1 ? v[2] : v[3]);
    const u32 a0 = o1 ? t0 : v[0], a1 = o1 ? v[1] : t0, a2 = o1 ? t1 : v[2], a3 = o1 ? v[3] : t1;
    t0 = dpp_u<0x4E>(o2 ? a0 : a2); t1 = dpp_u<0x4E>(o2 ? a1 : a3);
    const u32x4 o4 = {o2 ? t0 : a0, o2 ? t1 : a1, o2 ? a2 : t0, o2 ? a3 : t1};
    st16(c.rs, (2 * c.q + (c.lane & 1)) * 8192 + (c.lane & 2) * 32 + (c.l15 >> 2) * 16,
         OFF_XP + 2097152 + (c.db * 32 + dfin * 8) * 8192 + HALF * 4096 + c.rt * 128, o4);
    SYNC_LDS();
    if (c.w == 0) {
      LAS float* s = (LAS float*)(c.lds + L_ST) + c.lane;
      const float sv = (s[0] + s[64]) + (s[128] + s[192]);
      const int t = c.rt * 32 + (c.q >> 1) * 16 + c.l15;
      __hip_atomic_fetch_add((GAS double*)(c.ws + OFF_ST3 + HALF * 16384 + (t & 3) * 4096 + (t >> 2) * 16 + (c.q & 1) * 8), (double)sv, RLX_AGENT);
    }
    vm_wait<0>();
    BARRIER();
    flag_all(c, E_DOWN + HALF);
  } else {
    swap16(v[0], v[2]); swap16(v[1], v[3]);
    const u32x4 o4 = {v[0], v[1], v[2], v[3]};
    *(u32x4*)(p.out + (size_t)(HALF * 1024 + c.rt * 32 + (c.q & 1) * 16 + c.l15) * DM + c.db * 64 + dfin * 16 + (c.q >> 1) * 8) = o4;
    if constexpr (HALF == 0) BARRIER();
  }
  STAMP(17 + 2 * HALF + layer * 20);
}

__global__ void __launch_bounds__(256, 1) mega_kernel(Params p) {
  __shared__ __attribute__((aligned(16))) char smem[LDS_BYTES];
  const int w0 = __builtin_amdgcn_readfirstlane(threadIdx.x >> 6);
  Ctx c;
  c.ws = p.ws; c.lds = (LAS char*)smem; c.lds0 = (u32)(size_t)c.lds;
  c.rs = mk_rsrc(p.ws, WS_BYTES);
  refresh(c, w0);
  STAMP(0);
  u32 dsk;

  {
    const int b = c.b, tid = c.tid, lane = c.lane, w = c.w, q = c.q, l15 = c.l15;
    LAS char* lds = c.lds;
    const int kch = w >> 1, khalf = w & 1;
    const rsrc_t rx = mk_rsrc(p.x0, SEQ * DM * 2), rg = mk_rsrc(p.wg, DM * DF * 2), ru = mk_rsrc(p.wu, DM * DF * 2), rd = mk_rsrc(p.wd, DM * DF * 2);
    const u32x4 r0 = ld16p(rx, lane * 16, (8 * b + 2 * w) * 1024), r1 = ld16p(rx, lane * 16 + 1024, (8 * b + 2 * w) * 1024);
    u32x4 xt[2];
#pragma unroll
    for (int i = 0; i < 2; ++i) xt[i] = ld16p(rx, (tid >> 3) * 1024 + (tid & 7) * 16 + i * 128, (b >> 2) * 32768 + (b & 3) * 256);
    const int dch = 2 * c.cp + kch, pn = dch * NS + 64 * khalf + lane;
    const u16 pA = p.logA[pn], pI = p.Aim[pn], pCr = p.Cre[pn], pCi = p.Cim[pn], pdt = p.logdt[dch];
    dsk = *(const u32*)(p.Dskip + 2 * c.cp);
    const u32 ep = __hip_atomic_load((GAS u32*)(p.ws + OFF_EPOCH), RLX_AGENT);
    const int cbR = b >> 4, sR = b & 15;
    u32x4 wg[2], wu[2];
    int wrow[2], wtau[2];
#pragma unroll
    for (int i = 0; i < 2; ++i) {
      const int id = tid + 256 * i;
      wrow[i] = (id * 5958) >> 16; wtau[i] = id - wrow[i] * 11;
      const u32 vo = wrow[i] * (DF * 2) + wtau[i] * 16 + (id < 352 ? 0u : 0x40000000u) + sR * (32 * DF * 2) + cbR * 172;
      wg[i] = ld16p(rg, vo, 0); wu[i] = ld16p(ru, vo, 0);
    }
    u32x4 dv[2];
    const int s2 = b >> 2, dq = b & 3;
#pragma unroll
    for (int i = 0; i < 2; ++i) {
      const int krow = (tid >> 4) + 16 * i, U = 4 * s2 + (krow >> 3), cbU = (U * 5958) >> 16, fl = 8 * (U - cbU * 11) + (krow & 7);
      dv[i] = ld16p(rd, (86 * cbU + fl) * 1024 + (tid & 15) * 16 + dq * 256 + (fl < 86 ? 0u : 0x40000000u), 0);
    }
    PIN();
    int ci[15];
#pragma unroll
    for (int i = 0; i < 15; ++i) {
      const int lag = 2047 - 8 * tid - i;
      const int bits = (int)(pk_bf16((float)lag, 0.f) & 0xffffu) - (134 << 7) + 128;
      ci[i] = 4 * (lag < 0 ? 768 : (lag < 128 ? lag : bits));
      asm volatile("" : "+v"(ci[i]));
    }
    PIN();
    c.epoch = ep + 1u;
    {
      float s0 = 0.f, q0 = 0.f, s1 = 0.f, q1 = 0.f;
#pragma unroll
      for (int i = 0; i < 4; ++i) {
        float a = bf_lo(r0[i]), e = bf_hi(r0[i]);
        s0 += a + e; q0 += a * a + e * e;
        a = bf_lo(r1[i]); e = bf_hi(r1[i]);
        s1 += a + e; q1 += a * a + e * e;
      }
      s0 = wave_sum(s0); q0 = wave_sum(q0); s1 = wave_sum(s1); q1 = wave_sum(q1);
      const float m0 = s0 * (1.f / DM), m1 = s1 * (1.f / DM);
      const float v0 = fmaxf(q0 * (1.f / DM) - m0 * m0, 0.f), v1 = fmaxf(q1 * (1.f / DM) - m1 * m1, 0.f);
      const f32x4 o = {m0, __builtin_amdgcn_rsqf(v0 + 1e-5f), m1, __builtin_amdgcn_rsqf(v1 + 1e-5f)};
      const u32x4 z = {0u, 0u, 0u, 0u};
      const u32 vo = lane == 0 ? OFF_ST1 + (b >> 7) * 8192 + (w & 1) * 4096 + ((2 * b + (w >> 1)) & 255) * 16 : OFF_ST3 + b * 128 + w * 32 + (lane - 1) * 16;
      if (lane < 3) st16(c.rs, vo, 0, lane == 0 ? __builtin_bit_cast(u32x4, o) : z);
    }
#pragma unroll
    for (int i = 0; i < 2; ++i) *(LAS u32x4*)(lds + L_XT + (tid >> 3) * 272 + (tid & 7) * 16 + i * 128) = xt[i];
#pragma unroll
    for (int i = 0; i < 2; ++i) {
      const bool last = wtau[i] == 10;
      const u32x4 lo = {wg[i].x, wu[i].x, wg[i].y, wu[i].y}, hi = {wg[i].z, wu[i].z, last ? 0x00003F80u : wg[i].w, last ? 0u : wu[i].w};
      if (i == 0 || tid < 96) {
        *(LAS u32x4*)(lds + L_IMG1 + wtau[i] * 1056 + wrow[i] * 32) = lo;
        *(LAS u32x4*)(lds + L_IMG1 + wtau[i] * 1056 + wrow[i] * 32 + 16) = hi;
      }
    }
#pragma unroll
    for (int i = 0; i < 2; ++i) {
      const int krow = (tid >> 4) + 16 * i, c16 = tid & 15, kg = krow >> 3, j = krow & 7;
      *(LAS u32x4*)(lds + L_IMG2 + (c16 >> 1) * 1056 + (4 * kg + j + (j < 4 ? 0 : 12)) * 32 + (c16 & 1) * 16) = dv[i];
    }
    SYNC_LDS();
#pragma unroll
    for (int i = 0; i < 2; ++i) {
      LAS char* s = lds + L_XT + (tid & 7) * (4 * 272) + ((tid >> 3) + 32 * i) * 4;
      const u32x4 v = {*(LAS u32*)s, *(LAS u32*)(s + 272), *(LAS u32*)(s + 544), *(LAS u32*)(s + 816)};
      st16(c.rs, ((tid >> 3) + 32 * i) * 8192 + (tid & 7) * 16, OFF_XP + (b & 3) * (64 * 8192) + (b >> 2) * 128, v);
    }
#pragma unroll
    for (int i = 0; i < 3; ++i) {
      const int tau = 3 * w + i;
      if (tau < 11) st16(c.rs, lane * 16, OFF_WGU + ((cbR * 11 + tau) * 16 + sR) * 1024, tr_frag(lds + L_IMG1 + tau * 1056 + lane * 8));
    }
    if (b < 176) {
#pragma unroll
      for (int i = 0; i < 2; ++i) st16(c.rs, lane * 16, OFF_WD + ((dq * 8 + 2 * w + i) * KS2 + s2) * 1024, tr_frag(lds + L_IMG2 + (2 * w + i) * 1056 + lane * 8));
    }
    STAMP(1);

    const float dt = rnd_bf16(expf(bf2f(pdt)));
    const float a2 = -rnd_bf16(expf(bf2f(pA))) * dt * 1.44269504f;
    const float ai = bf2f(pI) * dt;
    const float i2p_hi = 0.15915494f, i2p_lo = (float)(0.15915494309189535 - (double)0.15915494f);
    const float fhi = ai * i2p_hi, flo = __builtin_fmaf(ai, i2p_hi, -fhi) + ai * i2p_lo;
    f32x2 wp[3];
#pragma unroll
    for (int i = 0; i < 3; ++i) {
      const float ps = i == 0 ? 1.f : (i == 1 ? 8.f : 128.f);
      const float mag = __builtin_amdgcn_exp2f(a2 * ps);
      const float rev = __builtin_fmaf(flo, ps, __builtin_amdgcn_fractf(fhi * ps));
      wp[i] = f32x2{mag * __builtin_amdgcn_cosf(rev), mag * __builtin_amdgcn_sinf(rev)};
    }
    const float cr = bf2f(pCr), cim = bf2f(pCi);
    int ce = 14 - __builtin_amdgcn_frexp_expf(wave_max(fmaxf(fabsf(cr), fabsf(cim))));
    ce = ce > 100 ? 100 : (ce < -100 ? -100 : ce);
    LAS char* tb = lds + w * TB_WAVE + lane * 4;
    auto put2 = [&](int ra, f32x2 va, int rb2, f32x2 vb) {
      const u32 ha = __builtin_bit_cast(u32, __builtin_amdgcn_cvt_pkrtz(va.x, va.y)), hb = __builtin_bit_cast(u32, __builtin_amdgcn_cvt_pkrtz(vb.x, vb.y));
      u32 la, lb;
      asm("v_fma_mixlo_f16 %0, %2, 1.0, -%4 op_sel_hi:[0,0,1]\n\t"
          "v_fma_mixlo_f16 %1, %5, 1.0, -%7 op_sel_hi:[0,0,1]\n\t"
          "v_fma_mixhi_f16 %0, %3, 1.0, -%4 op_sel:[0,0,1] op_sel_hi:[0,0,1]\n\t"
          "v_fma_mixhi_f16 %1, %6, 1.0, -%7 op_sel:[0,0,1] op_sel_hi:[0,0,1]"
          : "=&v"(la), "=&v"(lb) : "v"(va.x), "v"(va.y), "v"(ha), "v"(vb.x), "v"(vb.y), "v"(hb));
      *(LAS u32*)(tb + ra * TB_ROW) = ha; *(LAS u32*)(tb + TB_HALF + ra * TB_ROW) = la;
      *(LAS u32*)(tb + rb2 * TB_ROW) = hb; *(LAS u32*)(tb + TB_HALF + rb2 * TB_ROW) = lb;
    };
    {
      const f32x2 w8c = {wp[1].x, -wp[1].y}, w16c = cmul(w8c, w8c);
      f32x2 y0 = {ldexpf(cr, ce), -ldexpf(cim, ce)}, y1 = cmul(y0, w8c);
#pragma unroll
      for (int i = 0; i < 8; ++i) {
        put2(41 + 2 * i, y0, 42 + 2 * i, y1);
        if (i < 7) { y0 = cmul(y0, w16c); y1 = cmul(y1, w16c); }
      }
    }
    vm_wait<0>();
    BARRIER();
    flag_all(c, E_P0);
    STAMP(2);
    {
      f32x2 e[8];
      e[1] = wp[0]; e[2] = cmul(e[1], e[1]); e[3] = cmul(e[2], e[1]); e[4] = cmul(e[2], e[2]);
      e[5] = cmul(e[4], e[1]); e[6] = cmul(e[4], e[2]); e[7] = cmul(e[4], e[3]);
      const f32x2 w256 = cmul(wp[2], wp[2]);
      f32x2 ta = {16384.f, 0.f}, tc = wp[2] * 16384.f;
      put2(0, ta, 8, tc);
#pragma unroll
      for (int i = 1; i < 8; ++i) put2(i, e[i] * 16384.f, 8 + i, cmul(tc, e[i]));
      ta = w256 * 16384.f; tc = cmul(tc, w256);
      put2(16, ta, 20, tc);
#pragma unroll
      for (int i = 1; i < 4; ++i) put2(16 + i, cmul(ta, e[2 * i]), 20 + i, cmul(tc, e[2 * i]));
#pragma unroll
      for (int i = 0; i < 2; ++i) {
        ta = cmul(ta, w256); tc = cmul(tc, w256);
        put2(24 + 4 * i, ta, 26 + 4 * i, tc);
        put2(25 + 4 * i, cmul(ta, e[4]), 27 + 4 * i, cmul(tc, e[4]));
      }
#pragma unroll
      for (int i = 0; i < 4; ++i) {
        ta = cmul(ta, w256); tc = cmul(tc, w256);
        put2(32 + 2 * i, ta, 33 + 2 * i, tc);
      }
      ta = cmul(ta, w256);
      put2(40, ta, 40, ta);
    }
    FENCE();
    f32x4 kk[3];
#pragma unroll
    for (int i = 0; i < 3; ++i) kk[i] = f32x4{0.f, 0.f, 0.f, 0.f};
    {
      LAS char* tr = lds + w * TB_WAVE + l15 * TB_ROW + q * 16;
#pragma unroll
      for (int G = 0; G < 4; ++G) {
        const u32x4 bh = lds16(tr + 41 * TB_ROW + G * 64), bl = lds16(tr + TB_HALF + 41 * TB_ROW + G * 64);
#pragma unroll
        for (int i = 0; i < 3; ++i) {
          const u32x4 ah = lds16(tr + i * 16 * TB_ROW + G * 64), al = lds16(tr + TB_HALF + i * 16 * TB_ROW + G * 64);
          kk[i] = mfma_f16(al, bh, kk[i]);
          kk[i] = mfma_f16(ah, bl, kk[i]);
          kk[i] = mfma_f16(ah, bh, kk[i]);
        }
      }
      const float ks = ldexpf(2.f * dt, -14 - ce);
#pragma unroll
      for (int i = 0; i < 3; ++i) kk[i] *= ks;
    }
    STAMP(40);
    LAS char* kx = lds + L_KX + kch * 3072 + lane * 16;
    if (khalf) {
#pragma unroll
      for (int i = 0; i < 3; ++i) *(LAS f32x4*)(kx + i * 1024) = kk[i];
    }
    SYNC_LDS();
    if (!khalf) {
#pragma unroll
      for (int i = 0; i < 3; ++i) kk[i] += *(LAS f32x4*)(kx + i * 1024);
      LAS float* T = (LAS float*)(lds + L_KT + kch * KT_CH);
      const int i0 = 128 * (q >> 1) + 4 * (q & 1) + 8 * l15;
      *(LAS f32x4*)(T + i0) = kk[0];
      if (q < 2) {
        *(LAS f32x4*)(T + 256 + 64 * q + 4 * l15) = kk[1];
      } else {
        *(LAS f32x2*)(T + 384 + 64 * (q - 2) + 2 * l15) = kk[1].xy;
        *(LAS f32x2*)(T + 416 + 64 * (q - 2) + 2 * l15) = kk[1].zw;
      }
#pragma unroll
      for (int j = 0; j < 4; ++j) T[512 + 64 * q + 16 * j + l15] = kk[2][j];
      if (lane == 0) T[768] = 0.f;
    }
    SYNC_LDS();
    STAMP(41);
#pragma unroll
    for (int ch = 0; ch < 2; ++ch) {
      float V[15];
#pragma unroll
      for (int i = 0; i < 15; ++i) V[i] = *(LAS float*)(lds + L_KT + ch * KT_CH + ci[i]);
      u32 pkd[14];
#pragma unroll
      for (int i = 0; i < 14; ++i) pkd[i] = pk_bf16(V[i], V[i + 1]);
#pragma unroll
      for (int s = 0; s < 8; ++s) {
        const u32x4 o = {pkd[7 - s], pkd[9 - s], pkd[11 - s], pkd[13 - s]};
        *(LAS u32x4*)(lds + L_KC + ch * KC_CH + (s & 3) * KC_COPY + (s >> 2) * KC_HALF + tid * 16) = o;
      }
    }
    if (tid < 16) {
      const u32x4 z = {0u, 0u, 0u, 0u};
      *(LAS u32x4*)(lds + L_KC + (tid >> 3) * KC_CH + (tid & 3) * KC_COPY + ((tid >> 2) & 1) * KC_HALF + 4096) = z;
    }
  }
  STAMP(3);

  conv_phase<0>(c, w0, dsk);
  vm_wait<0>();

#pragma unroll 1
  for (int layer = 0; layer < 2; ++layer) {
    refresh(c, w0);
    G1 g;
    g.layer = layer;
    g.xa = c.ws + OFF_XC + layer * 2097152 + (size_t)(c.rb * 4 + c.w) * 16384;
    g.res = c.ws + OFF_XC + layer * 2097152 + (size_t)(c.rt * 2 + (c.w >> 1)) * 16384 + c.db * 2048 + (c.w & 1) * 1024;
    g.ring = c.lds + L_RING + c.lane * 8;
    g.ldsw = c.lds0 + L_RING + c.w * 4096;
    g.v16 = c.lane * 16;
    wait_edge<false>(c, E_CONV + 4 * layer);
    STAMP(10 + layer * 20);
#pragma unroll
    for (int i = 0; i < 4; ++i) dma4(g.xa + i * 4096, g.v16, g.ldsw + i * 16384);
    dma1<0>(g.res, g.v16, c.lds0 + L_RES + c.w * 1024);
    PIN();
    f32x4 wsum[3];
    {
      u32x4 ones = {0x3F803F80u, 0x3F803F80u, 0x3F803F80u, 0x3F803F80u};
      asm volatile("s_nop 1" : "+v"(ones));
      sfor<48>([&](auto N) { mm_av<(N < 3), AW(N % 3, N / 3)>(wsum[N % 3], ones); });
      asm volatile("s_nop 7" : "+v"(wsum[0]), "+v"(wsum[1]), "+v"(wsum[2]));
    }
    PIN();
    u32x4 WD[NWDV], Bc[4], fl;
    f32x4 acc[3][4];
    vm_wait<13>();
    BARRIER(); FENCE();
#pragma unroll
    for (int ct = 0; ct < 4; ++ct) Bc[ct] = tr_frag(g.ring + ct * 4096);
    STAMP(11 + layer * 20);
    g1_main<0>(c, g, WD, acc, Bc, fl);
    vm_wait<9>();
    SYNC_LDS();
    STAMP(12 + layer * 20);
    dma4(g.xa + 1048576 + 3 * 4096, g.v16, g.ldsw + 2 * 16384);
    g1_epilogue<0>(c, g, acc, wsum, nullptr);
#pragma unroll
    for (int ct = 0; ct < 4; ++ct) Bc[ct] = tr_frag(g.ring + 4 * 16384 + ct * 4096);
    STAMP(13 + layer * 20);
    g1_main<1>(c, g, WD, acc, Bc, fl);
    check_edge<true>(c, 2 * layer, fl);
    SYNC_LDS();
    STAMP(14 + layer * 20);
    refresh(c, w0);
    const char* hsrc = c.ws + OFF_H + layer * H_BYTES + (size_t)(c.rt * 2 + (c.w & 1)) * (KS2 * 1024) + (c.w >> 1) * 4096;
    g1_epilogue<1>(c, g, acc, wsum, hsrc);
    STAMP(15 + layer * 20);
    g2_unit<0>(c, p, layer, hsrc, WD);
    wait_edge<true>(c, 2 * layer + 1);
    g2_unit<1>(c, p, layer, hsrc + (size_t)64 * KS2 * 1024, WD);
    if (layer == 0) conv_phase<1>(c, w0, dsk);
  }
}

}

extern "C" void kernel_launch(void* const* d_in, const int* in_sizes, int n_in, void* d_out, int out_size, void* d_ws,
                              size_t ws_size, hipStream_t stream) {
  Params p;
  p.x0 = (const u16*)d_in[0];
  p.logA = (const u16*)d_in[1];
  p.Aim = (const u16*)d_in[2];
  p.Cre = (const u16*)d_in[3];
  p.Cim = (const u16*)d_in[4];
  p.logdt = (const u16*)d_in[5];
  p.Dskip = (const u16*)d_in[6];
  p.wg = (const u16*)d_in[8];
  p.wu = (const u16*)d_in[9];
  p.wd = (const u16*)d_in[10];
  p.out = (u16*)d_out;
  p.ws = (char*)d_ws;
  mega_kernel<<<NBLK, 256, 0, stream>>>(p);
}
```

```cpp
#define PROBE 1
#include <hip/hip_runtime.h>
#include <hip/hip_bf16.h>
#include <utility>

typedef unsigned int u32;
typedef unsigned short u16;
typedef u32 u32x4 __attribute__((ext_vector_type(4)));
typedef u32 u32x2 __attribute__((ext_vector_type(2)));
typedef float f32x4 __attribute__((ext_vector_type(4)));
typedef float f32x2 __attribute__((ext_vector_type(2)));
typedef short s16x4 __attribute__((ext_vector_type(4)));
typedef __bf16 bf16x8 __attribute__((ext_vector_type(8)));
typedef __bf16 bf16x2 __attribute__((ext_vector_type(2)));
typedef _Float16 f16x8 __attribute__((ext_vector_type(8)));
#define LAS __attribute__((address_space(3)))
#define GAS __attribute__((address_space(1)))
#define RLX_AGENT __ATOMIC_RELAXED, __HIP_MEMORY_SCOPE_AGENT
#define DEV __device__ __forceinline__

namespace {

constexpr int SEQ = 2048, DM = 512, NS = 128, DF = 1376;
constexpr int NBLK = 256;
constexpr int KS2 = 44;

constexpr u32 OFF_EPOCH = 0;
constexpr u32 OFF_FLAG = 4096;
constexpr u32 OFF_GFLAG = OFF_FLAG + 8 * 8192;
constexpr u32 OFF_ST1 = OFF_GFLAG + 8192;
constexpr u32 OFF_ST3 = OFF_ST1 + 16384;
constexpr u32 OFF_WGU = OFF_ST3 + 32768;
constexpr u32 OFF_WD = OFF_WGU + 16 * 11 * 16 * 1024;
constexpr u32 OFF_XC = OFF_WD + 32 * KS2 * 1024;
constexpr u32 OFF_XP = OFF_XC + 2 * 2097152;
constexpr u32 OFF_H = OFF_XP + 2 * 2097152;
constexpr u32 H_BYTES = 128 * KS2 * 1024;
constexpr u32 OFF_DBG = OFF_H + 2 * H_BYTES;
constexpr u32 WS_BYTES = OFF_DBG + 256 * 64 * 16;
constexpr int E_P0 = 0, E_CONV = 1  , E_DOWN = 3  ;

constexpr int KC_COPY = 4160, KC_HALF = 4 * KC_COPY + 32, KC_CH = 33344;
constexpr int L_KC = 0;
constexpr int L_RING = 2 * KC_CH;
constexpr int L_RES = L_RING + 5 * 16384;
constexpr int L_ST = L_RES + 8192;
constexpr int LDS_BYTES = 163840;
constexpr int L_U = L_RING;
constexpr int U_CH = 4608;
constexpr int L_P = L_RING + 16384;
constexpr int TB_ROW = 272, TB_HALF = 57 * TB_ROW, TB_WAVE = 2 * TB_HALF;
constexpr int L_IMG1 = 4 * TB_WAVE;
constexpr int L_IMG2 = L_IMG1 + 11 * 1056;
constexpr int L_XT = L_IMG2 + 8 * 1056;
constexpr int L_KX = L_XT + 32 * 272;
constexpr int L_KT = L_IMG1;
constexpr int KT_CH = 772 * 4;
static_assert(L_KX + 6144 <= LDS_BYTES, "lds");

struct Params {
  const u16 *x0, *logA, *Aim, *Cre, *Cim, *logdt, *Dskip, *wg, *wu, *wd;
  u16* out;
  char* ws;
};

DEV float bf_lo(u32 v) { return __builtin_bit_cast(float, v << 16); }
DEV float bf_hi(u32 v) { return __builtin_bit_cast(float, v & 0xffff0000u); }
DEV float bf2f(u16 v) { return __builtin_bit_cast(float, (u32)v << 16); }
DEV u32 pk_bf16(float lo, float hi) {
  f32x2 f = {lo, hi};
  return __builtin_bit_cast(u32, __builtin_convertvector(f, bf16x2));
}
DEV float rnd_bf16(float v) { return bf_lo(pk_bf16(v, 0.f)); }
DEV f32x4 mfma_bf16(u32x4 a, u32x4 b, f32x4 c) {
  return __builtin_amdgcn_mfma_f32_16x16x32_bf16(__builtin_bit_cast(bf16x8, a), __builtin_bit_cast(bf16x8, b), c, 0, 0, 0);
}
DEV f32x4 mfma_f16(u32x4 a, u32x4 b, f32x4 c) {
  return __builtin_amdgcn_mfma_f32_16x16x32_f16(__builtin_bit_cast(f16x8, a), __builtin_bit_cast(f16x8, b), c, 0, 0, 0);
}
DEV u32x2 tr_read(LAS char* p) {
  return __builtin_bit_cast(u32x2, __builtin_amdgcn_ds_read_tr16_b64_v4i16((LAS s16x4*)p));
}
DEV u32x4 tr_frag(LAS char* p) {
  u32x2 a = tr_read(p), b = tr_read(p + 512);
  u32x4 r = {a.x, a.y, b.x, b.y};
  return r;
}
DEV u32x4 lds16(LAS char* p) { return *(LAS u32x4*)p; }
template <int CTRL> DEV float dpp_f(float v) {
  return __builtin_bit_cast(float, __builtin_amdgcn_mov_dpp(__builtin_bit_cast(int, v), CTRL, 0xf, 0xf, true));
}
template <int CTRL> DEV u32 dpp_u(u32 v) { return (u32)__builtin_amdgcn_mov_dpp((int)v, CTRL, 0xf, 0xf, true); }
DEV void swap16(u32& a, u32& b) { auto r = __builtin_amdgcn_permlane16_swap(a, b, false, false); a = r[0]; b = r[1]; }
DEV void swap32(u32& a, u32& b) { auto r = __builtin_amdgcn_permlane32_swap(a, b, false, false); a = r[0]; b = r[1]; }
DEV void swap16f(float& a, float& b) { u32 x = __builtin_bit_cast(u32, a), y = __builtin_bit_cast(u32, b); swap16(x, y); a = __builtin_bit_cast(float, x); b = __builtin_bit_cast(float, y); }
DEV void swap32f(float& a, float& b) { u32 x = __builtin_bit_cast(u32, a), y = __builtin_bit_cast(u32, b); swap32(x, y); a = __builtin_bit_cast(float, x); b = __builtin_bit_cast(float, y); }
DEV float wave_sum(float v) {
  v += dpp_f<0xB1>(v);
  v += dpp_f<0x4E>(v);
  v += dpp_f<0x141>(v);
  v += dpp_f<0x140>(v);
  float a = v, b = v;
  swap16f(a, b); v = a + b;
  a = v; b = v;
  swap32f(a, b); v = a + b;
  return v;
}
DEV float wave_max(float v) {
  v = fmaxf(v, dpp_f<0xB1>(v)); v = fmaxf(v, dpp_f<0x4E>(v)); v = fmaxf(v, dpp_f<0x141>(v)); v = fmaxf(v, dpp_f<0x140>(v));
  float a = v, b = v;
  swap16f(a, b); v = fmaxf(a, b);
  a = v; b = v;
  swap32f(a, b); v = fmaxf(a, b);
  return v;
}
DEV f32x2 cmul(f32x2 a, f32x2 b) {
  const f32x2 t = a.yy * f32x2{-b.y, b.x};
  return __builtin_elementwise_fma(a.xx, b, t);
}
#define ACLOB "a0","a1","a2","a3","a4","a5","a6","a7","a8","a9","a10","a11","a12","a13","a14","a15","a16","a17","a18","a19","a20","a21","a22","a23","a24","a25","a26","a27","a28","a29","a30","a31","a32","a33","a34","a35","a36","a37","a38","a39","a40","a41","a42","a43","a44","a45","a46","a47","a48","a49","a50","a51","a52","a53","a54","a55","a56","a57","a58","a59","a60","a61","a62","a63","a64","a65","a66","a67","a68","a69","a70","a71","a72","a73","a74","a75","a76","a77","a78","a79","a80","a81","a82","a83","a84","a85","a86","a87","a88","a89","a90","a91","a92","a93","a94","a95","a96","a97","a98","a99","a100","a101","a102","a103","a104","a105","a106","a107","a108","a109","a110","a111","a112","a113","a114","a115","a116","a117","a118","a119","a120","a121","a122","a123","a124","a125","a126","a127","a128","a129","a130","a131","a132","a133","a134","a135","a136","a137","a138","a139","a140","a141","a142","a143","a144","a145","a146","a147","a148","a149","a150","a151","a152","a153","a154","a155","a156","a157","a158","a159","a160","a161","a162","a163","a164","a165","a166","a167","a168","a169","a170","a171","a172","a173","a174","a175","a176","a177","a178","a179","a180","a181","a182","a183","a184","a185","a186","a187","a188","a189","a190","a191","a192","a193","a194","a195","a196","a197","a198","a199","a200","a201","a202","a203","a204","a205","a206","a207","a208","a209","a210","a211","a212","a213","a214","a215","a216","a217","a218","a219","a220","a221","a222","a223","a224","a225","a226","a227","a228","a229","a230","a231","a232","a233","a234","a235","a236","a237","a238","a239","a240","a241","a242","a243","a244","a245","a246","a247","a248","a249","a250","a251","a252","a253","a254","a255"
#define BARRIER() __builtin_amdgcn_s_barrier()
#define FENCE() asm volatile("" ::: "memory")
#define PIN() __builtin_amdgcn_sched_barrier(0)
#define LGKM0() asm volatile("s_waitcnt lgkmcnt(0)" ::: "memory", ACLOB)
#define SYNC_LDS() do { LGKM0(); BARRIER(); FENCE(); } while (0)
template <int N> DEV void vm_wait() { asm volatile("s_waitcnt vmcnt(%0)" ::"i"(N) : "memory", ACLOB); }

typedef __amdgpu_buffer_rsrc_t rsrc_t;
DEV rsrc_t mk_rsrc(const void* p, u32 bytes) { return __builtin_amdgcn_make_buffer_rsrc((void*)p, 0, bytes, 0x00020000); }
DEV u32x4 ld16(rsrc_t r, u32 voff, u32 soff) { return __builtin_amdgcn_raw_buffer_load_b128(r, voff, soff, 16); }
DEV u32x4 ld16p(rsrc_t r, u32 voff, u32 soff) { return __builtin_amdgcn_raw_buffer_load_b128(r, voff, soff, 0); }
DEV void st16(rsrc_t r, u32 voff, u32 soff, u32x4 v) { __builtin_amdgcn_raw_buffer_store_b128(v, r, voff, soff, 16); }

template <int... Is, class F> DEV void sfor_impl(std::integer_sequence<int, Is...>, F&& f) { (f(std::integral_constant<int, Is>{}), ...); }
template <int N, class F> DEV void sfor(F&& f) { sfor_impl(std::make_integer_sequence<int, N>{}, f); }
template <bool FIRST> DEV void mm_vv(f32x4& acc, const u32x4& a, const u32x4& b) {
  if constexpr (FIRST) asm volatile("v_mfma_f32_16x16x32_bf16 %0, %1, %2, 0" : "=&v"(acc) : "v"(a), "v"(b));
  else asm volatile("v_mfma_f32_16x16x32_bf16 %0, %1, %2, %0" : "+v"(acc) : "v"(a), "v"(b));
}
template <bool FIRST, int A> DEV void mm_av(f32x4& acc, const u32x4& b) {
  if constexpr (FIRST) asm volatile("v_mfma_f32_16x16x32_bf16 %0, a[%c2:%c3], %1, 0" : "=&v"(acc) : "v"(b), "i"(A), "i"(A + 3));
  else asm volatile("v_mfma_f32_16x16x32_bf16 %0, a[%c2:%c3], %1, %0" : "+v"(acc) : "v"(b), "i"(A), "i"(A + 3));
}
template <int A, int OFF> DEV void ld16_a(rsrc_t r, u32 voff, u32 soff) {
  asm volatile("buffer_load_dwordx4 a[%c3:%c4], %0, %1, %2 offen offset:%c5 sc1" ::"v"(voff), "s"(r), "s"(soff), "i"(A), "i"(A + 3), "i"(OFF) : "memory");
}
#define MM_DONE4(a) asm volatile("s_nop 7" : "+v"((a)[0]), "+v"((a)[1]), "+v"((a)[2]), "+v"((a)[3]))

template <int OFF> DEV void dma1(const char* base, u32 voff, u32 lds) {
  asm volatile("s_mov_b32 m0, %2\n\ts_nop 0\n\tglobal_load_lds_dwordx4 %0, %1 offset:%3 sc1" ::"v"(voff), "s"(base), "s"(lds), "i"(OFF) : "memory");
}
DEV void dma4(const char* base, u32 voff, u32 lds) {
  asm volatile(
      "s_mov_b32 m0, %2\n\ts_nop 0\n\t"
      "global_load_lds_dwordx4 %0, %1 sc1\n\t"
      "global_load_lds_dwordx4 %0, %1 offset:1024 sc1\n\t"
      "global_load_lds_dwordx4 %0, %1 offset:2048 sc1\n\t"
      "global_load_lds_dwordx4 %0, %1 offset:3072 sc1" ::"v"(voff), "s"(base), "s"(lds) : "memory");
}

constexpr u32 SPIN_CAP = 1u << 16;
constexpr int SYNC_WAVE = 3;

struct Ctx {
  char* ws;
  rsrc_t rs;
  LAS char* lds;
  u32 lds0, epoch;
  int b, tid, lane, w, q, l15, cp, cb, rb, db, rt;
  mutable unsigned long long t0, tj;
};

DEV void flag_all(const Ctx& c, int edge) {
  if (c.w == SYNC_WAVE && c.lane < 8)
    __hip_atomic_store((GAS u32*)(c.ws + OFF_FLAG + edge * 8192 + c.lane * 1024 + c.b * 4), c.epoch, RLX_AGENT);
}
DEV void flag_grp(const Ctx& c, int edge) {
  if (c.w == SYNC_WAVE && c.lane == 0)
    __hip_atomic_store((GAS u32*)(c.ws + OFF_GFLAG + edge * 2048 + c.rb * 128 + c.cb * 4), c.epoch, RLX_AGENT);
}
DEV u32x4 peek_all(const Ctx& c, int edge) { return ld16(c.rs, c.lane * 16, OFF_FLAG + edge * 8192 + (c.b & 7) * 1024); }
DEV u32x4 peek_grp(const Ctx& c, int edge) { return ld16(c.rs, (c.lane & 3) * 16, OFF_GFLAG + edge * 2048 + (c.rt >> 1) * 128); }
DEV bool flags_ok(const Ctx& c, u32x4 f) { return __all(f.x == c.epoch && f.y == c.epoch && f.z == c.epoch && f.w == c.epoch); }
template <bool GRP> DEV void spin(const Ctx& c, int edge) {
#pragma unroll 1
  for (u32 sp = 0; sp < SPIN_CAP; ++sp) {
    if (flags_ok(c, GRP ? peek_grp(c, edge) : peek_all(c, edge))) break;
    __builtin_amdgcn_s_sleep(1);
    FENCE();
  }
}
template <bool GRP> DEV void wait_edge(const Ctx& c, int edge) {
  if (c.w == SYNC_WAVE) spin<GRP>(c, edge);
  FENCE(); BARRIER(); FENCE();
}
template <bool GRP> DEV void check_edge(const Ctx& c, int edge, u32x4 f) {
  if (!flags_ok(c, f)) spin<GRP>(c, edge);
  FENCE();
}

#define STAMP(i) do { if ((i) == 0) c.t0 = wall_clock64(); if ((i) == PROBE) c.tj = wall_clock64(); } while (0)

DEV void refresh(Ctx& c, int w0) {
  int tid = threadIdx.x, b = blockIdx.x, w = w0;
  asm volatile("" : "+v"(tid)); asm volatile("" : "+s"(b)); asm volatile("" : "+s"(w));
  c.tid = tid; c.b = b; c.w = w; c.lane = tid & 63; c.q = c.lane >> 4; c.l15 = tid & 15;
  const int xcd = b & 7, rr = b >> 3;
  c.cp = xcd * 32 + rr;
  c.cb = (xcd & 3) * 4 + (rr & 3); c.rb = (xcd >> 2) * 8 + (rr >> 2);
  c.db = (xcd & 1) * 4 + (rr & 3); c.rt = (xcd >> 1) * 8 + (rr >> 2);
}

constexpr int AW(int tile, int s) { return (tile * 16 + s) * 4; }
template <int N> DEV void ld_wgu(const Ctx& c) {
  constexpr int tl = N % 3, s = N / 3;
  const u32 v = c.lane * 16 + ((tl == 2 && c.w == 3) ? 0x40000000u : 0u);
  ld16_a<AW(tl, s), (s & 3) * 1024>(c.rs, v, OFF_WGU + (c.cb * 11 + 3 * c.w + tl) * 16384 + (s >> 2) * 4096);
}
constexpr int NWDV = KS2 - 16;
template <int N> DEV void ld_wd(const Ctx& c, u32x4 (&WD)[NWDV]) {
  constexpr int j = N >> 1;
  const int kh = c.w >> 1;
  const u32 base = OFF_WD + (c.db * 4 + 2 * (c.w & 1) + (N & 1)) * (KS2 * 1024);
  if constexpr (N < 16) ld16_a<192 + 4 * N, (j & 3) * 1024>(c.rs, c.lane * 16, base + ((j >> 2) * 8 + 4 * kh) * 1024);
  else if constexpr (j < 20) WD[N - 16] = ld16(c.rs, c.lane * 16 + (j & 3) * 1024, base + ((j >> 2) * 8 + 4 * kh) * 1024);
  else WD[N - 16] = ld16(c.rs, c.lane * 16 + (j & 1) * 1024, base + (40 + 2 * kh) * 1024);
}

struct ConvIn { u32x4 x; u32x4 st[4]; };
DEV void ld16_v(u32x4& d, rsrc_t r, u32 voff, u32 soff) {
  asm volatile("buffer_load_dwordx4 %0, %1, %2, %3 offen sc1" : "=v"(d) : "v"(voff), "s"(r), "s"(soff) : "memory");
}
template <int LAYER> DEV void conv_load(const Ctx& c, int half, ConvIn& in) {
  if (LAYER == 0) {
    ld16_v(in.x, c.rs, c.tid * 16, OFF_XP + c.cp * 8192 + half * 4096);
#pragma unroll
    for (int i = 0; i < 2; ++i) ld16_v(in.st[i], c.rs, c.tid * 16, OFF_ST1 + half * 8192 + i * 4096);
  } else {
    in.x = ld16(c.rs, c.tid * 16, OFF_XP + 2097152 + c.cp * 8192 + half * 4096);
#pragma unroll
    for (int i = 0; i < 4; ++i) in.st[i] = ld16(c.rs, c.tid * 16, OFF_ST3 + half * 16384 + i * 4096);
  }
}
template <int LAYER> DEV void conv_norm(const Ctx& c, int half, const ConvIn& in, u32 dsk, float (&base)[2][4]) {
  float mean[4], rstd[4];
  if (LAYER == 0) {
#pragma unroll
    for (int i = 0; i < 2; ++i) {
      const f32x4 v = __builtin_bit_cast(f32x4, in.st[i]);
      mean[2 * i] = v.x; rstd[2 * i] = v.y; mean[2 * i + 1] = v.z; rstd[2 * i + 1] = v.w;
    }
  } else {
#pragma unroll
    for (int i = 0; i < 4; ++i) {
      const u32x2 lo = {in.st[i].x, in.st[i].y}, hi = {in.st[i].z, in.st[i].w};
      const float m = (float)__builtin_bit_cast(double, lo) * (1.f / DM);
      const float e2 = (float)__builtin_bit_cast(double, hi) * (1.f / DM);
      mean[i] = m;
      rstd[i] = __builtin_amdgcn_rsqf(fmaxf(__builtin_fmaf(-m, m, e2), 0.f) + 1e-5f);
    }
  }
  const float d0 = bf_lo(dsk), d1 = bf_hi(dsk);
  u32x2 u0, u1;
#pragma unroll
  for (int i = 0; i < 2; ++i) {
    const float a0 = bf_lo(in.x[2 * i]), a1 = bf_hi(in.x[2 * i]), c0 = bf_lo(in.x[2 * i + 1]), c1 = bf_hi(in.x[2 * i + 1]);
    u0[i] = pk_bf16((a0 - mean[2 * i]) * rstd[2 * i], (c0 - mean[2 * i + 1]) * rstd[2 * i + 1]);
    u1[i] = pk_bf16((a1 - mean[2 * i]) * rstd[2 * i], (c1 - mean[2 * i + 1]) * rstd[2 * i + 1]);
    base[0][2 * i] = a0 + bf_lo(u0[i]) * d0; base[0][2 * i + 1] = c0 + bf_hi(u0[i]) * d0;
    base[1][2 * i] = a1 + bf_lo(u1[i]) * d1; base[1][2 * i + 1] = c1 + bf_hi(u1[i]) * d1;
  }
  *(LAS u32x2*)(c.lds + L_U + 512 + half * 2048 + c.tid * 8) = u0;
  *(LAS u32x2*)(c.lds + L_U + U_CH + 512 + half * 2048 + c.tid * 8) = u1;
}
constexpr int tri_j(int m) { return m < 4 ? 0 : (m < 7 ? 1 : (m < 9 ? 2 : 3)); }
constexpr int tri_i(int m) { return m < 4 ? m : (m < 7 ? m - 3 : (m < 9 ? m - 5 : 3)); }
template <int B0, bool ZERO, int WN0, int WCNT>
DEV void conv_tri(const Ctx& c, LAS char* pa, LAS char* pb, u32x4 (&Ac)[4], f32x4 (&acc)[4]) {
  u32x4 Bc[4];
#pragma unroll
  for (int j = 0; j < 4; ++j) Bc[j] = lds16(pb + (B0 + j) * 512);
  PIN();
  sfor<4>([&](auto IT) {
    constexpr int it = IT;
    u32x4 An[4], Bn[4];
    sfor<10>([&](auto M) {
      constexpr int m = M, i = tri_i(m), j = tri_j(m);
      if constexpr (it < 3 && m < 8) {
        if constexpr (m & 1) Bn[m >> 1] = lds16(pb - 64 * (it + 1) + (B0 + (m >> 1)) * 512);
        else An[m >> 1] = lds16(pa - 64 * (it + 1) + (7 - (m >> 1)) * 512);
      }
      if constexpr (m >= 8 && it * 2 + m - 8 < WCNT) ld_wgu<WN0 + it * 2 + m - 8>(c);
      mm_vv<ZERO && it == 0 && j == 0>(acc[i], Ac[i - j], Bc[j]);
      PIN();
    });
    if constexpr (it < 3) {
#pragma unroll
      for (int j = 0; j < 4; ++j) { Ac[j] = An[j]; Bc[j] = Bn[j]; }
    }
  });
  MM_DONE4(acc);
}
template <int WN0, int WCNT>
DEV void conv_cross(const Ctx& c, LAS char* pa, LAS char* pb, f32x4 (&acc)[4]) {
  u32x4 Ac[7], Bc[4];
#pragma unroll
  for (int k = 0; k < 4; ++k) { Ac[3 + k] = lds16(pa + (3 - k) * 512); if (k == 0) Bc[0] = lds16(pb); }
#pragma unroll
  for (int k = 1; k < 4; ++k) { Bc[k] = lds16(pb + k * 512); Ac[3 - k] = lds16(pa + (3 + k) * 512); }
  PIN();
  sfor<4>([&](auto IT) {
    constexpr int it = IT;
    u32x4 An[7], Bn[4];
    sfor<16>([&](auto M) {
      constexpr int m = M, j = m >> 2, i = m & 3;
      if constexpr (it < 3 && m < 11) {
        if constexpr (m < 7) An[6 - m] = lds16(pa - 64 * (it + 1) + m * 512);
        else Bn[m - 7] = lds16(pb - 64 * (it + 1) + (m - 7) * 512);
      }
      if constexpr (m >= 11 && (m & 1) && it * 3 + (m - 11) / 2 < WCNT) ld_wgu<WN0 + it * 3 + (m - 11) / 2>(c);
      mm_vv<it == 0 && j == 0>(acc[i], Ac[3 + i - j], Bc[j]);
      PIN();
    });
    if constexpr (it < 3) {
#pragma unroll
      for (int k = 0; k < 7; ++k) Ac[k] = An[k];
#pragma unroll
      for (int k = 0; k < 4; ++k) Bc[k] = Bn[k];
    }
  });
  MM_DONE4(acc);
}
DEV void conv_finish(const Ctx& c, int layer, int half, const f32x4 (&acc)[4], const float (&base)[2][4]) {
#pragma unroll
  for (int i = 0; i < 4; ++i) *(LAS f32x4*)(c.lds + L_P + c.w * 4096 + i * 1024 + (c.q * 16 + (c.l15 ^ (c.q * 4))) * 16) = acc[i];
  SYNC_LDS();
  const int qq = c.tid & 3, ii = (c.tid >> 2) & 15;
  LAS char* pr = c.lds + L_P + c.w * 1024 + (qq * 16 + (ii ^ (qq * 4))) * 16;
  u32x2 o[2];
#pragma unroll
  for (int ch = 0; ch < 2; ++ch) {
    const f32x4 a = *(LAS f32x4*)(pr + ch * 8192), bb = *(LAS f32x4*)(pr + ch * 8192 + 4096);
    o[ch].x = pk_bf16(a.x + bb.x + base[ch][0], a.y + bb.y + base[ch][1]);
    o[ch].y = pk_bf16(a.z + bb.z + base[ch][2], a.w + bb.w + base[ch][3]);
  }
  const bool odd = (c.tid & 1) != 0;
  const u32 r0 = dpp_u<0xB1>(odd ? o[0].x : o[1].x), r1 = dpp_u<0xB1>(odd ? o[0].y : o[1].y);
  const u32x4 v = {odd ? r0 : o[0].x, odd ? r1 : o[0].y, odd ? o[1].x : r0, odd ? o[1].y : r1};
  st16(c.rs, (c.tid >> 2) * 16384 + (c.tid & 1) * 32 + (c.tid & 2) * 8, OFF_XC + layer * 2097152 + half * 1048576 + c.cp * 64, v);
}

template <int LAYER> DEV void conv_phase(Ctx& c, int w0, u32 dsk) {
  refresh(c, w0);
  const int kch = c.w >> 1, khalf = c.w & 1;
  LAS char* pa = c.lds + L_KC + kch * KC_CH + (c.l15 & 3) * KC_COPY + ((c.l15 >> 2) & 1) * KC_HALF +
                 2 * (2040 - 16 * (8 * khalf + 1) - 8 * (c.l15 >> 3) + 8 * c.q) - 7 * 512;
  LAS char* pb = c.lds + L_U + kch * U_CH + 2 * (256 + 16 * (c.l15 - 8 * khalf - 1) + 8 * c.q);
  LGKM0();
  wait_edge<false>(c, LAYER == 0 ? E_P0 : E_DOWN);
  if (LAYER == 0 && c.b == 0 && c.tid == 0) __hip_atomic_store((GAS u32*)(c.ws + OFF_EPOCH), c.epoch, RLX_AGENT);
  STAMP(4 + LAYER * 20);
  ConvIn inA, inB;
  float baseA[2][4], baseB[2][4];
  conv_load<LAYER>(c, 0, inA);
  if (LAYER == 0) conv_load<LAYER>(c, 1, inB);
  PIN();
  if constexpr (LAYER == 0) sfor<16>([&](auto N) { ld_wgu<N>(c); });
  u32x4 Ac[4];
#pragma unroll
  for (int j = 0; j < 4; ++j) Ac[j] = lds16(pa + (7 - j) * 512);
  {
    u32 z = 0u;
    asm volatile("" : "+v"(z));
    *(LAS u32*)(c.lds + L_U + (c.tid >> 7) * U_CH + (c.tid & 127) * 4) = z;
  }
  PIN();
  if constexpr (LAYER == 0)
    asm volatile("s_waitcnt vmcnt(16)" : "+v"(inA.x), "+v"(inA.st[0]), "+v"(inA.st[1]), "+v"(inB.x), "+v"(inB.st[0]), "+v"(inB.st[1])::"memory");
  conv_norm<LAYER>(c, 0, inA, dsk, baseA);
  if (LAYER == 0) conv_norm<LAYER>(c, 1, inB, dsk, baseB);
  SYNC_LDS();
  STAMP(5 + LAYER * 20);
  f32x4 acc[4];
  conv_tri<0, true, 0, 0>(c, pa, pb, Ac, acc);
  conv_finish(c, LAYER, 0, acc, baseA);
  STAMP(6 + LAYER * 20);
  conv_cross<16, LAYER == 0 ? 12 : 0>(c, pa, pb, acc);
#pragma unroll
  for (int j = 0; j < 4; ++j) Ac[j] = lds16(pa + (7 - j) * 512);
  vm_wait<LAYER == 0 ? 12 : 0>();
  BARRIER();
  flag_all(c, E_CONV + 4 * LAYER);
  STAMP(7 + LAYER * 20);
  if (LAYER == 1) {
    wait_edge<false>(c, E_DOWN + 1);
    conv_load<LAYER>(c, 1, inB);
    conv_norm<LAYER>(c, 1, inB, dsk, baseB);
    SYNC_LDS();
  }
  STAMP(8 + LAYER * 20);
  conv_tri<4, false, 28, LAYER == 0 ? 8 : 0>(c, pa, pb, Ac, acc);
  conv_finish(c, LAYER, 1, acc, baseB);
  if constexpr (LAYER == 0) sfor<12>([&](auto N) { ld_wgu<36 + N>(c); });
  vm_wait<LAYER == 0 ? 12 : 0>();
  BARRIER();
  flag_all(c, E_CONV + 4 * LAYER + 1);
  STAMP(9 + LAYER * 20);
}

struct G1 {
  const char* xa;
  const char* res;
  LAS char* ring;
  u32 ldsw;
  u32 v16;
  int layer;
};
template <int HALF, bool STATS, int S>
DEV void g1_step(const Ctx& c, const G1& g, u32x4 (&WD)[NWDV], f32x4 (&acc)[3][4], u32x4 (&Bc)[4], u32x4& fl) {
  if constexpr (HALF == 0) {
    if constexpr (S == 3) { vm_wait<15>(); BARRIER(); FENCE(); }
    if constexpr (S == 7) { vm_wait<23>(); BARRIER(); FENCE(); }
    if constexpr (S == 11) { vm_wait<0>(); check_edge<false>(c, E_CONV + 4 * g.layer + 1, fl); BARRIER(); FENCE(); }
  } else {
    if constexpr (S == 3) { vm_wait<9>(); BARRIER(); FENCE(); if constexpr (STATS) flag_grp(c, 2 * g.layer); }
  }
  u32x4 Bn[4];
  sfor<12>([&](auto M) {
    constexpr int m = M, ct = m / 3, k = m % 3;
    if constexpr (S < 15 && k < 2) {
      constexpr int slot = (HALF * 4 + (S + 1) / 4) % 5;
      const u32x2 r = tr_read(g.ring + slot * 16384 + ct * 4096 + ((S + 1) & 3) * 1024 + k * 512);
      if constexpr (k == 0) { Bn[ct].x = r.x; Bn[ct].y = r.y; } else { Bn[ct].z = r.x; Bn[ct].w = r.y; }
    }
    if constexpr (k == 2) {
      if constexpr (HALF == 0) {
        if constexpr (S >= 1 && S <= 8 && ct < 3) ld_wd<3 * (S - 1) + ct>(c, WD);
        if constexpr (S == 9 && ct == 0) fl = peek_all(c, E_CONV + 4 * g.layer + 1);
        if constexpr (S >= 11 && S <= 13) dma1<ct * 1024>(g.xa + 1048576 + (S - 11) * 4096, g.v16, g.ldsw + ((S - 11 + 4) % 5) * 16384);
        if constexpr (S == 14 && ct == 0) dma1<0>(g.res + 1048576, g.v16, c.lds0 + L_RES + 4096 + c.w * 1024);
      } else {
        if constexpr (S <= 6 && ct < 3 && 24 + 3 * S + ct < KS2) ld_wd<24 + 3 * S + ct>(c, WD);
        if constexpr (S == 13 && ct == 0) fl = peek_grp(c, 2 * g.layer);
      }
    }
    if constexpr (STATS && k == 2) mm_vv<S == 0>(acc[k][ct], Bc[ct], Bc[ct]);
    else mm_av<S == 0, AW(k, S)>(acc[k][ct], Bc[ct]);
    PIN();
  });
  if constexpr (S < 15) {
#pragma unroll
    for (int ct = 0; ct < 4; ++ct) Bc[ct] = Bn[ct];
    g1_step<HALF, STATS, S + 1>(c, g, WD, acc, Bc, fl);
  } else {
    MM_DONE4(acc[2]); asm volatile("" : "+v"(acc[0][3]), "+v"(acc[1][3]), "+v"(acc[0][2]), "+v"(acc[1][2]), "+v"(acc[0][1]), "+v"(acc[1][1]));
  }
}
template <int HALF>
DEV void g1_main(const Ctx& c, const G1& g, u32x4 (&WD)[NWDV], f32x4 (&acc)[3][4], u32x4 (&Bc)[4], u32x4& fl) {
  if (c.w == 3) {
    g1_step<HALF, true, 0>(c, g, WD, acc, Bc, fl);
    const int j = c.l15 & 3;
    const bool hasq = c.q == (c.l15 >> 2);
#pragma unroll
    for (int ct = 0; ct < 4; ++ct) {
      const float s01 = (j & 1) ? acc[2][ct][1] : acc[2][ct][0], s23 = (j & 1) ? acc[2][ct][3] : acc[2][ct][2];
      const float sq = (j & 2) ? s23 : s01;
      LAS char* d = c.lds + L_ST + (ct * 16 + c.l15) * 8;
      *(LAS float*)(c.q == 3 ? d : c.lds + L_ST + 1024 + c.lane * 4) = acc[1][ct][0];
      *(LAS float*)(hasq ? d + 4 : c.lds + L_ST + 1024 + c.lane * 4) = sq;
    }
  } else {
    g1_step<HALF, false, 0>(c, g, WD, acc, Bc, fl);
  }
}
template <int HALF>
DEV void g1_epilogue(const Ctx& c, const G1& g, const f32x4 (&acc)[3][4], const f32x4 (&wsum)[3], const char* hsrc) {
  float mu[4], c1[4], c2[4], vr[4];
#pragma unroll
  for (int ct = 0; ct < 4; ++ct) {
    const f32x2 s = *(LAS f32x2*)(c.lds + L_ST + (ct * 16 + c.l15) * 8);
    mu[ct] = s.x * (-1.f / DM);
    vr[ct] = fmaxf(__builtin_fmaf(-mu[ct], mu[ct], s.y * (1.f / DM)), 0.f) + 1e-5f;
    const float r = __builtin_amdgcn_rsqf(vr[ct]);
    c1[ct] = (-2.f * 0.7978845608f * 1.44269504f) * r;
    c2[ct] = (-2.f * 0.7978845608f * 1.44269504f * 0.044715f) * r * r * r;
  }
#pragma unroll
  for (int i = 0; i < 3; ++i) {
    if constexpr (HALF == 1) {
      if (i == 0) dma4(hsrc, g.v16, g.ldsw);
      dma4(hsrc + (i + 1) * 8192, g.v16, g.ldsw + (i + 1) * 16384);
    }
    if (i == 2 && c.w == 3) break;
    u32 hv[4];
#pragma unroll
    for (int ct = 0; ct < 4; ++ct) {
      const f32x2 m2 = {mu[ct], mu[ct]};
      const f32x2 gg = __builtin_elementwise_fma(m2, wsum[i].xy, acc[i][ct].xy);
      const f32x2 uu = __builtin_elementwise_fma(m2, wsum[i].zw, acc[i][ct].zw);
      const f32x2 pl = __builtin_elementwise_fma(gg * gg, f32x2{c2[ct], c2[ct]}, f32x2{c1[ct], c1[ct]});
      const f32x2 ar = gg * pl;
      const f32x2 ex = {__builtin_amdgcn_exp2f(ar.x), __builtin_amdgcn_exp2f(ar.y)};
      const f32x2 v2 = {vr[ct], vr[ct]};
      const f32x2 dn = __builtin_elementwise_fma(ex, v2, v2);
      const f32x2 h = (gg * uu) * f32x2{__builtin_amdgcn_rcpf(dn.x), __builtin_amdgcn_rcpf(dn.y)};
      hv[ct] = pk_bf16(h.x, h.y);
    }
    swap32(hv[0], hv[2]); swap32(hv[1], hv[3]);
    swap16(hv[0], hv[1]); swap16(hv[2], hv[3]);
    const u32x4 o = {hv[0], hv[1], hv[2], hv[3]};
    const int U = c.cb * 11 + 3 * c.w + i;
    st16(c.rs, c.q * (KS2 * 1024) + c.l15 * 16, OFF_H + g.layer * H_BYTES + ((HALF * 64 + c.rb * 4) * KS2 + (U >> 2)) * 1024 + (U & 3) * 256, o);
    FENCE();
  }
}

template <int HALF, int C>
DEV void g2_chunk(const Ctx& c, int layer, LAS char* rd, const char* hsrc, u32 v16, u32 ldsw, const u32x4 (&WD)[NWDV], f32x4 (&acc)[4], u32x4 (&Bc)[8]) {
  if constexpr (C < 5) {
    constexpr int n = C == 0 ? 8 : (C == 1 ? 8 : (C == 2 ? 6 : (C == 3 ? 2 : 0)));
    vm_wait<(HALF == 0 && C == 1) ? 4 : n>();
    LGKM0();
    BARRIER(); FENCE();
    if constexpr (HALF == 0 && C == 1) flag_grp(c, 2 * layer + 1);
    if constexpr (C == 0) dma4(hsrc + 4 * 8192, v16, ldsw + 4 * 16384);
    if constexpr (C == 1) {
      const char* s5 = hsrc + 5 * 8192 - (c.w >> 1) * 2048;
      dma1<0>(s5, v16, ldsw); dma1<1024>(s5, v16, ldsw);
    }
  }
  constexpr int NK = C < 5 ? 4 : 2, NKN = C < 4 ? 4 : 2;
  u32x4 Bn[8];
  sfor<NK * 4>([&](auto M) {
    constexpr int m = M, ks = m >> 2, ct = (m >> 1) & 1, dt = m & 1, n = (C * 4 + ks) * 2 + dt;
    if constexpr (C < 5 && m < NKN * 2) Bn[m] = lds16(rd + ((C + 1) % 5) * 16384 + (m & 1) * 4096 + (m >> 1) * 1024);
    if constexpr (n < 16) mm_av<C == 0 && ks == 0, 192 + 4 * n>(acc[dt * 2 + ct], Bc[ks * 2 + ct]);
    else mm_vv<false>(acc[dt * 2 + ct], WD[n - 16], Bc[ks * 2 + ct]);
    PIN();
  });
  if constexpr (C < 5) {
#pragma unroll
    for (int m = 0; m < NKN * 2; ++m) Bc[m] = Bn[m];
    g2_chunk<HALF, C + 1>(c, layer, rd, hsrc, v16, ldsw, WD, acc, Bc);
  }
}
template <int HALF>
DEV void g2_unit(const Ctx& c, const Params& p, int layer, const char* hsrc, const u32x4 (&WD)[NWDV]) {
  const u32 v16 = c.lane * 16, ldsw = c.lds0 + L_RING + c.w * 4096;
  const int kh = c.w >> 1, dfin = 2 * (c.w & 1) + kh;
  LAS char* rd = c.lds + L_RING + kh * 8192 + c.lane * 16;
  if constexpr (HALF == 1) {
#pragma unroll
    for (int i = 0; i < 4; ++i) dma4(hsrc + i * 8192, v16, ldsw + i * 16384);
  }
  u32x2 rres[2];
#pragma unroll
  for (int ct = 0; ct < 2; ++ct) rres[ct] = tr_read(c.lds + L_RES + HALF * 4096 + ct * 2048 + dfin * 512 + c.lane * 8);
  f32x4 acc[4];
  vm_wait<12>();
  BARRIER(); FENCE();
  u32x4 Bc[8];
#pragma unroll
  for (int m = 0; m < 8; ++m) Bc[m] = lds16(rd + (m & 1) * 4096 + (m >> 1) * 1024);
  g2_chunk<HALF, 0>(c, layer, rd, hsrc, v16, ldsw, WD, acc, Bc);
  MM_DONE4(acc);
  STAMP(16 + 2 * HALF + layer * 20);
  f32x4 o[2];
#pragma unroll
  for (int ct = 0; ct < 2; ++ct) {
    *(LAS f32x4*)(c.lds + L_RING + 2 * 16384 + c.w * 2048 + ct * 1024 + c.lane * 16) = kh ? acc[ct] : acc[2 + ct];
    o[ct] = (kh ? acc[2 + ct] : acc[ct]) + f32x4{bf_lo(rres[ct].x), bf_hi(rres[ct].x), bf_lo(rres[ct].y), bf_hi(rres[ct].y)};
  }
  SYNC_LDS();
  u32 v[4];
#pragma unroll
  for (int ct = 0; ct < 2; ++ct) {
    o[ct] += *(LAS f32x4*)(c.lds + L_RING + 2 * 16384 + (c.w ^ 2) * 2048 + ct * 1024 + c.lane * 16);
    v[2 * ct] = pk_bf16(o[ct][0], o[ct][1]); v[2 * ct + 1] = pk_bf16(o[ct][2], o[ct][3]);
  }
  if (layer == 0) {
    float st[4];
#pragma unroll
    for (int ct = 0; ct < 2; ++ct) {
      const float a = bf_lo(v[2 * ct]), bb = bf_hi(v[2 * ct]), d = bf_lo(v[2 * ct + 1]), e = bf_hi(v[2 * ct + 1]);
      st[2 * ct] = (a + bb) + (d + e);
      st[2 * ct + 1] = (a * a + bb * bb) + (d * d + e * e);
    }
    swap32f(st[0], st[2]); swap32f(st[1], st[3]);
    float sA = st[0] + st[2], sB = st[1] + st[3];
    swap16f(sA, sB);
    *(LAS float*)(c.lds + L_ST + c.tid * 4) = sA + sB;
    const bool o1 = (c.lane & 1) != 0, o2 = (c.lane & 2) != 0;
    u32 t0 = dpp_u<0xB1>(o1 ? v[0] : v[1]), t1 = dpp_u<0xB1>(o1 ? v[2] : v[3]);
    const u32 a0 = o1 ? t0 : v[0], a1 = o1 ? v[1] : t0, a2 = o1 ? t1 : v[2], a3 = o1 ? v[3] : t1;
    t0 = dpp_u<0x4E>(o2 ? a0 : a2); t1 = dpp_u<0x4E>(o2 ? a1 : a3);
    const u32x4 o4 = {o2 ? t0 : a0, o2 ? t1 : a1, o2 ? a2 : t0, o2 ? a3 : t1};
    st16(c.rs, (2 * c.q + (c.lane & 1)) * 8192 + (c.lane & 2) * 32 + (c.l15 >> 2) * 16,
         OFF_XP + 2097152 + (c.db * 32 + dfin * 8) * 8192 + HALF * 4096 + c.rt * 128, o4);
    SYNC_LDS();
    if (c.w == 0) {
      LAS float* s = (LAS float*)(c.lds + L_ST) + c.lane;
      const float sv = (s[0] + s[64]) + (s[128] + s[192]);
      const int t = c.rt * 32 + (c.q >> 1) * 16 + c.l15;
      __hip_atomic_fetch_add((GAS double*)(c.ws + OFF_ST3 + HALF * 16384 + (t & 3) * 4096 + (t >> 2) * 16 + (c.q & 1) * 8), (double)sv, RLX_AGENT);
    }
    vm_wait<0>();
    BARRIER();
    flag_all(c, E_DOWN + HALF);
  } else {
    swap16(v[0], v[2]); swap16(v[1], v[3]);
    const u32x4 o4 = {v[0], v[1], v[2], v[3]};
    *(u32x4*)(p.out + (size_t)(HALF * 1024 + c.rt * 32 + (c.q & 1) * 16 + c.l15) * DM + c.db * 64 + dfin * 16 + (c.q >> 1) * 8) = o4;
    if constexpr (HALF == 0) BARRIER();
  }
  STAMP(17 + 2 * HALF + layer * 20);
}

__global__ void __launch_bounds__(256, 1) mega_kernel(Params p) {
  __shared__ __attribute__((aligned(16))) char smem[LDS_BYTES];
  const int w0 = __builtin_amdgcn_readfirstlane(threadIdx.x >> 6);
  Ctx c;
  c.ws = p.ws; c.lds = (LAS char*)smem; c.lds0 = (u32)(size_t)c.lds;
  c.rs = mk_rsrc(p.ws, WS_BYTES);
  refresh(c, w0);
  c.tj = 0;
  STAMP(0);
  u32 dsk;

  {
    const int b = c.b, tid = c.tid, lane = c.lane, w = c.w, q = c.q, l15 = c.l15;
    LAS char* lds = c.lds;
    const int kch = w >> 1, khalf = w & 1;
    const rsrc_t rx = mk_rsrc(p.x0, SEQ * DM * 2), rg = mk_rsrc(p.wg, DM * DF * 2), ru = mk_rsrc(p.wu, DM * DF * 2), rd = mk_rsrc(p.wd, DM * DF * 2);
    const u32x4 r0 = ld16p(rx, lane * 16, (8 * b + 2 * w) * 1024), r1 = ld16p(rx, lane * 16 + 1024, (8 * b + 2 * w) * 1024);
    u32x4 xt[2];
#pragma unroll
    for (int i = 0; i < 2; ++i) xt[i] = ld16p(rx, (tid >> 3) * 1024 + (tid & 7) * 16 + i * 128, (b >> 2) * 32768 + (b & 3) * 256);
    const int dch = 2 * c.cp + kch, pn = dch * NS + 64 * khalf + lane;
    const u16 pA = p.logA[pn], pI = p.Aim[pn], pCr = p.Cre[pn], pCi = p.Cim[pn], pdt = p.logdt[dch];
    dsk = *(const u32*)(p.Dskip + 2 * c.cp);
    const u32 ep = __hip_atomic_load((GAS u32*)(p.ws + OFF_EPOCH), RLX_AGENT);
    const int cbR = b >> 4, sR = b & 15;
    u32x4 wg[2], wu[2];
    int wrow[2], wtau[2];
#pragma unroll
    for (int i = 0; i < 2; ++i) {
      const int id = tid + 256 * i;
      wrow[i] = (id * 5958) >> 16; wtau[i] = id - wrow[i] * 11;
      const u32 vo = wrow[i] * (DF * 2) + wtau[i] * 16 + (id < 352 ? 0u : 0x40000000u) + sR * (32 * DF * 2) + cbR * 172;
      wg[i] = ld16p(rg, vo, 0); wu[i] = ld16p(ru, vo, 0);
    }
    u32x4 dv[2];
    const int s2 = b >> 2, dq = b & 3;
#pragma unroll
    for (int i = 0; i < 2; ++i) {
      const int krow = (tid >> 4) + 16 * i, U = 4 * s2 + (krow >> 3), cbU = (U * 5958) >> 16, fl = 8 * (U - cbU * 11) + (krow & 7);
      dv[i] = ld16p(rd, (86 * cbU + fl) * 1024 + (tid & 15) * 16 + dq * 256 + (fl < 86 ? 0u : 0x40000000u), 0);
    }
    PIN();
    int ci[15];
#pragma unroll
    for (int i = 0; i < 15; ++i) {
      const int lag = 2047 - 8 * tid - i;
      const int bits = (int)(pk_bf16((float)lag, 0.f) & 0xffffu) - (134 << 7) + 128;
      ci[i] = 4 * (lag < 0 ? 768 : (lag < 128 ? lag : bits));
      asm volatile("" : "+v"(ci[i]));
    }
    PIN();
    c.epoch = ep + 1u;
    {
      float s0 = 0.f, q0 = 0.f, s1 = 0.f, q1 = 0.f;
#pragma unroll
      for (int i = 0; i < 4; ++i) {
        float a = bf_lo(r0[i]), e = bf_hi(r0[i]);
        s0 += a + e; q0 += a * a + e * e;
        a = bf_lo(r1[i]); e = bf_hi(r1[i]);
        s1 += a + e; q1 += a * a + e * e;
      }
      s0 = wave_sum(s0); q0 = wave_sum(q0); s1 = wave_sum(s1); q1 = wave_sum(q1);
      const float m0 = s0 * (1.f / DM), m1 = s1 * (1.f / DM);
      const float v0 = fmaxf(q0 * (1.f / DM) - m0 * m0, 0.f), v1 = fmaxf(q1 * (1.f / DM) - m1 * m1, 0.f);
      const f32x4 o = {m0, __builtin_amdgcn_rsqf(v0 + 1e-5f), m1, __builtin_amdgcn_rsqf(v1 + 1e-5f)};
      const u32x4 z = {0u, 0u, 0u, 0u};
      const u32 vo = lane == 0 ? OFF_ST1 + (b >> 7) * 8192 + (w & 1) * 4096 + ((2 * b + (w >> 1)) & 255) * 16 : OFF_ST3 + b * 128 + w * 32 + (lane - 1) * 16;
      if (lane < 3) st16(c.rs, vo, 0, lane == 0 ? __builtin_bit_cast(u32x4, o) : z);
    }
#pragma unroll
    for (int i = 0; i < 2; ++i) *(LAS u32x4*)(lds + L_XT + (tid >> 3) * 272 + (tid & 7) * 16 + i * 128) = xt[i];
#pragma unroll
    for (int i = 0; i < 2; ++i) {
      const bool last = wtau[i] == 10;
      const u32x4 lo = {wg[i].x, wu[i].x, wg[i].y, wu[i].y}, hi = {wg[i].z, wu[i].z, last ? 0x00003F80u : wg[i].w, last ? 0u : wu[i].w};
      if (i == 0 || tid < 96) {
        *(LAS u32x4*)(lds + L_IMG1 + wtau[i] * 1056 + wrow[i] * 32) = lo;
        *(LAS u32x4*)(lds + L_IMG1 + wtau[i] * 1056 + wrow[i] * 32 + 16) = hi;
      }
    }
#pragma unroll
    for (int i = 0; i < 2; ++i) {
      const int krow = (tid >> 4) + 16 * i, c16 = tid & 15, kg = krow >> 3, j = krow & 7;
      *(LAS u32x4*)(lds + L_IMG2 + (c16 >> 1) * 1056 + (4 * kg + j + (j < 4 ? 0 : 12)) * 32 + (c16 & 1) * 16) = dv[i];
    }
    SYNC_LDS();
#pragma unroll
    for (int i = 0; i < 2; ++i) {
      LAS char* s = lds + L_XT + (tid & 7) * (4 * 272) + ((tid >> 3) + 32 * i) * 4;
      const u32x4 v = {*(LAS u32*)s, *(LAS u32*)(s + 272), *(LAS u32*)(s + 544), *(LAS u32*)(s + 816)};
      st16(c.rs, ((tid >> 3) + 32 * i) * 8192 + (tid & 7) * 16, OFF_XP + (b & 3) * (64 * 8192) + (b >> 2) * 128, v);
    }
#pragma unroll
    for (int i = 0; i < 3; ++i) {
      const int tau = 3 * w + i;
      if (tau < 11) st16(c.rs, lane * 16, OFF_WGU + ((cbR * 11 + tau) * 16 + sR) * 1024, tr_frag(lds + L_IMG1 + tau * 1056 + lane * 8));
    }
    if (b < 176) {
#pragma unroll
      for (int i = 0; i < 2; ++i) st16(c.rs, lane * 16, OFF_WD + ((dq * 8 + 2 * w + i) * KS2 + s2) * 1024, tr_frag(lds + L_IMG2 + (2 * w + i) * 1056 + lane * 8));
    }
    STAMP(1);

    const float dt = rnd_bf16(expf(bf2f(pdt)));
    const float a2 = -rnd_bf16(expf(bf2f(pA))) * dt * 1.44269504f;
    const float ai = bf2f(pI) * dt;
    const float i2p_hi = 0.15915494f, i2p_lo = (float)(0.15915494309189535 - (double)0.15915494f);
    const float fhi = ai * i2p_hi, flo = __builtin_fmaf(ai, i2p_hi, -fhi) + ai * i2p_lo;
    f32x2 wp[3];
#pragma unroll
    for (int i = 0; i < 3; ++i) {
      const float ps = i == 0 ? 1.f : (i == 1 ? 8.f : 128.f);
      const float mag = __builtin_amdgcn_exp2f(a2 * ps);
      const float rev = __builtin_fmaf(flo, ps, __builtin_amdgcn_fractf(fhi * ps));
      wp[i] = f32x2{mag * __builtin_amdgcn_cosf(rev), mag * __builtin_amdgcn_sinf(rev)};
    }
    const float cr = bf2f(pCr), cim = bf2f(pCi);
    int ce = 14 - __builtin_amdgcn_frexp_expf(wave_max(fmaxf(fabsf(cr), fabsf(cim))));
    ce = ce > 100 ? 100 : (ce < -100 ? -100 : ce);
    LAS char* tb = lds + w * TB_WAVE + lane * 4;
    auto put2 = [&](int ra, f32x2 va, int rb2, f32x2 vb) {
      const u32 ha = __builtin_bit_cast(u32, __builtin_amdgcn_cvt_pkrtz(va.x, va.y)), hb = __builtin_bit_cast(u32, __builtin_amdgcn_cvt_pkrtz(vb.x, vb.y));
      u32 la, lb;
      asm("v_fma_mixlo_f16 %0, %2, 1.0, -%4 op_sel_hi:[0,0,1]\n\t"
          "v_fma_mixlo_f16 %1, %5, 1.0, -%7 op_sel_hi:[0,0,1]\n\t"
          "v_fma_mixhi_f16 %0, %3, 1.0, -%4 op_sel:[0,0,1] op_sel_hi:[0,0,1]\n\t"
          "v_fma_mixhi_f16 %1, %6, 1.0, -%7 op_sel:[0,0,1] op_sel_hi:[0,0,1]"
          : "=&v"(la), "=&v"(lb) : "v"(va.x), "v"(va.y), "v"(ha), "v"(vb.x), "v"(vb.y), "v"(hb));
      *(LAS u32*)(tb + ra * TB_ROW) = ha; *(LAS u32*)(tb + TB_HALF + ra * TB_ROW) = la;
      *(LAS u32*)(tb + rb2 * TB_ROW) = hb; *(LAS u32*)(tb + TB_HALF + rb2 * TB_ROW) = lb;
    };
    {
      const f32x2 w8c = {wp[1].x, -wp[1].y}, w16c = cmul(w8c, w8c);
      f32x2 y0 = {ldexpf(cr, ce), -ldexpf(cim, ce)}, y1 = cmul(y0, w8c);
#pragma unroll
      for (int i = 0; i < 8; ++i) {
        put2(41 + 2 * i, y0, 42 + 2 * i, y1);
        if (i < 7) { y0 = cmul(y0, w16c); y1 = cmul(y1, w16c); }
      }
    }
    vm_wait<0>();
    BARRIER();
    flag_all(c, E_P0);
    STAMP(2);
    {
      f32x2 e[8];
      e[1] = wp[0]; e[2] = cmul(e[1], e[1]); e[3] = cmul(e[2], e[1]); e[4] = cmul(e[2], e[2]);
      e[5] = cmul(e[4], e[1]); e[6] = cmul(e[4], e[2]); e[7] = cmul(e[4], e[3]);
      const f32x2 w256 = cmul(wp[2], wp[2]);
      f32x2 ta = {16384.f, 0.f}, tc = wp[2] * 16384.f;
      put2(0, ta, 8, tc);
#pragma unroll
      for (int i = 1; i < 8; ++i) put2(i, e[i] * 16384.f, 8 + i, cmul(tc, e[i]));
      ta = w256 * 16384.f; tc = cmul(tc, w256);
      put2(16, ta, 20, tc);
#pragma unroll
      for (int i = 1; i < 4; ++i) put2(16 + i, cmul(ta, e[2 * i]), 20 + i, cmul(tc, e[2 * i]));
#pragma unroll
      for (int i = 0; i < 2; ++i) {
        ta = cmul(ta, w256); tc = cmul(tc, w256);
        put2(24 + 4 * i, ta, 26 + 4 * i, tc);
        put2(25 + 4 * i, cmul(ta, e[4]), 27 + 4 * i, cmul(tc, e[4]));
      }
#pragma unroll
      for (int i = 0; i < 4; ++i) {
        ta = cmul(ta, w256); tc = cmul(tc, w256);
        put2(32 + 2 * i, ta, 33 + 2 * i, tc);
      }
      ta = cmul(ta, w256);
      put2(40, ta, 40, ta);
    }
    FENCE();
    f32x4 kk[3];
#pragma unroll
    for (int i = 0; i < 3; ++i) kk[i] = f32x4{0.f, 0.f, 0.f, 0.f};
    {
      LAS char* tr = lds + w * TB_WAVE + l15 * TB_ROW + q * 16;
#pragma unroll
      for (int G = 0; G < 4; ++G) {
        const u32x4 bh = lds16(tr + 41 * TB_ROW + G * 64), bl = lds16(tr + TB_HALF + 41 * TB_ROW + G * 64);
#pragma unroll
        for (int i = 0; i < 3; ++i) {
          const u32x4 ah = lds16(tr + i * 16 * TB_ROW + G * 64), al = lds16(tr + TB_HALF + i * 16 * TB_ROW + G * 64);
          kk[i] = mfma_f16(al, bh, kk[i]);
          kk[i] = mfma_f16(ah, bl, kk[i]);
          kk[i] = mfma_f16(ah, bh, kk[i]);
        }
      }
      const float ks = ldexpf(2.f * dt, -14 - ce);
#pragma unroll
      for (int i = 0; i < 3; ++i) kk[i] *= ks;
    }
    STAMP(40);
    LAS char* kx = lds + L_KX + kch * 3072 + lane * 16;
    if (khalf) {
#pragma unroll
      for (int i = 0; i < 3; ++i) *(LAS f32x4*)(kx + i * 1024) = kk[i];
    }
    SYNC_LDS();
    if (!khalf) {
#pragma unroll
      for (int i = 0; i < 3; ++i) kk[i] += *(LAS f32x4*)(kx + i * 1024);
      LAS float* T = (LAS float*)(lds + L_KT + kch * KT_CH);
      const int i0 = 128 * (q >> 1) + 4 * (q & 1) + 8 * l15;
      *(LAS f32x4*)(T + i0) = kk[0];
      if (q < 2) {
        *(LAS f32x4*)(T + 256 + 64 * q + 4 * l15) = kk[1];
      } else {
        *(LAS f32x2*)(T + 384 + 64 * (q - 2) + 2 * l15) = kk[1].xy;
        *(LAS f32x2*)(T + 416 + 64 * (q - 2) + 2 * l15) = kk[1].zw;
      }
#pragma unroll
      for (int j = 0; j < 4; ++j) T[512 + 64 * q + 16 * j + l15] = kk[2][j];
      if (lane == 0) T[768] = 0.f;
    }
    SYNC_LDS();
    STAMP(41);
#pragma unroll
    for (int ch = 0; ch < 2; ++ch) {
      float V[15];
#pragma unroll
      for (int i = 0; i < 15; ++i) V[i] = *(LAS float*)(lds + L_KT + ch * KT_CH + ci[i]);
      u32 pkd[14];
#pragma unroll
      for (int i = 0; i < 14; ++i) pkd[i] = pk_bf16(V[i], V[i + 1]);
#pragma unroll
      for (int s = 0; s < 8; ++s) {
        const u32x4 o = {pkd[7 - s], pkd[9 - s], pkd[11 - s], pkd[13 - s]};
        *(LAS u32x4*)(lds + L_KC + ch * KC_CH + (s & 3) * KC_COPY + (s >> 2) * KC_HALF + tid * 16) = o;
      }
    }
    if (tid < 16) {
      const u32x4 z = {0u, 0u, 0u, 0u};
      *(LAS u32x4*)(lds + L_KC + (tid >> 3) * KC_CH + (tid & 3) * KC_COPY + ((tid >> 2) & 1) * KC_HALF + 4096) = z;
    }
  }
  STAMP(3);

  conv_phase<0>(c, w0, dsk);
  vm_wait<0>();

#pragma unroll 1
  for (int layer = 0; layer < 2; ++layer) {
    refresh(c, w0);
    G1 g;
    g.layer = layer;
    g.xa = c.ws + OFF_XC + layer * 2097152 + (size_t)(c.rb * 4 + c.w) * 16384;
    g.res = c.ws + OFF_XC + layer * 2097152 + (size_t)(c.rt * 2 + (c.w >> 1)) * 16384 + c.db * 2048 + (c.w & 1) * 1024;
    g.ring = c.lds + L_RING + c.lane * 8;
    g.ldsw = c.lds0 + L_RING + c.w * 4096;
    g.v16 = c.lane * 16;
    wait_edge<false>(c, E_CONV + 4 * layer);
    STAMP(10 + layer * 20);
#pragma unroll
    for (int i = 0; i < 4; ++i) dma4(g.xa + i * 4096, g.v16, g.ldsw + i * 16384);
    dma1<0>(g.res, g.v16, c.lds0 + L_RES + c.w * 1024);
    PIN();
    f32x4 wsum[3];
    {
      u32x4 ones = {0x3F803F80u, 0x3F803F80u, 0x3F803F80u, 0x3F803F80u};
      asm volatile("s_nop 1" : "+v"(ones));
      sfor<48>([&](auto N) { mm_av<(N < 3), AW(N % 3, N / 3)>(wsum[N % 3], ones); });
      asm volatile("s_nop 7" : "+v"(wsum[0]), "+v"(wsum[1]), "+v"(wsum[2]));
    }
    PIN();
    u32x4 WD[NWDV], Bc[4], fl;
    f32x4 acc[3][4];
    vm_wait<13>();
    BARRIER(); FENCE();
#pragma unroll
    for (int ct = 0; ct < 4; ++ct) Bc[ct] = tr_frag(g.ring + ct * 4096);
    STAMP(11 + layer * 20);
    g1_main<0>(c, g, WD, acc, Bc, fl);
    vm_wait<9>();
    SYNC_LDS();
    STAMP(12 + layer * 20);
    dma4(g.xa + 1048576 + 3 * 4096, g.v16, g.ldsw + 2 * 16384);
    g1_epilogue<0>(c, g, acc, wsum, nullptr);
#pragma unroll
    for (int ct = 0; ct < 4; ++ct) Bc[ct] = tr_frag(g.ring + 4 * 16384 + ct * 4096);
    STAMP(13 + layer * 20);
    g1_main<1>(c, g, WD, acc, Bc, fl);
    check_edge<true>(c, 2 * layer, fl);
    SYNC_LDS();
    STAMP(14 + layer * 20);
    refresh(c, w0);
    const char* hsrc = c.ws + OFF_H + layer * H_BYTES + (size_t)(c.rt * 2 + (c.w & 1)) * (KS2 * 1024) + (c.w >> 1) * 4096;
    g1_epilogue<1>(c, g, acc, wsum, hsrc);
    STAMP(15 + layer * 20);
    g2_unit<0>(c, p, layer, hsrc, WD);
    wait_edge<true>(c, 2 * layer + 1);
    g2_unit<1>(c, p, layer, hsrc + (size_t)64 * KS2 * 1024, WD);
    if (layer == 0) conv_phase<1>(c, w0, dsk);
  }
  if (blockIdx.x == 5) { const unsigned long long te = wall_clock64() + 4 * (c.tj - c.t0); while (wall_clock64() < te) __builtin_amdgcn_s_sleep(2); }
}

}

extern "C" void kernel_launch(void* const* d_in, const int* in_sizes, int n_in, void* d_out, int out_size, void* d_ws,
                              size_t ws_size, hipStream_t stream) {
  Params p;
  p.x0 = (const u16*)d_in[0];
  p.logA = (const u16*)d_in[1];
  p.Aim = (const u16*)d_in[2];
  p.Cre = (const u16*)d_in[3];
  p.Cim = (const u16*)d_in[4];
  p.logdt = (const u16*)d_in[5];
  p.Dskip = (const u16*)d_in[6];
  p.wg = (const u16*)d_in[8];
  p.wu = (const u16*)d_in[9];
  p.wd = (const u16*)d_in[10];
  p.out = (u16*)d_out;
  p.ws = (char*)d_ws;
  mega_kernel<<<NBLK, 256, 0, stream>>>(p);
}
```

```cpp
#define CODE_PREFETCH 44800
#define PROBE 0
#include <hip/hip_runtime.h>
#include <hip/hip_bf16.h>
#include <utility>

typedef unsigned int u32;
typedef unsigned short u16;
typedef u32 u32x4 __attribute__((ext_vector_type(4)));
typedef u32 u32x2 __attribute__((ext_vector_type(2)));
typedef float f32x4 __attribute__((ext_vector_type(4)));
typedef float f32x2 __attribute__((ext_vector_type(2)));
typedef short s16x4 __attribute__((ext_vector_type(4)));
typedef __bf16 bf16x8 __attribute__((ext_vector_type(8)));
typedef __bf16 bf16x2 __attribute__((ext_vector_type(2)));
typedef _Float16 f16x8 __attribute__((ext_vector_type(8)));
#define LAS __attribute__((address_space(3)))
#define GAS __attribute__((address_space(1)))
#define RLX_AGENT __ATOMIC_RELAXED, __HIP_MEMORY_SCOPE_AGENT
#define DEV __device__ __forceinline__

namespace {

constexpr int SEQ = 2048, DM = 512, NS = 128, DF = 1376;
constexpr int NBLK = 256;
constexpr int KS2 = 44;

constexpr u32 OFF_EPOCH = 0;
constexpr u32 OFF_FLAG = 4096;
constexpr u32 OFF_GFLAG = OFF_FLAG + 8 * 8192;
constexpr u32 OFF_ST1 = OFF_GFLAG + 8192;
constexpr u32 OFF_ST3 = OFF_ST1 + 16384;
constexpr u32 OFF_WGU = OFF_ST3 + 32768;
constexpr u32 OFF_WD = OFF_WGU + 16 * 11 * 16 * 1024;
constexpr u32 OFF_XC = OFF_WD + 32 * KS2 * 1024;
constexpr u32 OFF_XP = OFF_XC + 2 * 2097152;
constexpr u32 OFF_H = OFF_XP + 2 * 2097152;
constexpr u32 H_BYTES = 128 * KS2 * 1024;
constexpr u32 OFF_DBG = OFF_H + 2 * H_BYTES;
constexpr u32 WS_BYTES = OFF_DBG + 256 * 64 * 16;
constexpr int E_P0 = 0, E_CONV = 1  , E_DOWN = 3  ;

constexpr int KC_COPY = 4160, KC_HALF = 4 * KC_COPY + 32, KC_CH = 33344;
constexpr int L_KC = 0;
constexpr int L_RING = 2 * KC_CH;
constexpr int L_RES = L_RING + 5 * 16384;
constexpr int L_ST = L_RES + 8192;
constexpr int LDS_BYTES = 163840;
constexpr int L_U = L_RING;
constexpr int U_CH = 4608;
constexpr int L_P = L_RING + 16384;
constexpr int TB_ROW = 272, TB_HALF = 57 * TB_ROW, TB_WAVE = 2 * TB_HALF;
constexpr int L_IMG1 = 4 * TB_WAVE;
constexpr int L_IMG2 = L_IMG1 + 11 * 1056;
constexpr int L_XT = L_IMG2 + 8 * 1056;
constexpr int L_KX = L_XT + 32 * 272;
constexpr int L_KT = L_IMG1;
constexpr int KT_CH = 772 * 4;
static_assert(L_KX + 6144 <= LDS_BYTES, "lds");

struct Params {
  const u16 *x0, *logA, *Aim, *Cre, *Cim, *logdt, *Dskip, *wg, *wu, *wd;
  u16* out;
  char* ws;
};

DEV float bf_lo(u32 v) { return __builtin_bit_cast(float, v << 16); }
DEV float bf_hi(u32 v) { return __builtin_bit_cast(float, v & 0xffff0000u); }
DEV float bf2f(u16 v) { return __builtin_bit_cast(float, (u32)v << 16); }
DEV u32 pk_bf16(float lo, float hi) {
  f32x2 f = {lo, hi};
  return __builtin_bit_cast(u32, __builtin_convertvector(f, bf16x2));
}
DEV float rnd_bf16(float v) { return bf_lo(pk_bf16(v, 0.f)); }
DEV f32x4 mfma_bf16(u32x4 a, u32x4 b, f32x4 c) {
  return __builtin_amdgcn_mfma_f32_16x16x32_bf16(__builtin_bit_cast(bf16x8, a), __builtin_bit_cast(bf16x8, b), c, 0, 0, 0);
}
DEV f32x4 mfma_f16(u32x4 a, u32x4 b, f32x4 c) {
  return __builtin_amdgcn_mfma_f32_16x16x32_f16(__builtin_bit_cast(f16x8, a), __builtin_bit_cast(f16x8, b), c, 0, 0, 0);
}
DEV u32x2 tr_read(LAS char* p) {
  return __builtin_bit_cast(u32x2, __builtin_amdgcn_ds_read_tr16_b64_v4i16((LAS s16x4*)p));
}
DEV u32x4 tr_frag(LAS char* p) {
  u32x2 a = tr_read(p), b = tr_read(p + 512);
  u32x4 r = {a.x, a.y, b.x, b.y};
  return r;
}
DEV u32x4 lds16(LAS char* p) { return *(LAS u32x4*)p; }
template <int CTRL> DEV float dpp_f(float v) {
  return __builtin_bit_cast(float, __builtin_amdgcn_mov_dpp(__builtin_bit_cast(int, v), CTRL, 0xf, 0xf, true));
}
template <int CTRL> DEV u32 dpp_u(u32 v) { return (u32)__builtin_amdgcn_mov_dpp((int)v, CTRL, 0xf, 0xf, true); }
DEV void swap16(u32& a, u32& b) { auto r = __builtin_amdgcn_permlane16_swap(a, b, false, false); a = r[0]; b = r[1]; }
DEV void swap32(u32& a, u32& b) { auto r = __builtin_amdgcn_permlane32_swap(a, b, false, false); a = r[0]; b = r[1]; }
DEV void swap16f(float& a, float& b) { u32 x = __builtin_bit_cast(u32, a), y = __builtin_bit_cast(u32, b); swap16(x, y); a = __builtin_bit_cast(float, x); b = __builtin_bit_cast(float, y); }
DEV void swap32f(float& a, float& b) { u32 x = __builtin_bit_cast(u32, a), y = __builtin_bit_cast(u32, b); swap32(x, y); a = __builtin_bit_cast(float, x); b = __builtin_bit_cast(float, y); }
DEV float wave_sum(float v) {
  v += dpp_f<0xB1>(v);
  v += dpp_f<0x4E>(v);
  v += dpp_f<0x141>(v);
  v += dpp_f<0x140>(v);
  float a = v, b = v;
  swap16f(a, b); v = a + b;
  a = v; b = v;
  swap32f(a, b); v = a + b;
  return v;
}
DEV float wave_max(float v) {
  v = fmaxf(v, dpp_f<0xB1>(v)); v = fmaxf(v, dpp_f<0x4E>(v)); v = fmaxf(v, dpp_f<0x141>(v)); v = fmaxf(v, dpp_f<0x140>(v));
  float a = v, b = v;
  swap16f(a, b); v = fmaxf(a, b);
  a = v; b = v;
  swap32f(a, b); v = fmaxf(a, b);
  return v;
}
DEV f32x2 cmul(f32x2 a, f32x2 b) {
  const f32x2 t = a.yy * f32x2{-b.y, b.x};
  return __builtin_elementwise_fma(a.xx, b, t);
}
#define ACLOB "a0","a1","a2","a3","a4","a5","a6","a7","a8","a9","a10","a11","a12","a13","a14","a15","a16","a17","a18","a19","a20","a21","a22","a23","a24","a25","a26","a27","a28","a29","a30","a31","a32","a33","a34","a35","a36","a37","a38","a39","a40","a41","a42","a43","a44","a45","a46","a47","a48","a49","a50","a51","a52","a53","a54","a55","a56","a57","a58","a59","a60","a61","a62","a63","a64","a65","a66","a67","a68","a69","a70","a71","a72","a73","a74","a75","a76","a77","a78","a79","a80","a81","a82","a83","a84","a85","a86","a87","a88","a89","a90","a91","a92","a93","a94","a95","a96","a97","a98","a99","a100","a101","a102","a103","a104","a105","a106","a107","a108","a109","a110","a111","a112","a113","a114","a115","a116","a117","a118","a119","a120","a121","a122","a123","a124","a125","a126","a127","a128","a129","a130","a131","a132","a133","a134","a135","a136","a137","a138","a139","a140","a141","a142","a143","a144","a145","a146","a147","a148","a149","a150","a151","a152","a153","a154","a155","a156","a157","a158","a159","a160","a161","a162","a163","a164","a165","a166","a167","a168","a169","a170","a171","a172","a173","a174","a175","a176","a177","a178","a179","a180","a181","a182","a183","a184","a185","a186","a187","a188","a189","a190","a191","a192","a193","a194","a195","a196","a197","a198","a199","a200","a201","a202","a203","a204","a205","a206","a207","a208","a209","a210","a211","a212","a213","a214","a215","a216","a217","a218","a219","a220","a221","a222","a223","a224","a225","a226","a227","a228","a229","a230","a231","a232","a233","a234","a235","a236","a237","a238","a239","a240","a241","a242","a243","a244","a245","a246","a247","a248","a249","a250","a251","a252","a253","a254","a255"
#define BARRIER() __builtin_amdgcn_s_barrier()
#define FENCE() asm volatile("" ::: "memory")
#define PIN() __builtin_amdgcn_sched_barrier(0)
#define LGKM0() asm volatile("s_waitcnt lgkmcnt(0)" ::: "memory", ACLOB)
#define SYNC_LDS() do { LGKM0(); BARRIER(); FENCE(); } while (0)
template <int N> DEV void vm_wait() { asm volatile("s_waitcnt vmcnt(%0)" ::"i"(N) : "memory", ACLOB); }

typedef __amdgpu_buffer_rsrc_t rsrc_t;
DEV rsrc_t mk_rsrc(const void* p, u32 bytes) { return __builtin_amdgcn_make_buffer_rsrc((void*)p, 0, bytes, 0x00020000); }
DEV u32x4 ld16(rsrc_t r, u32 voff, u32 soff) { return __builtin_amdgcn_raw_buffer_load_b128(r, voff, soff, 16); }
DEV u32x4 ld16p(rsrc_t r, u32 voff, u32 soff) { return __builtin_amdgcn_raw_buffer_load_b128(r, voff, soff, 0); }
DEV void st16(rsrc_t r, u32 voff, u32 soff, u32x4 v) { __builtin_amdgcn_raw_buffer_store_b128(v, r, voff, soff, 16); }

template <int... Is, class F> DEV void sfor_impl(std::integer_sequence<int, Is...>, F&& f) { (f(std::integral_constant<int, Is>{}), ...); }
template <int N, class F> DEV void sfor(F&& f) { sfor_impl(std::make_integer_sequence<int, N>{}, f); }
template <bool FIRST> DEV void mm_vv(f32x4& acc, const u32x4& a, const u32x4& b) {
  if constexpr (FIRST) asm volatile("v_mfma_f32_16x16x32_bf16 %0, %1, %2, 0" : "=&v"(acc) : "v"(a), "v"(b));
  else asm volatile("v_mfma_f32_16x16x32_bf16 %0, %1, %2, %0" : "+v"(acc) : "v"(a), "v"(b));
}
template <bool FIRST, int A> DEV void mm_av(f32x4& acc, const u32x4& b) {
  if constexpr (FIRST) asm volatile("v_mfma_f32_16x16x32_bf16 %0, a[%c2:%c3], %1, 0" : "=&v"(acc) : "v"(b), "i"(A), "i"(A + 3));
  else asm volatile("v_mfma_f32_16x16x32_bf16 %0, a[%c2:%c3], %1, %0" : "+v"(acc) : "v"(b), "i"(A), "i"(A + 3));
}
template <int A, int OFF> DEV void ld16_a(rsrc_t r, u32 voff, u32 soff) {
  asm volatile("buffer_load_dwordx4 a[%c3:%c4], %0, %1, %2 offen offset:%c5 sc1" ::"v"(voff), "s"(r), "s"(soff), "i"(A), "i"(A + 3), "i"(OFF) : "memory");
}
#define MM_DONE4(a) asm volatile("s_nop 7" : "+v"((a)[0]), "+v"((a)[1]), "+v"((a)[2]), "+v"((a)[3]))

template <int OFF> DEV void dma1(const char* base, u32 voff, u32 lds) {
  asm volatile("s_mov_b32 m0, %2\n\ts_nop 0\n\tglobal_load_lds_dwordx4 %0, %1 offset:%3 sc1" ::"v"(voff), "s"(base), "s"(lds), "i"(OFF) : "memory");
}
DEV void dma4(const char* base, u32 voff, u32 lds) {
  asm volatile(
      "s_mov_b32 m0, %2\n\ts_nop 0\n\t"
      "global_load_lds_dwordx4 %0, %1 sc1\n\t"
      "global_load_lds_dwordx4 %0, %1 offset:1024 sc1\n\t"
      "global_load_lds_dwordx4 %0, %1 offset:2048 sc1\n\t"
      "global_load_lds_dwordx4 %0, %1 offset:3072 sc1" ::"v"(voff), "s"(base), "s"(lds) : "memory");
}

constexpr u32 SPIN_CAP = 1u << 16;
constexpr int SYNC_WAVE = 3;

struct Ctx {
  char* ws;
  rsrc_t rs;
  LAS char* lds;
  u32 lds0, epoch;
  int b, tid, lane, w, q, l15, cp, cb, rb, db, rt;
  mutable unsigned long long t0, tj;
};

DEV void flag_all(const Ctx& c, int edge) {
  if (c.w == SYNC_WAVE && c.lane < 8)
    __hip_atomic_store((GAS u32*)(c.ws + OFF_FLAG + edge * 8192 + c.lane * 1024 + c.b * 4), c.epoch, RLX_AGENT);
}
DEV void flag_grp(const Ctx& c, int edge) {
  if (c.w == SYNC_WAVE && c.lane == 0)
    __hip_atomic_store((GAS u32*)(c.ws + OFF_GFLAG + edge * 2048 + c.rb * 128 + c.cb * 4), c.epoch, RLX_AGENT);
}
DEV u32x4 peek_all(const Ctx& c, int edge) { return ld16(c.rs, c.lane * 16, OFF_FLAG + edge * 8192 + (c.b & 7) * 1024); }
DEV u32x4 peek_grp(const Ctx& c, int edge) { return ld16(c.rs, (c.lane & 3) * 16, OFF_GFLAG + edge * 2048 + (c.rt >> 1) * 128); }
DEV bool flags_ok(const Ctx& c, u32x4 f) { return __all(f.x == c.epoch && f.y == c.epoch && f.z == c.epoch && f.w == c.epoch); }
template <bool GRP> DEV void spin(const Ctx& c, int edge) {
#pragma unroll 1
  for (u32 sp = 0; sp < SPIN_CAP; ++sp) {
    if (flags_ok(c, GRP ? peek_grp(c, edge) : peek_all(c, edge))) break;
    __builtin_amdgcn_s_sleep(1);
    FENCE();
  }
}
template <bool GRP> DEV void wait_edge(const Ctx& c, int edge) {
  if (c.w == SYNC_WAVE) spin<GRP>(c, edge);
  FENCE(); BARRIER(); FENCE();
}
template <bool GRP> DEV void check_edge(const Ctx& c, int edge, u32x4 f) {
  if (!flags_ok(c, f)) spin<GRP>(c, edge);
  FENCE();
}

#define STAMP(i) do { if ((i) == 0) c.t0 = wall_clock64(); if ((i) == PROBE) c.tj = wall_clock64(); } while (0)

DEV void refresh(Ctx& c, int w0) {
  int tid = threadIdx.x, b = blockIdx.x, w = w0;
  asm volatile("" : "+v"(tid)); asm volatile("" : "+s"(b)); asm volatile("" : "+s"(w));
  c.tid = tid; c.b = b; c.w = w; c.lane = tid & 63; c.q = c.lane >> 4; c.l15 = tid & 15;
  const int xcd = b & 7, rr = b >> 3;
  c.cp = xcd * 32 + rr;
  c.cb = (xcd & 3) * 4 + (rr & 3); c.rb = (xcd >> 2) * 8 + (rr >> 2);
  c.db = (xcd & 1) * 4 + (rr & 3); c.rt = (xcd >> 1) * 8 + (rr >> 2);
}

constexpr int AW(int tile, int s) { return (tile * 16 + s) * 4; }
template <int N> DEV void ld_wgu(const Ctx& c) {
  constexpr int tl = N % 3, s = N / 3;
  const u32 v = c.lane * 16 + ((tl == 2 && c.w == 3) ? 0x40000000u : 0u);
  ld16_a<AW(tl, s), (s & 3) * 1024>(c.rs, v, OFF_WGU + (c.cb * 11 + 3 * c.w + tl) * 16384 + (s >> 2) * 4096);
}
constexpr int NWDV = KS2 - 16;
template <int N> DEV void ld_wd(const Ctx& c, u32x4 (&WD)[NWDV]) {
  constexpr int j = N >> 1;
  const int kh = c.w >> 1;
  const u32 base = OFF_WD + (c.db * 4 + 2 * (c.w & 1) + (N & 1)) * (KS2 * 1024);
  if constexpr (N < 16) ld16_a<192 + 4 * N, (j & 3) * 1024>(c.rs, c.lane * 16, base + ((j >> 2) * 8 + 4 * kh) * 1024);
  else if constexpr (j < 20) WD[N - 16] = ld16(c.rs, c.lane * 16 + (j & 3) * 1024, base + ((j >> 2) * 8 + 4 * kh) * 1024);
  else WD[N - 16] = ld16(c.rs, c.lane * 16 + (j & 1) * 1024, base + (40 + 2 * kh) * 1024);
}

struct ConvIn { u32x4 x; u32x4 st[4]; };
DEV void ld16_v(u32x4& d, rsrc_t r, u32 voff, u32 soff) {
  asm volatile("buffer_load_dwordx4 %0, %1, %2, %3 offen sc1" : "=v"(d) : "v"(voff), "s"(r), "s"(soff) : "memory");
}
template <int LAYER> DEV void conv_load(const Ctx& c, int half, ConvIn& in) {
  if (LAYER == 0) {
    ld16_v(in.x, c.rs, c.tid * 16, OFF_XP + c.cp * 8192 + half * 4096);
#pragma unroll
    for (int i = 0; i < 2; ++i) ld16_v(in.st[i], c.rs, c.tid * 16, OFF_ST1 + half * 8192 + i * 4096);
  } else {
    in.x = ld16(c.rs, c.tid * 16, OFF_XP + 2097152 + c.cp * 8192 + half * 4096);
#pragma unroll
    for (int i = 0; i < 4; ++i) in.st[i] = ld16(c.rs, c.tid * 16, OFF_ST3 + half * 16384 + i * 4096);
  }
}
template <int LAYER> DEV void conv_norm(const Ctx& c, int half, const ConvIn& in, u32 dsk, float (&base)[2][4]) {
  float mean[4], rstd[4];
  if (LAYER == 0) {
#pragma unroll
    for (int i = 0; i < 2; ++i) {
      const f32x4 v = __builtin_bit_cast(f32x4, in.st[i]);
      mean[2 * i] = v.x; rstd[2 * i] = v.y; mean[2 * i + 1] = v.z; rstd[2 * i + 1] = v.w;
    }
  } else {
#pragma unroll
    for (int i = 0; i < 4; ++i) {
      const u32x2 lo = {in.st[i].x, in.st[i].y}, hi = {in.st[i].z, in.st[i].w};
      const float m = (float)__builtin_bit_cast(double, lo) * (1.f / DM);
      const float e2 = (float)__builtin_bit_cast(double, hi) * (1.f / DM);
      mean[i] = m;
      rstd[i] = __builtin_amdgcn_rsqf(fmaxf(__builtin_fmaf(-m, m, e2), 0.f) + 1e-5f);
    }
  }
  const float d0 = bf_lo(dsk), d1 = bf_hi(dsk);
  u32x2 u0, u1;
#pragma unroll
  for (int i = 0; i < 2; ++i) {
    const float a0 = bf_lo(in.x[2 * i]), a1 = bf_hi(in.x[2 * i]), c0 = bf_lo(in.x[2 * i + 1]), c1 = bf_hi(in.x[2 * i + 1]);
    u0[i] = pk_bf16((a0 - mean[2 * i]) * rstd[2 * i], (c0 - mean[2 * i + 1]) * rstd[2 * i + 1]);
    u1[i] = pk_bf16((a1 - mean[2 * i]) * rstd[2 * i], (c1 - mean[2 * i + 1]) * rstd[2 * i + 1]);
    base[0][2 * i] = a0 + bf_lo(u0[i]) * d0; base[0][2 * i + 1] = c0 + bf_hi(u0[i]) * d0;
    base[1][2 * i] = a1 + bf_lo(u1[i]) * d1; base[1][2 * i + 1] = c1 + bf_hi(u1[i]) * d1;
  }
  *(LAS u32x2*)(c.lds + L_U + 512 + half * 2048 + c.tid * 8) = u0;
  *(LAS u32x2*)(c.lds + L_U + U_CH + 512 + half * 2048 + c.tid * 8) = u1;
}
constexpr int tri_j(int m) { return m < 4 ? 0 : (m < 7 ? 1 : (m < 9 ? 2 : 3)); }
constexpr int tri_i(int m) { return m < 4 ? m : (m < 7 ? m - 3 : (m < 9 ? m - 5 : 3)); }
template <int B0, bool ZERO, int WN0, int WCNT>
DEV void conv_tri(const Ctx& c, LAS char* pa, LAS char* pb, u32x4 (&Ac)[4], f32x4 (&acc)[4]) {
  u32x4 Bc[4];
#pragma unroll
  for (int j = 0; j < 4; ++j) Bc[j] = lds16(pb + (B0 + j) * 512);
  PIN();
  sfor<4>([&](auto IT) {
    constexpr int it = IT;
    u32x4 An[4], Bn[4];
    sfor<10>([&](auto M) {
      constexpr int m = M, i = tri_i(m), j = tri_j(m);
      if constexpr (it < 3 && m < 8) {
        if constexpr (m & 1) Bn[m >> 1] = lds16(pb - 64 * (it + 1) + (B0 + (m >> 1)) * 512);
        else An[m >> 1] = lds16(pa - 64 * (it + 1) + (7 - (m >> 1)) * 512);
      }
      if constexpr (m >= 8 && it * 2 + m - 8 < WCNT) ld_wgu<WN0 + it * 2 + m - 8>(c);
      mm_vv<ZERO && it == 0 && j == 0>(acc[i], Ac[i - j], Bc[j]);
      PIN();
    });
    if constexpr (it < 3) {
#pragma unroll
      for (int j = 0; j < 4; ++j) { Ac[j] = An[j]; Bc[j] = Bn[j]; }
    }
  });
  MM_DONE4(acc);
}
template <int WN0, int WCNT>
DEV void conv_cross(const Ctx& c, LAS char* pa, LAS char* pb, f32x4 (&acc)[4]) {
  u32x4 Ac[7], Bc[4];
#pragma unroll
  for (int k = 0; k < 4; ++k) { Ac[3 + k] = lds16(pa + (3 - k) * 512); if (k == 0) Bc[0] = lds16(pb); }
#pragma unroll
  for (int k = 1; k < 4; ++k) { Bc[k] = lds16(pb + k * 512); Ac[3 - k] = lds16(pa + (3 + k) * 512); }
  PIN();
  sfor<4>([&](auto IT) {
    constexpr int it = IT;
    u32x4 An[7], Bn[4];
    sfor<16>([&](auto M) {
      constexpr int m = M, j = m >> 2, i = m & 3;
      if constexpr (it < 3 && m < 11) {
        if constexpr (m < 7) An[6 - m] = lds16(pa - 64 * (it + 1) + m * 512);
        else Bn[m - 7] = lds16(pb - 64 * (it + 1) + (m - 7) * 512);
      }
      if constexpr (m >= 11 && (m & 1) && it * 3 + (m - 11) / 2 < WCNT) ld_wgu<WN0 + it * 3 + (m - 11) / 2>(c);
      mm_vv<it == 0 && j == 0>(acc[i], Ac[3 + i - j], Bc[j]);
      PIN();
    });
    if constexpr (it < 3) {
#pragma unroll
      for (int k = 0; k < 7; ++k) Ac[k] = An[k];
#pragma unroll
      for (int k = 0; k < 4; ++k) Bc[k] = Bn[k];
    }
  });
  MM_DONE4(acc);
}
DEV void conv_finish(const Ctx& c, int layer, int half, const f32x4 (&acc)[4], const float (&base)[2][4]) {
#pragma unroll
  for (int i = 0; i < 4; ++i) *(LAS f32x4*)(c.lds + L_P + c.w * 4096 + i * 1024 + (c.q * 16 + (c.l15 ^ (c.q * 4))) * 16) = acc[i];
  SYNC_LDS();
  const int qq = c.tid & 3, ii = (c.tid >> 2) & 15;
  LAS char* pr = c.lds + L_P + c.w * 1024 + (qq * 16 + (ii ^ (qq * 4))) * 16;
  u32x2 o[2];
#pragma unroll
  for (int ch = 0; ch < 2; ++ch) {
    const f32x4 a = *(LAS f32x4*)(pr + ch * 8192), bb = *(LAS f32x4*)(pr + ch * 8192 + 4096);
    o[ch].x = pk_bf16(a.x + bb.x + base[ch][0], a.y + bb.y + base[ch][1]);
    o[ch].y = pk_bf16(a.z + bb.z + base[ch][2], a.w + bb.w + base[ch][3]);
  }
  const bool odd = (c.tid & 1) != 0;
  const u32 r0 = dpp_u<0xB1>(odd ? o[0].x : o[1].x), r1 = dpp_u<0xB1>(odd ? o[0].y : o[1].y);
  const u32x4 v = {odd ? r0 : o[0].x, odd ? r1 : o[0].y, odd ? o[1].x : r0, odd ? o[1].y : r1};
  st16(c.rs, (c.tid >> 2) * 16384 + (c.tid & 1) * 32 + (c.tid & 2) * 8, OFF_XC + layer * 2097152 + half * 1048576 + c.cp * 64, v);
}

template <int LAYER> DEV void conv_phase(Ctx& c, int w0, u32 dsk) {
  refresh(c, w0);
  const int kch = c.w >> 1, khalf = c.w & 1;
  LAS char* pa = c.lds + L_KC + kch * KC_CH + (c.l15 & 3) * KC_COPY + ((c.l15 >> 2) & 1) * KC_HALF +
                 2 * (2040 - 16 * (8 * khalf + 1) - 8 * (c.l15 >> 3) + 8 * c.q) - 7 * 512;
  LAS char* pb = c.lds + L_U + kch * U_CH + 2 * (256 + 16 * (c.l15 - 8 * khalf - 1) + 8 * c.q);
  LGKM0();
  wait_edge<false>(c, LAYER == 0 ? E_P0 : E_DOWN);
  if (LAYER == 0 && c.b == 0 && c.tid == 0) __hip_atomic_store((GAS u32*)(c.ws + OFF_EPOCH), c.epoch, RLX_AGENT);
  STAMP(4 + LAYER * 20);
  ConvIn inA, inB;
  float baseA[2][4], baseB[2][4];
  conv_load<LAYER>(c, 0, inA);
  if (LAYER == 0) conv_load<LAYER>(c, 1, inB);
  PIN();
  if constexpr (LAYER == 0) sfor<16>([&](auto N) { ld_wgu<N>(c); });
  u32x4 Ac[4];
#pragma unroll
  for (int j = 0; j < 4; ++j) Ac[j] = lds16(pa + (7 - j) * 512);
  {
    u32 z = 0u;
    asm volatile("" : "+v"(z));
    *(LAS u32*)(c.lds + L_U + (c.tid >> 7) * U_CH + (c.tid & 127) * 4) = z;
  }
  PIN();
  if constexpr (LAYER == 0)
    asm volatile("s_waitcnt vmcnt(16)" : "+v"(inA.x), "+v"(inA.st[0]), "+v"(inA.st[1]), "+v"(inB.x), "+v"(inB.st[0]), "+v"(inB.st[1])::"memory");
  conv_norm<LAYER>(c, 0, inA, dsk, baseA);
  if (LAYER == 0) conv_norm<LAYER>(c, 1, inB, dsk, baseB);
  SYNC_LDS();
  STAMP(5 + LAYER * 20);
  f32x4 acc[4];
  conv_tri<0, true, 0, 0>(c, pa, pb, Ac, acc);
  conv_finish(c, LAYER, 0, acc, baseA);
  STAMP(6 + LAYER * 20);
  conv_cross<16, LAYER == 0 ? 12 : 0>(c, pa, pb, acc);
#pragma unroll
  for (int j = 0; j < 4; ++j) Ac[j] = lds16(pa + (7 - j) * 512);
  vm_wait<LAYER == 0 ? 12 : 0>();
  BARRIER();
  flag_all(c, E_CONV + 4 * LAYER);
  STAMP(7 + LAYER * 20);
  if (LAYER == 1) {
    wait_edge<false>(c, E_DOWN + 1);
    conv_load<LAYER>(c, 1, inB);
    conv_norm<LAYER>(c, 1, inB, dsk, baseB);
    SYNC_LDS();
  }
  STAMP(8 + LAYER * 20);
  conv_tri<4, false, 28, LAYER == 0 ? 8 : 0>(c, pa, pb, Ac, acc);
  conv_finish(c, LAYER, 1, acc, baseB);
  if constexpr (LAYER == 0) sfor<12>([&](auto N) { ld_wgu<36 + N>(c); });
  vm_wait<LAYER == 0 ? 12 : 0>();
  BARRIER();
  flag_all(c, E_CONV + 4 * LAYER + 1);
  STAMP(9 + LAYER * 20);
}

struct G1 {
  const char* xa;
  const char* res;
  LAS char* ring;
  u32 ldsw;
  u32 v16;
  int layer;
};
template <int HALF, bool STATS, int S>
DEV void g1_step(const Ctx& c, const G1& g, u32x4 (&WD)[NWDV], f32x4 (&acc)[3][4], u32x4 (&Bc)[4], u32x4& fl) {
  if constexpr (HALF == 0) {
    if constexpr (S == 3) { vm_wait<15>(); BARRIER(); FENCE(); }
    if constexpr (S == 7) { vm_wait<23>(); BARRIER(); FENCE(); }
    if constexpr (S == 11) { vm_wait<0>(); check_edge<false>(c, E_CONV + 4 * g.layer + 1, fl); BARRIER(); FENCE(); }
  } else {
    if constexpr (S == 3) { vm_wait<9>(); BARRIER(); FENCE(); if constexpr (STATS) flag_grp(c, 2 * g.layer); }
  }
  u32x4 Bn[4];
  sfor<12>([&](auto M) {
    constexpr int m = M, ct = m / 3, k = m % 3;
    if constexpr (S < 15 && k < 2) {
      constexpr int slot = (HALF * 4 + (S + 1) / 4) % 5;
      const u32x2 r = tr_read(g.ring + slot * 16384 + ct * 4096 + ((S + 1) & 3) * 1024 + k * 512);
      if constexpr (k == 0) { Bn[ct].x = r.x; Bn[ct].y = r.y; } else { Bn[ct].z = r.x; Bn[ct].w = r.y; }
    }
    if constexpr (k == 2) {
      if constexpr (HALF == 0) {
        if constexpr (S >= 1 && S <= 8 && ct < 3) ld_wd<3 * (S - 1) + ct>(c, WD);
        if constexpr (S == 9 && ct == 0) fl = peek_all(c, E_CONV + 4 * g.layer + 1);
        if constexpr (S >= 11 && S <= 13) dma1<ct * 1024>(g.xa + 1048576 + (S - 11) * 4096, g.v16, g.ldsw + ((S - 11 + 4) % 5) * 16384);
        if constexpr (S == 14 && ct == 0) dma1<0>(g.res + 1048576, g.v16, c.lds0 + L_RES + 4096 + c.w * 1024);
      } else {
        if constexpr (S <= 6 && ct < 3 && 24 + 3 * S + ct < KS2) ld_wd<24 + 3 * S + ct>(c, WD);
        if constexpr (S == 13 && ct == 0) fl = peek_grp(c, 2 * g.layer);
      }
    }
    if constexpr (STATS && k == 2) mm_vv<S == 0>(acc[k][ct], Bc[ct], Bc[ct]);
    else mm_av<S == 0, AW(k, S)>(acc[k][ct], Bc[ct]);
    PIN();
  });
  if constexpr (S < 15) {
#pragma unroll
    for (int ct = 0; ct < 4; ++ct) Bc[ct] = Bn[ct];
    g1_step<HALF, STATS, S + 1>(c, g, WD, acc, Bc, fl);
  } else {
    MM_DONE4(acc[2]); asm volatile("" : "+v"(acc[0][3]), "+v"(acc[1][3]), "+v"(acc[0][2]), "+v"(acc[1][2]), "+v"(acc[0][1]), "+v"(acc[1][1]));
  }
}
template <int HALF>
DEV void g1_main(const Ctx& c, const G1& g, u32x4 (&WD)[NWDV], f32x4 (&acc)[3][4], u32x4 (&Bc)[4], u32x4& fl) {
  if (c.w == 3) {
    g1_step<HALF, true, 0>(c, g, WD, acc, Bc, fl);
    const int j = c.l15 & 3;
    const bool hasq = c.q == (c.l15 >> 2);
#pragma unroll
    for (int ct = 0; ct < 4; ++ct) {
      const float s01 = (j & 1) ? acc[2][ct][1] : acc[2][ct][0], s23 = (j & 1) ? acc[2][ct][3] : acc[2][ct][2];
      const float sq = (j & 2) ? s23 : s01;
      LAS char* d = c.lds + L_ST + (ct * 16 + c.l15) * 8;
      *(LAS float*)(c.q == 3 ? d : c.lds + L_ST + 1024 + c.lane * 4) = acc[1][ct][0];
      *(LAS float*)(hasq ? d + 4 : c.lds + L_ST + 1024 + c.lane * 4) = sq;
    }
  } else {
    g1_step<HALF, false, 0>(c, g, WD, acc, Bc, fl);
  }
}
template <int HALF>
DEV void g1_epilogue(const Ctx& c, const G1& g, const f32x4 (&acc)[3][4], const f32x4 (&wsum)[3], const char* hsrc) {
  float mu[4], c1[4], c2[4], vr[4];
#pragma unroll
  for (int ct = 0; ct < 4; ++ct) {
    const f32x2 s = *(LAS f32x2*)(c.lds + L_ST + (ct * 16 + c.l15) * 8);
    mu[ct] = s.x * (-1.f / DM);
    vr[ct] = fmaxf(__builtin_fmaf(-mu[ct], mu[ct], s.y * (1.f / DM)), 0.f) + 1e-5f;
    const float r = __builtin_amdgcn_rsqf(vr[ct]);
    c1[ct] = (-2.f * 0.7978845608f * 1.44269504f) * r;
    c2[ct] = (-2.f * 0.7978845608f * 1.44269504f * 0.044715f) * r * r * r;
  }
#pragma unroll
  for (int i = 0; i < 3; ++i) {
    if constexpr (HALF == 1) {
      if (i == 0) dma4(hsrc, g.v16, g.ldsw);
      dma4(hsrc + (i + 1) * 8192, g.v16, g.ldsw + (i + 1) * 16384);
    }
    if (i == 2 && c.w == 3) break;
    u32 hv[4];
#pragma unroll
    for (int ct = 0; ct < 4; ++ct) {
      const f32x2 m2 = {mu[ct], mu[ct]};
      const f32x2 gg = __builtin_elementwise_fma(m2, wsum[i].xy, acc[i][ct].xy);
      const f32x2 uu = __builtin_elementwise_fma(m2, wsum[i].zw, acc[i][ct].zw);
      const f32x2 pl = __builtin_elementwise_fma(gg * gg, f32x2{c2[ct], c2[ct]}, f32x2{c1[ct], c1[ct]});
      const f32x2 ar = gg * pl;
      const f32x2 ex = {__builtin_amdgcn_exp2f(ar.x), __builtin_amdgcn_exp2f(ar.y)};
      const f32x2 v2 = {vr[ct], vr[ct]};
      const f32x2 dn = __builtin_elementwise_fma(ex, v2, v2);
      const f32x2 h = (gg * uu) * f32x2{__builtin_amdgcn_rcpf(dn.x), __builtin_amdgcn_rcpf(dn.y)};
      hv[ct] = pk_bf16(h.x, h.y);
    }
    swap32(hv[0], hv[2]); swap32(hv[1], hv[3]);
    swap16(hv[0], hv[1]); swap16(hv[2], hv[3]);
    const u32x4 o = {hv[0], hv[1], hv[2], hv[3]};
    const int U = c.cb * 11 + 3 * c.w + i;
    st16(c.rs, c.q * (KS2 * 1024) + c.l15 * 16, OFF_H + g.layer * H_BYTES + ((HALF * 64 + c.rb * 4) * KS2 + (U >> 2)) * 1024 + (U & 3) * 256, o);
    FENCE();
  }
}

template <int HALF, int C>
DEV void g2_chunk(const Ctx& c, int layer, LAS char* rd, const char* hsrc, u32 v16, u32 ldsw, const u32x4 (&WD)[NWDV], f32x4 (&acc)[4], u32x4 (&Bc)[8]) {
  if constexpr (C < 5) {
    constexpr int n = C == 0 ? 8 : (C == 1 ? 8 : (C == 2 ? 6 : (C == 3 ? 2 : 0)));
    vm_wait<(HALF == 0 && C == 1) ? 4 : n>();
    LGKM0();
    BARRIER(); FENCE();
    if constexpr (HALF == 0 && C == 1) flag_grp(c, 2 * layer + 1);
    if constexpr (C == 0) dma4(hsrc + 4 * 8192, v16, ldsw + 4 * 16384);
    if constexpr (C == 1) {
      const char* s5 = hsrc + 5 * 8192 - (c.w >> 1) * 2048;
      dma1<0>(s5, v16, ldsw); dma1<1024>(s5, v16, ldsw);
    }
  }
  constexpr int NK = C < 5 ? 4 : 2, NKN = C < 4 ? 4 : 2;
  u32x4 Bn[8];
  sfor<NK * 4>([&](auto M) {
    constexpr int m = M, ks = m >> 2, ct = (m >> 1) & 1, dt = m & 1, n = (C * 4 + ks) * 2 + dt;
    if constexpr (C < 5 && m < NKN * 2) Bn[m] = lds16(rd + ((C + 1) % 5) * 16384 + (m & 1) * 4096 + (m >> 1) * 1024);
    if constexpr (n < 16) mm_av<C == 0 && ks == 0, 192 + 4 * n>(acc[dt * 2 + ct], Bc[ks * 2 + ct]);
    else mm_vv<false>(acc[dt * 2 + ct], WD[n - 16], Bc[ks * 2 + ct]);
    PIN();
  });
  if constexpr (C < 5) {
#pragma unroll
    for (int m = 0; m < NKN * 2; ++m) Bc[m] = Bn[m];
    g2_chunk<HALF, C + 1>(c, layer, rd, hsrc, v16, ldsw, WD, acc, Bc);
  }
}
template <int HALF>
DEV void g2_unit(const Ctx& c, const Params& p, int layer, const char* hsrc, const u32x4 (&WD)[NWDV]) {
  const u32 v16 = c.lane * 16, ldsw = c.lds0 + L_RING + c.w * 4096;
  const int kh = c.w >> 1, dfin = 2 * (c.w & 1) + kh;
  LAS char* rd = c.lds + L_RING + kh * 8192 + c.lane * 16;
  if constexpr (HALF == 1) {
#pragma unroll
    for (int i = 0; i < 4; ++i) dma4(hsrc + i * 8192, v16, ldsw + i * 16384);
  }
  u32x2 rres[2];
#pragma unroll
  for (int ct = 0; ct < 2; ++ct) rres[ct] = tr_read(c.lds + L_RES + HALF * 4096 + ct * 2048 + dfin * 512 + c.lane * 8);
  f32x4 acc[4];
  vm_wait<12>();
  BARRIER(); FENCE();
  u32x4 Bc[8];
#pragma unroll
  for (int m = 0; m < 8; ++m) Bc[m] = lds16(rd + (m & 1) * 4096 + (m >> 1) * 1024);
  g2_chunk<HALF, 0>(c, layer, rd, hsrc, v16, ldsw, WD, acc, Bc);
  MM_DONE4(acc);
  STAMP(16 + 2 * HALF + layer * 20);
  f32x4 o[2];
#pragma unroll
  for (int ct = 0; ct < 2; ++ct) {
    *(LAS f32x4*)(c.lds + L_RING + 2 * 16384 + c.w * 2048 + ct * 1024 + c.lane * 16) = kh ? acc[ct] : acc[2 + ct];
    o[ct] = (kh ? acc[2 + ct] : acc[ct]) + f32x4{bf_lo(rres[ct].x), bf_hi(rres[ct].x), bf_lo(rres[ct].y), bf_hi(rres[ct].y)};
  }
  SYNC_LDS();
  u32 v[4];
#pragma unroll
  for (int ct = 0; ct < 2; ++ct) {
    o[ct] += *(LAS f32x4*)(c.lds + L_RING + 2 * 16384 + (c.w ^ 2) * 2048 + ct * 1024 + c.lane * 16);
    v[2 * ct] = pk_bf16(o[ct][0], o[ct][1]); v[2 * ct + 1] = pk_bf16(o[ct][2], o[ct][3]);
  }
  if (layer == 0) {
    float st[4];
#pragma unroll
    for (int ct = 0; ct < 2; ++ct) {
      const float a = bf_lo(v[2 * ct]), bb = bf_hi(v[2 * ct]), d = bf_lo(v[2 * ct + 1]), e = bf_hi(v[2 * ct + 1]);
      st[2 * ct] = (a + bb) + (d + e);
      st[2 * ct + 1] = (a * a + bb * bb) + (d * d + e * e);
    }
    swap32f(st[0], st[2]); swap32f(st[1], st[3]);
    float sA = st[0] + st[2], sB = st[1] + st[3];
    swap16f(sA, sB);
    *(LAS float*)(c.lds + L_ST + c.tid * 4) = sA + sB;
    const bool o1 = (c.lane & 1) != 0, o2 = (c.lane & 2) != 0;
    u32 t0 = dpp_u<0xB1>(o1 ? v[0] : v[1]), t1 = dpp_u<0xB1>(o1 ? v[2] : v[3]);
    const u32 a0 = o1 ? t0 : v[0], a1 = o1 ? v[1] : t0, a2 = o1 ? t1 : v[2], a3 = o1 ? v[3] : t1;
    t0 = dpp_u<0x4E>(o2 ? a0 : a2); t1 = dpp_u<0x4E>(o2 ? a1 : a3);
    const u32x4 o4 = {o2 ? t0 : a0, o2 ? t1 : a1, o2 ? a2 : t0, o2 ? a3 : t1};
    st16(c.rs, (2 * c.q + (c.lane & 1)) * 8192 + (c.lane & 2) * 32 + (c.l15 >> 2) * 16,
         OFF_XP + 2097152 + (c.db * 32 + dfin * 8) * 8192 + HALF * 4096 + c.rt * 128, o4);
    SYNC_LDS();
    if (c.w == 0) {
      LAS float* s = (LAS float*)(c.lds + L_ST) + c.lane;
      const float sv = (s[0] + s[64]) + (s[128] + s[192]);
      const int t = c.rt * 32 + (c.q >> 1) * 16 + c.l15;
      __hip_atomic_fetch_add((GAS double*)(c.ws + OFF_ST3 + HALF * 16384 + (t & 3) * 4096 + (t >> 2) * 16 + (c.q & 1) * 8), (double)sv, RLX_AGENT);
    }
    vm_wait<0>();
    BARRIER();
    flag_all(c, E_DOWN + HALF);
  } else {
    swap16(v[0], v[2]); swap16(v[1], v[3]);
    const u32x4 o4 = {v[0], v[1], v[2], v[3]};
    *(u32x4*)(p.out + (size_t)(HALF * 1024 + c.rt * 32 + (c.q & 1) * 16 + c.l15) * DM + c.db * 64 + dfin * 16 + (c.q >> 1) * 8) = o4;
    if constexpr (HALF == 0) BARRIER();
  }
  STAMP(17 + 2 * HALF + layer * 20);
}

__global__ void __launch_bounds__(256, 1) mega_kernel(Params p) {
  __shared__ __attribute__((aligned(16))) char smem[LDS_BYTES];
  const int w0 = __builtin_amdgcn_readfirstlane(threadIdx.x >> 6);
  Ctx c;
  c.ws = p.ws; c.lds = (LAS char*)smem; c.lds0 = (u32)(size_t)c.lds;
  c.rs = mk_rsrc(p.ws, WS_BYTES);
  refresh(c, w0);
  c.tj = 0;
  STAMP(0);
  u32 dsk;
  u32 pf_sink;
  {
    unsigned long long pc;
    asm volatile("s_getpc_b64 %0" : "=s"(pc));
    const u32 off = ((c.b >> 3) * 16 + (c.tid & 15)) * 128;
    pf_sink = 0;
    if (c.tid < 16 && off < CODE_PREFETCH) pf_sink = *(const GAS u32*)((pc & ~127ull) + off);
  }

  {
    const int b = c.b, tid = c.tid, lane = c.lane, w = c.w, q = c.q, l15 = c.l15;
    LAS char* lds = c.lds;
    const int kch = w >> 1, khalf = w & 1;
    const rsrc_t rx = mk_rsrc(p.x0, SEQ * DM * 2), rg = mk_rsrc(p.wg, DM * DF * 2), ru = mk_rsrc(p.wu, DM * DF * 2), rd = mk_rsrc(p.wd, DM * DF * 2);
    const u32x4 r0 = ld16p(rx, lane * 16, (8 * b + 2 * w) * 1024), r1 = ld16p(rx, lane * 16 + 1024, (8 * b + 2 * w) * 1024);
    u32x4 xt[2];
#pragma unroll
    for (int i = 0; i < 2; ++i) xt[i] = ld16p(rx, (tid >> 3) * 1024 + (tid & 7) * 16 + i * 128, (b >> 2) * 32768 + (b & 3) * 256);
    const int dch = 2 * c.cp + kch, pn = dch * NS + 64 * khalf + lane;
    const u16 pA = p.logA[pn], pI = p.Aim[pn], pCr = p.Cre[pn], pCi = p.Cim[pn], pdt = p.logdt[dch];
    dsk = *(const u32*)(p.Dskip + 2 * c.cp);
    const u32 ep = __hip_atomic_load((GAS u32*)(p.ws + OFF_EPOCH), RLX_AGENT);
    const int cbR = b >> 4, sR = b & 15;
    u32x4 wg[2], wu[2];
    int wrow[2], wtau[2];
#pragma unroll
    for (int i = 0; i < 2; ++i) {
      const int id = tid + 256 * i;
      wrow[i] = (id * 5958) >> 16; wtau[i] = id - wrow[i] * 11;
      const u32 vo = wrow[i] * (DF * 2) + wtau[i] * 16 + (id < 352 ? 0u : 0x40000000u) + sR * (32 * DF * 2) + cbR * 172;
      wg[i] = ld16p(rg, vo, 0); wu[i] = ld16p(ru, vo, 0);
    }
    u32x4 dv[2];
    const int s2 = b >> 2, dq = b & 3;
#pragma unroll
    for (int i = 0; i < 2; ++i) {
      const int krow = (tid >> 4) + 16 * i, U = 4 * s2 + (krow >> 3), cbU = (U * 5958) >> 16, fl = 8 * (U - cbU * 11) + (krow & 7);
      dv[i] = ld16p(rd, (86 * cbU + fl) * 1024 + (tid & 15) * 16 + dq * 256 + (fl < 86 ? 0u : 0x40000000u), 0);
    }
    PIN();
    int ci[15];
#pragma unroll
    for (int i = 0; i < 15; ++i) {
      const int lag = 2047 - 8 * tid - i;
      const int bits = (int)(pk_bf16((float)lag, 0.f) & 0xffffu) - (134 << 7) + 128;
      ci[i] = 4 * (lag < 0 ? 768 : (lag < 128 ? lag : bits));
      asm volatile("" : "+v"(ci[i]));
    }
    PIN();
    c.epoch = ep + 1u;
    asm volatile("" :: "v"(pf_sink));
    {
      float s0 = 0.f, q0 = 0.f, s1 = 0.f, q1 = 0.f;
#pragma unroll
      for (int i = 0; i < 4; ++i) {
        float a = bf_lo(r0[i]), e = bf_hi(r0[i]);
        s0 += a + e; q0 += a * a + e * e;
        a = bf_lo(r1[i]); e = bf_hi(r1[i]);
        s1 += a + e; q1 += a * a + e * e;
      }
      s0 = wave_sum(s0); q0 = wave_sum(q0); s1 = wave_sum(s1); q1 = wave_sum(q1);
      const float m0 = s0 * (1.f / DM), m1 = s1 * (1.f / DM);
      const float v0 = fmaxf(q0 * (1.f / DM) - m0 * m0, 0.f), v1 = fmaxf(q1 * (1.f / DM) - m1 * m1, 0.f);
      const f32x4 o = {m0, __builtin_amdgcn_rsqf(v0 + 1e-5f), m1, __builtin_amdgcn_rsqf(v1 + 1e-5f)};
      const u32x4 z = {0u, 0u, 0u, 0u};
      const u32 vo = lane == 0 ? OFF_ST1 + (b >> 7) * 8192 + (w & 1) * 4096 + ((2 * b + (w >> 1)) & 255) * 16 : OFF_ST3 + b * 128 + w * 32 + (lane - 1) * 16;
      if (lane < 3) st16(c.rs, vo, 0, lane == 0 ? __builtin_bit_cast(u32x4, o) : z);
    }
#pragma unroll
    for (int i = 0; i < 2; ++i) *(LAS u32x4*)(lds + L_XT + (tid >> 3) * 272 + (tid & 7) * 16 + i * 128) = xt[i];
#pragma unroll
    for (int i = 0; i < 2; ++i) {
      const bool last = wtau[i] == 10;
      const u32x4 lo = {wg[i].x, wu[i].x, wg[i].y, wu[i].y}, hi = {wg[i].z, wu[i].z, last ? 0x00003F80u : wg[i].w, last ? 0u : wu[i].w};
      if (i == 0 || tid < 96) {
        *(LAS u32x4*)(lds + L_IMG1 + wtau[i] * 1056 + wrow[i] * 32) = lo;
        *(LAS u32x4*)(lds + L_IMG1 + wtau[i] * 1056 + wrow[i] * 32 + 16) = hi;
      }
    }
#pragma unroll
    for (int i = 0; i < 2; ++i) {
      const int krow = (tid >> 4) + 16 * i, c16 = tid & 15, kg = krow >> 3, j = krow & 7;
      *(LAS u32x4*)(lds + L_IMG2 + (c16 >> 1) * 1056 + (4 * kg + j + (j < 4 ? 0 : 12)) * 32 + (c16 & 1) * 16) = dv[i];
    }
    SYNC_LDS();
#pragma unroll
    for (int i = 0; i < 2; ++i) {
      LAS char* s = lds + L_XT + (tid & 7) * (4 * 272) + ((tid >> 3) + 32 * i) * 4;
      const u32x4 v = {*(LAS u32*)s, *(LAS u32*)(s + 272), *(LAS u32*)(s + 544), *(LAS u32*)(s + 816)};
      st16(c.rs, ((tid >> 3) + 32 * i) * 8192 + (tid & 7) * 16, OFF_XP + (b & 3) * (64 * 8192) + (b >> 2) * 128, v);
    }
#pragma unroll
    for (int i = 0; i < 3; ++i) {
      const int tau = 3 * w + i;
      if (tau < 11) st16(c.rs, lane * 16, OFF_WGU + ((cbR * 11 + tau) * 16 + sR) * 1024, tr_frag(lds + L_IMG1 + tau * 1056 + lane * 8));
    }
    if (b < 176) {
#pragma unroll
      for (int i = 0; i < 2; ++i) st16(c.rs, lane * 16, OFF_WD + ((dq * 8 + 2 * w + i) * KS2 + s2) * 1024, tr_frag(lds + L_IMG2 + (2 * w + i) * 1056 + lane * 8));
    }
    STAMP(1);

    const float dt = rnd_bf16(expf(bf2f(pdt)));
    const float a2 = -rnd_bf16(expf(bf2f(pA))) * dt * 1.44269504f;
    const float ai = bf2f(pI) * dt;
    const float i2p_hi = 0.15915494f, i2p_lo = (float)(0.15915494309189535 - (double)0.15915494f);
    const float fhi = ai * i2p_hi, flo = __builtin_fmaf(ai, i2p_hi, -fhi) + ai * i2p_lo;
    f32x2 wp[3];
#pragma unroll
    for (int i = 0; i < 3; ++i) {
      const float ps = i == 0 ? 1.f : (i == 1 ? 8.f : 128.f);
      const float mag = __builtin_amdgcn_exp2f(a2 * ps);
      const float rev = __builtin_fmaf(flo, ps, __builtin_amdgcn_fractf(fhi * ps));
      wp[i] = f32x2{mag * __builtin_amdgcn_cosf(rev), mag * __builtin_amdgcn_sinf(rev)};
    }
    const float cr = bf2f(pCr), cim = bf2f(pCi);
    int ce = 14 - __builtin_amdgcn_frexp_expf(wave_max(fmaxf(fabsf(cr), fabsf(cim))));
    ce = ce > 100 ? 100 : (ce < -100 ? -100 : ce);
    LAS char* tb = lds + w * TB_WAVE + lane * 4;
    auto put2 = [&](int ra, f32x2 va, int rb2, f32x2 vb) {
      const u32 ha = __builtin_bit_cast(u32, __builtin_amdgcn_cvt_pkrtz(va.x, va.y)), hb = __builtin_bit_cast(u32, __builtin_amdgcn_cvt_pkrtz(vb.x, vb.y));
      u32 la, lb;
      asm("v_fma_mixlo_f16 %0, %2, 1.0, -%4 op_sel_hi:[0,0,1]\n\t"
          "v_fma_mixlo_f16 %1, %5, 1.0, -%7 op_sel_hi:[0,0,1]\n\t"
          "v_fma_mixhi_f16 %0, %3, 1.0, -%4 op_sel:[0,0,1] op_sel_hi:[0,0,1]\n\t"
          "v_fma_mixhi_f16 %1, %6, 1.0, -%7 op_sel:[0,0,1] op_sel_hi:[0,0,1]"
          : "=&v"(la), "=&v"(lb) : "v"(va.x), "v"(va.y), "v"(ha), "v"(vb.x), "v"(vb.y), "v"(hb));
      *(LAS u32*)(tb + ra * TB_ROW) = ha; *(LAS u32*)(tb + TB_HALF + ra * TB_ROW) = la;
      *(LAS u32*)(tb + rb2 * TB_ROW) = hb; *(LAS u32*)(tb + TB_HALF + rb2 * TB_ROW) = lb;
    };
    {
      const f32x2 w8c = {wp[1].x, -wp[1].y}, w16c = cmul(w8c, w8c);
      f32x2 y0 = {ldexpf(cr, ce), -ldexpf(cim, ce)}, y1 = cmul(y0, w8c);
#pragma unroll
      for (int i = 0; i < 8; ++i) {
        put2(41 + 2 * i, y0, 42 + 2 * i, y1);
        if (i < 7) { y0 = cmul(y0, w16c); y1 = cmul(y1, w16c); }
      }
    }
    vm_wait<0>();
    BARRIER();
    flag_all(c, E_P0);
    STAMP(2);
    {
      f32x2 e[8];
      e[1] = wp[0]; e[2] = cmul(e[1], e[1]); e[3] = cmul(e[2], e[1]); e[4] = cmul(e[2], e[2]);
      e[5] = cmul(e[4], e[1]); e[6] = cmul(e[4], e[2]); e[7] = cmul(e[4], e[3]);
      const f32x2 w256 = cmul(wp[2], wp[2]);
      f32x2 ta = {16384.f, 0.f}, tc = wp[2] * 16384.f;
      put2(0, ta, 8, tc);
#pragma unroll
      for (int i = 1; i < 8; ++i) put2(i, e[i] * 16384.f, 8 + i, cmul(tc, e[i]));
      ta = w256 * 16384.f; tc = cmul(tc, w256);
      put2(16, ta, 20, tc);
#pragma unroll
      for (int i = 1; i < 4; ++i) put2(16 + i, cmul(ta, e[2 * i]), 20 + i, cmul(tc, e[2 * i]));
#pragma unroll
      for (int i = 0; i < 2; ++i) {
        ta = cmul(ta, w256); tc = cmul(tc, w256);
        put2(24 + 4 * i, ta, 26 + 4 * i, tc);
        put2(25 + 4 * i, cmul(ta, e[4]), 27 + 4 * i, cmul(tc, e[4]));
      }
#pragma unroll
      for (int i = 0; i < 4; ++i) {
        ta = cmul(ta, w256); tc = cmul(tc, w256);
        put2(32 + 2 * i, ta, 33 + 2 * i, tc);
      }
      ta = cmul(ta, w256);
      put2(40, ta, 40, ta);
    }
    FENCE();
    f32x4 kk[3];
#pragma unroll
    for (int i = 0; i < 3; ++i) kk[i] = f32x4{0.f, 0.f, 0.f, 0.f};
    {
      LAS char* tr = lds + w * TB_WAVE + l15 * TB_ROW + q * 16;
#pragma unroll
      for (int G = 0; G < 4; ++G) {
        const u32x4 bh = lds16(tr + 41 * TB_ROW + G * 64), bl = lds16(tr + TB_HALF + 41 * TB_ROW + G * 64);
#pragma unroll
        for (int i = 0; i < 3; ++i) {
          const u32x4 ah = lds16(tr + i * 16 * TB_ROW + G * 64), al = lds16(tr + TB_HALF + i * 16 * TB_ROW + G * 64);
          kk[i] = mfma_f16(al, bh, kk[i]);
          kk[i] = mfma_f16(ah, bl, kk[i]);
          kk[i] = mfma_f16(ah, bh, kk[i]);
        }
      }
      const float ks = ldexpf(2.f * dt, -14 - ce);
#pragma unroll
      for (int i = 0; i < 3; ++i) kk[i] *= ks;
    }
    STAMP(40);
    LAS char* kx = lds + L_KX + kch * 3072 + lane * 16;
    if (khalf) {
#pragma unroll
      for (int i = 0; i < 3; ++i) *(LAS f32x4*)(kx + i * 1024) = kk[i];
    }
    SYNC_LDS();
    if (!khalf) {
#pragma unroll
      for (int i = 0; i < 3; ++i) kk[i] += *(LAS f32x4*)(kx + i * 1024);
      LAS float* T = (LAS float*)(lds + L_KT + kch * KT_CH);
      const int i0 = 128 * (q >> 1) + 4 * (q & 1) + 8 * l15;
      *(LAS f32x4*)(T + i0) = kk[0];
      if (q < 2) {
        *(LAS f32x4*)(T + 256 + 64 * q + 4 * l15) = kk[1];
      } else {
        *(LAS f32x2*)(T + 384 + 64 * (q - 2) + 2 * l15) = kk[1].xy;
        *(LAS f32x2*)(T + 416 + 64 * (q - 2) + 2 * l15) = kk[1].zw;
      }
#pragma unroll
      for (int j = 0; j < 4; ++j) T[512 + 64 * q + 16 * j + l15] = kk[2][j];
      if (lane == 0) T[768] = 0.f;
    }
    SYNC_LDS();
    STAMP(41);
#pragma unroll
    for (int ch = 0; ch < 2; ++ch) {
      float V[15];
#pragma unroll
      for (int i = 0; i < 15; ++i) V[i] = *(LAS float*)(lds + L_KT + ch * KT_CH + ci[i]);
      u32 pkd[14];
#pragma unroll
      for (int i = 0; i < 14; ++i) pkd[i] = pk_bf16(V[i], V[i + 1]);
#pragma unroll
      for (int s = 0; s < 8; ++s) {
        const u32x4 o = {pkd[7 - s], pkd[9 - s], pkd[11 - s], pkd[13 - s]};
        *(LAS u32x4*)(lds + L_KC + ch * KC_CH + (s & 3) * KC_COPY + (s >> 2) * KC_HALF + tid * 16) = o;
      }
    }
    if (tid < 16) {
      const u32x4 z = {0u, 0u, 0u, 0u};
      *(LAS u32x4*)(lds + L_KC + (tid >> 3) * KC_CH + (tid & 3) * KC_COPY + ((tid >> 2) & 1) * KC_HALF + 4096) = z;
    }
  }
  STAMP(3);

  conv_phase<0>(c, w0, dsk);
  vm_wait<0>();

#pragma unroll 1
  for (int layer = 0; layer < 2; ++layer) {
    refresh(c, w0);
    G1 g;
    g.layer = layer;
    g.xa = c.ws + OFF_XC + layer * 2097152 + (size_t)(c.rb * 4 + c.w) * 16384;
    g.res = c.ws + OFF_XC + layer * 2097152 + (size_t)(c.rt * 2 + (c.w >> 1)) * 16384 + c.db * 2048 + (c.w & 1) * 1024;
    g.ring = c.lds + L_RING + c.lane * 8;
    g.ldsw = c.lds0 + L_RING + c.w * 4096;
    g.v16 = c.lane * 16;
    wait_edge<false>(c, E_CONV + 4 * layer);
    STAMP(10 + layer * 20);
#pragma unroll
    for (int i = 0; i < 4; ++i) dma4(g.xa + i * 4096, g.v16, g.ldsw + i * 16384);
    dma1<0>(g.res, g.v16, c.lds0 + L_RES + c.w * 1024);
    PIN();
    f32x4 wsum[3];
    {
      u32x4 ones = {0x3F803F80u, 0x3F803F80u, 0x3F803F80u, 0x3F803F80u};
      asm volatile("s_nop 1" : "+v"(ones));
      sfor<48>([&](auto N) { mm_av<(N < 3), AW(N % 3, N / 3)>(wsum[N % 3], ones); });
      asm volatile("s_nop 7" : "+v"(wsum[0]), "+v"(wsum[1]), "+v"(wsum[2]));
    }
    PIN();
    u32x4 WD[NWDV], Bc[4], fl;
    f32x4 acc[3][4];
    vm_wait<13>();
    BARRIER(); FENCE();
#pragma unroll
    for (int ct = 0; ct < 4; ++ct) Bc[ct] = tr_frag(g.ring + ct * 4096);
    STAMP(11 + layer * 20);
    g1_main<0>(c, g, WD, acc, Bc, fl);
    vm_wait<9>();
    SYNC_LDS();
    STAMP(12 + layer * 20);
    dma4(g.xa + 1048576 + 3 * 4096, g.v16, g.ldsw + 2 * 16384);
    g1_epilogue<0>(c, g, acc, wsum, nullptr);
#pragma unroll
    for (int ct = 0; ct < 4; ++ct) Bc[ct] = tr_frag(g.ring + 4 * 16384 + ct * 4096);
    STAMP(13 + layer * 20);
    g1_main<1>(c, g, WD, acc, Bc, fl);
    check_edge<true>(c, 2 * layer, fl);
    SYNC_LDS();
    STAMP(14 + layer * 20);
    refresh(c, w0);
    const char* hsrc = c.ws + OFF_H + layer * H_BYTES + (size_t)(c.rt * 2 + (c.w & 1)) * (KS2 * 1024) + (c.w >> 1) * 4096;
    g1_epilogue<1>(c, g, acc, wsum, hsrc);
    STAMP(15 + layer * 20);
    g2_unit<0>(c, p, layer, hsrc, WD);
    wait_edge<true>(c, 2 * layer + 1);
    g2_unit<1>(c, p, layer, hsrc + (size_t)64 * KS2 * 1024, WD);
    if (layer == 0) conv_phase<1>(c, w0, dsk);
  }
  if (blockIdx.x == 5) { const unsigned long long te = wall_clock64() + 4 * (c.tj - c.t0); while (wall_clock64() < te) __builtin_amdgcn_s_sleep(2); }
}

}

extern "C" void kernel_launch(void* const* d_in, const int* in_sizes, int n_in, void* d_out, int out_size, void* d_ws,
                              size_t ws_size, hipStream_t stream) {
  Params p;
  p.x0 = (const u16*)d_in[0];
  p.logA = (const u16*)d_in[1];
  p.Aim = (const u16*)d_in[2];
  p.Cre = (const u16*)d_in[3];
  p.Cim = (const u16*)d_in[4];
  p.logdt = (const u16*)d_in[5];
  p.Dskip = (const u16*)d_in[6];
  p.wg = (const u16*)d_in[8];
  p.wu = (const u16*)d_in[9];
  p.wd = (const u16*)d_in[10];
  p.out = (u16*)d_out;
  p.ws = (char*)d_ws;
  mega_kernel<<<NBLK, 256, 0, stream>>>(p);
}
```

```cpp
#include <hip/hip_runtime.h>
#include <hip/hip_bf16.h>
#include <utility>

typedef unsigned int u32;
typedef unsigned short u16;
typedef unsigned long long u64;
typedef u32 u32x4 __attribute__((ext_vector_type(4)));
typedef u32 u32x2 __attribute__((ext_vector_type(2)));
typedef float f32x4 __attribute__((ext_vector_type(4)));
typedef float f32x2 __attribute__((ext_vector_type(2)));
typedef short s16x4 __attribute__((ext_vector_type(4)));
typedef __bf16 bf16x8 __attribute__((ext_vector_type(8)));
typedef __bf16 bf16x2 __attribute__((ext_vector_type(2)));
typedef _Float16 f16x8 __attribute__((ext_vector_type(8)));
typedef _Float16 f16x2 __attribute__((ext_vector_type(2)));
#define LAS __attribute__((address_space(3)))
#define GAS __attribute__((address_space(1)))
#define RLX_AGENT __ATOMIC_RELAXED, __HIP_MEMORY_SCOPE_AGENT
#define DEV __device__ __forceinline__

#define CODE_PREFETCH 1
#define MAP_ROWS 0
#define H_REFETCH 0
#define STAMP_WAVE 0

namespace {

constexpr int SEQ = 2048, DM = 512, NS = 128, DF = 1376;
constexpr int NBLK = 256;
constexpr int KS2 = 44;

constexpr u32 OFF_EPOCH = 0;
constexpr u32 OFF_FLAG = 4096;
constexpr u32 OFF_GFLAG = OFF_FLAG + 8 * 8192;
constexpr u32 OFF_ST1 = OFF_GFLAG + 8192;
constexpr u32 OFF_ST3 = OFF_ST1 + 16384;
constexpr u32 OFF_WGU = OFF_ST3 + 32768;
constexpr u32 OFF_WD = OFF_WGU + 16 * 11 * 16 * 1024;
constexpr u32 OFF_XC = OFF_WD + 32 * KS2 * 1024;
constexpr u32 OFF_XP = OFF_XC + 2 * 2097152;
constexpr u32 OFF_H = OFF_XP + 2 * 2097152;
constexpr u32 H_BYTES = 128 * KS2 * 1024;
constexpr u32 OFF_DBG = OFF_H + 2 * H_BYTES;
constexpr u32 WS_BYTES = OFF_DBG + 256 * 64 * 16;
constexpr int E_P0 = 0, E_W = 7, E_CONV = 1  , E_DOWN = 3  ;

constexpr int KC_COPY = 4160, KC_CH = 4 * KC_COPY;
constexpr int L_KC = 0;
constexpr int L_RING = 2 * KC_CH;
constexpr int L_RES = L_RING + 7 * 16384;
constexpr int L_ST = L_RES + 8192;
constexpr int L_WS = L_ST + 2048;
constexpr int L_VD = L_WS + 768;
constexpr int LDS_BYTES = 163840;
constexpr int L_U = L_RING + 5 * 16384;
constexpr int U_CH = 4608;
constexpr int L_P = L_RING + 6 * 16384;
constexpr int TB_ROW = 272, TB_WAVE = 57 * TB_ROW;
constexpr int L_IMG1 = 4 * TB_WAVE;
constexpr int L_IMG2 = L_IMG1 + 11 * 1056;
constexpr int L_XT = L_IMG2 + 8 * 1056;
constexpr int L_KX = L_XT + 32 * 272;
constexpr int L_KT = L_KX + 6144 + 16;
constexpr int KT_CH = 772 * 4;
static_assert(L_KT + 2 * KT_CH <= L_U && L_VD + 16 <= LDS_BYTES, "lds");

struct Params {
  const u16 *x0, *logA, *Aim, *Cre, *Cim, *logdt, *Dskip, *wg, *wu, *wd;
  u16* out;
  char* ws;
};

DEV float bf_lo(u32 v) { return __builtin_bit_cast(float, v << 16); }
DEV float bf_hi(u32 v) { return __builtin_bit_cast(float, v & 0xffff0000u); }
DEV float bf2f(u16 v) { return __builtin_bit_cast(float, (u32)v << 16); }
DEV u32 pk_bf16(float lo, float hi) {
  f32x2 f = {lo, hi};
  return __builtin_bit_cast(u32, __builtin_convertvector(f, bf16x2));
}
DEV float rnd_bf16(float v) { return bf_lo(pk_bf16(v, 0.f)); }
DEV u32x2 tr_read(LAS char* p) {
  return __builtin_bit_cast(u32x2, __builtin_amdgcn_ds_read_tr16_b64_v4i16((LAS s16x4*)p));
}
DEV u32x4 tr_frag(LAS char* p) {
  u32x2 a = tr_read(p), b = tr_read(p + 512);
  u32x4 r = {a.x, a.y, b.x, b.y};
  return r;
}
DEV u32x4 lds16(LAS char* p) { return *(LAS u32x4*)p; }
template <int CTRL> DEV float dpp_f(float v) {
  return __builtin_bit_cast(float, __builtin_amdgcn_mov_dpp(__builtin_bit_cast(int, v), CTRL, 0xf, 0xf, true));
}
template <int CTRL> DEV u32 dpp_u(u32 v) { return (u32)__builtin_amdgcn_mov_dpp((int)v, CTRL, 0xf, 0xf, true); }
DEV void swap16(u32& a, u32& b) { auto r = __builtin_amdgcn_permlane16_swap(a, b, false, false); a = r[0]; b = r[1]; }
DEV void swap32(u32& a, u32& b) { auto r = __builtin_amdgcn_permlane32_swap(a, b, false, false); a = r[0]; b = r[1]; }
DEV void swap16f(float& a, float& b) { u32 x = __builtin_bit_cast(u32, a), y = __builtin_bit_cast(u32, b); swap16(x, y); a = __builtin_bit_cast(float, x); b = __builtin_bit_cast(float, y); }
DEV void swap32f(float& a, float& b) { u32 x = __builtin_bit_cast(u32, a), y = __builtin_bit_cast(u32, b); swap32(x, y); a = __builtin_bit_cast(float, x); b = __builtin_bit_cast(float, y); }
DEV float wave_sum(float v) {
  v += dpp_f<0xB1>(v);
  v += dpp_f<0x4E>(v);
  v += dpp_f<0x141>(v);
  v += dpp_f<0x140>(v);
  float a = v, b = v;
  swap16f(a, b); v = a + b;
  a = v; b = v;
  swap32f(a, b); v = a + b;
  return v;
}
DEV float wave_max(float v) {
  v = fmaxf(v, dpp_f<0xB1>(v)); v = fmaxf(v, dpp_f<0x4E>(v)); v = fmaxf(v, dpp_f<0x141>(v)); v = fmaxf(v, dpp_f<0x140>(v));
  float a = v, b = v;
  swap16f(a, b); v = fmaxf(a, b);
  a = v; b = v;
  swap32f(a, b); v = fmaxf(a, b);
  return v;
}
DEV float sel(u64 m, float a, float b) { float r; asm("v_cndmask_b32_e64 %0, %1, %2, %3" : "=v"(r) : "v"(a), "v"(b), "s"(m)); return r; }
DEV u32 selu(u64 m, u32 a, u32 b) { u32 r; asm("v_cndmask_b32_e64 %0, %1, %2, %3" : "=v"(r) : "v"(a), "v"(b), "s"(m)); return r; }
constexpr u64 M_B0 = 0xAAAAAAAAAAAAAAAAull, M_B1 = 0xCCCCCCCCCCCCCCCCull, M_B2 = 0xF0F0F0F0F0F0F0F0ull, M_B3 = 0xFF00FF00FF00FF00ull;
DEV float dot2(u32 a, u32 b, float acc) {
  return __builtin_amdgcn_fdot2_f32_bf16(__builtin_bit_cast(bf16x2, a), __builtin_bit_cast(bf16x2, b), acc, false);
}
DEV f32x2 cmul(f32x2 a, f32x2 b) {
  const f32x2 t = a.yy * f32x2{-b.y, b.x};
  return __builtin_elementwise_fma(a.xx, b, t);
}
#define ACLOB "a0","a1","a2","a3","a4","a5","a6","a7","a8","a9","a10","a11","a12","a13","a14","a15","a16","a17","a18","a19","a20","a21","a22","a23","a24","a25","a26","a27","a28","a29","a30","a31","a32","a33","a34","a35","a36","a37","a38","a39","a40","a41","a42","a43","a44","a45","a46","a47","a48","a49","a50","a51","a52","a53","a54","a55","a56","a57","a58","a59","a60","a61","a62","a63","a64","a65","a66","a67","a68","a69","a70","a71","a72","a73","a74","a75","a76","a77","a78","a79","a80","a81","a82","a83","a84","a85","a86","a87","a88","a89","a90","a91","a92","a93","a94","a95","a96","a97","a98","a99","a100","a101","a102","a103","a104","a105","a106","a107","a108","a109","a110","a111","a112","a113","a114","a115","a116","a117","a118","a119","a120","a121","a122","a123","a124","a125","a126","a127","a128","a129","a130","a131","a132","a133","a134","a135","a136","a137","a138","a139","a140","a141","a142","a143","a144","a145","a146","a147","a148","a149","a150","a151","a152","a153","a154","a155","a156","a157","a158","a159","a160","a161","a162","a163","a164","a165","a166","a167","a168","a169","a170","a171","a172","a173","a174","a175","a176","a177","a178","a179","a180","a181","a182","a183","a184","a185","a186","a187","a188","a189","a190","a191","a192","a193","a194","a195","a196","a197","a198","a199","a200","a201","a202","a203","a204","a205","a206","a207","a208","a209","a210","a211","a212","a213","a214","a215","a216","a217","a218","a219","a220","a221","a222","a223","a224","a225","a226","a227","a228","a229","a230","a231","a232","a233","a234","a235","a236","a237","a238","a239","a240","a241","a242","a243","a244","a245","a246","a247","a248","a249","a250","a251","a252","a253","a254","a255"
#define BARRIER() __builtin_amdgcn_s_barrier()
#define FENCE() asm volatile("" ::: "memory")
#define PIN() __builtin_amdgcn_sched_barrier(0)
#define LGKM0() asm volatile("s_waitcnt lgkmcnt(0)" ::: "memory", ACLOB)
#define SYNC_LDS() do { LGKM0(); BARRIER(); FENCE(); } while (0)
template <int N> DEV void vm_wait() { asm volatile("s_waitcnt vmcnt(%0)" ::"i"(N) : "memory", ACLOB); }

typedef __amdgpu_buffer_rsrc_t rsrc_t;
DEV rsrc_t mk_rsrc(const void* p, u32 bytes) { return __builtin_amdgcn_make_buffer_rsrc((void*)p, 0, bytes, 0x00020000); }
DEV u32x4 ld16(rsrc_t r, u32 voff, u32 soff) { return __builtin_amdgcn_raw_buffer_load_b128(r, voff, soff, 16); }
DEV u32x4 ld16p(rsrc_t r, u32 voff, u32 soff) { return __builtin_amdgcn_raw_buffer_load_b128(r, voff, soff, 0); }
template <int AUX = 16> DEV void st16(rsrc_t r, u32 voff, u32 soff, u32x4 v) {
  __builtin_amdgcn_raw_buffer_store_b128(v, r, voff, soff, AUX);
  asm volatile("s_nop 1" ::"v"(v) : "memory");
}

template <int... Is, class F> DEV void sfor_impl(std::integer_sequence<int, Is...>, F&& f) { (f(std::integral_constant<int, Is>{}), ...); }
template <int N, class F> DEV void sfor(F&& f) { sfor_impl(std::make_integer_sequence<int, N>{}, f); }
template <bool FIRST> DEV void mm_vv(f32x4& acc, const u32x4& a, const u32x4& b) {
  if constexpr (FIRST) asm volatile("v_mfma_f32_16x16x32_bf16 %0, %1, %2, 0" : "=&v"(acc) : "v"(a), "v"(b));
  else asm volatile("v_mfma_f32_16x16x32_bf16 %0, %1, %2, %0" : "+v"(acc) : "v"(a), "v"(b));
}
template <bool FIRST> DEV void mm_f16(f32x4& acc, const u32x4& a, const u32x4& b) {
  if constexpr (FIRST) asm volatile("v_mfma_f32_16x16x32_f16 %0, %1, %2, 0" : "=&v"(acc) : "v"(a), "v"(b));
  else asm volatile("v_mfma_f32_16x16x32_f16 %0, %1, %2, %0" : "+v"(acc) : "v"(a), "v"(b));
}
template <bool FIRST, int A> DEV void mm_av(f32x4& acc, const u32x4& b) {
  if constexpr (FIRST) asm volatile("v_mfma_f32_16x16x32_bf16 %0, a[%c2:%c3], %1, 0" : "=&v"(acc) : "v"(b), "i"(A), "i"(A + 3));
  else asm volatile("v_mfma_f32_16x16x32_bf16 %0, a[%c2:%c3], %1, %0" : "+v"(acc) : "v"(b), "i"(A), "i"(A + 3));
}
template <int A, int OFF> DEV void ld16_a(rsrc_t r, u32 voff, u32 soff) {
  asm volatile("buffer_load_dwordx4 a[%c3:%c4], %0, %1, %2 offen offset:%c5 sc1" ::"v"(voff), "s"(r), "s"(soff), "i"(A), "i"(A + 3), "i"(OFF) : "memory");
}
#define MM_DONE4(a) asm volatile("s_nop 7" : "+v"((a)[0]), "+v"((a)[1]), "+v"((a)[2]), "+v"((a)[3]))

template <int G, int L> DEV void dma1(rsrc_t r, u32 voff, u32 soff, u32 lds) {
  u32 t;
  asm volatile("s_add_u32 m0, %4, %6\n\ts_add_u32 %0, %3, %5\n\tbuffer_load_dwordx4 %1, %2, %0 offen sc1 lds"
               : "=&s"(t) : "v"(voff), "s"(r), "s"(soff), "s"(lds), "i"(G), "i"(L) : "memory", "scc");
}
template <int G, int L> DEV void dma4(rsrc_t r, u32 voff, u32 soff, u32 lds) {
  u32 t;
  asm volatile("s_add_u32 m0, %4, %6\n\ts_add_u32 %0, %3, %5\n\tbuffer_load_dwordx4 %1, %2, %0 offen sc1 lds\n\t"
               "buffer_load_dwordx4 %1, %2, %0 offen offset:1024 sc1 lds\n\tbuffer_load_dwordx4 %1, %2, %0 offen offset:2048 sc1 lds\n\t"
               "buffer_load_dwordx4 %1, %2, %0 offen offset:3072 sc1 lds"
               : "=&s"(t) : "v"(voff), "s"(r), "s"(soff), "s"(lds), "i"(G), "i"(L) : "memory", "scc");
}

constexpr u32 SPIN_CAP = 1u << 16;
constexpr int SYNC_WAVE = 2;

struct Ctx {
  char* ws;
  rsrc_t rs;
  LAS char* lds;
  u32 lds0, epoch;
  int b, tid, lane, w, q, l15, cp, cb, rb, db, rt;
  mutable u32 junk;
};

DEV void flag_all(const Ctx& c, int edge, bool on = true) {
  __builtin_amdgcn_raw_buffer_store_b32(c.epoch, c.rs, c.tid == 192 ? 0u : 0x7f000000u, on ? OFF_FLAG + edge * 4096 + (c.b >> 2) * 64 + (c.b & 3) * 4 : 0x7f000000u, 16);
  FENCE();
}
DEV void flag_grp(const Ctx& c, int edge) {
  __builtin_amdgcn_raw_buffer_store_b32(c.epoch, c.rs, c.tid == 192 ? 0u : 0x7f000000u, OFF_FLAG + (8 + edge) * 4096 + c.rb * 256 + (c.cb >> 2) * 64 + (c.cb & 3) * 4, 16);
  FENCE();
}
template <bool GRP, bool ANY = false> DEV void peek(const Ctx& c, int edge) {
  if constexpr (GRP) ld16_a<252, 0>(c.rs, (c.lane & 3) * 64, (ANY || c.w == SYNC_WAVE) ? OFF_FLAG + (8 + edge) * 4096 + (c.rt >> 1) * 256 : 0x7f000000u);
  else ld16_a<252, 0>(c.rs, c.lane * 64, (ANY || c.w == SYNC_WAVE) ? OFF_FLAG + edge * 4096 : 0x7f000000u);
}
template <int N> DEV u64 flags_miss(const Ctx& c) {
  u32 f0, f1, f2, f3;
  asm volatile("s_waitcnt vmcnt(%4)\n\tv_accvgpr_read_b32 %0, a252\n\tv_accvgpr_read_b32 %1, a253\n\tv_accvgpr_read_b32 %2, a254\n\tv_accvgpr_read_b32 %3, a255"
               : "=v"(f0), "=v"(f1), "=v"(f2), "=v"(f3) : "i"(N) : "memory");
  const u32 d = ((f0 ^ c.epoch) | (f1 ^ c.epoch)) | ((f2 ^ c.epoch) | (f3 ^ c.epoch));
  return __builtin_amdgcn_ballot_w64(d != 0u);
}
template <bool GRP> DEV void spin(const Ctx& c, int edge) {
#pragma unroll 1
  for (u32 sp = 0; sp < SPIN_CAP; ++sp) {
    peek<GRP, true>(c, edge);
    if (flags_miss<0>(c) == 0ull) break;
    __builtin_amdgcn_s_sleep(4);
  }
}
template <bool GRP, int N> DEV void check_edge(const Ctx& c, int edge) { if (flags_miss<N>(c)) spin<GRP>(c, edge); FENCE(); }
template <bool GRP, int N> DEV void poll_edge(const Ctx& c, int edge) {
  if (c.w == SYNC_WAVE) { if constexpr (N >= 0) check_edge<GRP, N>(c, edge); else spin<GRP>(c, edge); }
  FENCE();
}
template <int N> DEV void post_verdict(const Ctx& c, int k) {
  if (c.w == SYNC_WAVE) { const u64 m = flags_miss<N>(c); *(LAS u32*)(c.lds + L_VD + 4 * k) = (u32)m | (u32)(m >> 32); }
}
DEV u64 read_verdict(const Ctx& c, int k) { return (u64)(u32)__builtin_amdgcn_readfirstlane(*(LAS u32*)(c.lds + L_VD + 4 * k)); }

#define STAMP(i) do { } while (0)

DEV void refresh(Ctx& c, int w0) {
  int tid = threadIdx.x, b = blockIdx.x, w = w0;
  asm volatile("" : "+v"(tid)); asm volatile("" : "+s"(b)); asm volatile("" : "+s"(w));
  c.tid = tid; c.b = b; c.w = w; c.lane = tid & 63; c.q = c.lane >> 4; c.l15 = tid & 15;
  const int xcd = b & 7, rr = b >> 3;
  c.cp = xcd * 32 + rr;
  c.cb = (xcd & 3) * 4 + (rr & 3); c.rb = (xcd >> 2) * 8 + (rr >> 2);
  c.db = (xcd & 1) * 4 + (rr & 3); c.rt = (xcd >> 1) * 8 + (rr >> 2);
}

constexpr int AW(int tile, int s) { return (tile * 16 + s) * 4; }
template <int N> DEV void ld_wgu(const Ctx& c) {
  constexpr int tl = N % 3, s = N / 3;
  const u32 v = c.lane * 16 + ((tl == 2 && c.w == 3) ? 0x40000000u : 0u);
  ld16_a<AW(tl, s), (s & 3) * 1024>(c.rs, v, OFF_WGU + (c.cb * 11 + 3 * c.w + tl) * 16384 + (s >> 2) * 4096);
}
constexpr int NWDA = 15, NWDV = KS2 - NWDA;
template <int OFF> DEV void ld16_v(u32x4& d, rsrc_t r, u32 voff, u32 soff) {
  asm volatile("buffer_load_dwordx4 %0, %1, %2, %3 offen offset:%c4 sc1" : "=v"(d) : "v"(voff), "s"(r), "s"(soff), "i"(OFF) : "memory");
}
template <int N> DEV void ld_wd(const Ctx& c, u32 v16, u32 wd0, u32 wd1, u32x4 (&WD)[NWDV]) {
  constexpr int j = N >> 1;
  const u32 base = (N & 1) ? wd1 : wd0;
  if constexpr (N < NWDA) ld16_a<192 + 4 * N, (j & 3) * 1024>(c.rs, v16, base + (j >> 2) * 8192);
  else if constexpr (j < 20) ld16_v<(j & 3) * 1024>(WD[N - NWDA], c.rs, v16, base + (j >> 2) * 8192);
  else ld16_v<(j & 1) * 1024>(WD[N - NWDA], c.rs, v16, base + 40960 - (c.w >> 1) * 2048);
}

struct ConvIn { u32x4 x; u32x4 st[4]; };
template <int LAYER, int IFZ> DEV void conv_load(const Ctx& c, int half, ConvIn& in, u64 go) {
  const u32 v = c.tid * 16;
  if constexpr (LAYER == 0) {
    asm volatile("s_cmp_eq_u64 %8, 0\n\ts_cbranch_scc%c9 1f\n\t"
                 "buffer_load_dwordx4 %0, %3, %4, %5 offen sc1\n\tbuffer_load_dwordx4 %1, %3, %4, %6 offen sc1\n\tbuffer_load_dwordx4 %2, %3, %4, %7 offen sc1\n1:"
                 : "+v"(in.x), "+v"(in.st[0]), "+v"(in.st[1])
                 : "v"(v), "s"(c.rs), "s"(OFF_XP + c.cp * 8192 + half * 4096), "s"(OFF_ST1 + half * 8192), "s"(OFF_ST1 + half * 8192 + 4096), "s"(go), "i"(1 - IFZ)
                 : "memory", "scc");
  } else {
    const u32 so = OFF_ST3 + half * 16384;
    asm volatile("s_cmp_eq_u64 %12, 0\n\ts_cbranch_scc%c13 1f\n\t"
                 "buffer_load_dwordx4 %0, %5, %6, %7 offen sc1\n\tbuffer_load_dwordx4 %1, %5, %6, %8 offen sc1\n\tbuffer_load_dwordx4 %2, %5, %6, %9 offen sc1\n\t"
                 "buffer_load_dwordx4 %3, %5, %6, %10 offen sc1\n\tbuffer_load_dwordx4 %4, %5, %6, %11 offen sc1\n1:"
                 : "+v"(in.x), "+v"(in.st[0]), "+v"(in.st[1]), "+v"(in.st[2]), "+v"(in.st[3])
                 : "v"(v), "s"(c.rs), "s"(OFF_XP + 2097152 + c.cp * 8192 + half * 4096), "s"(so), "s"(so + 4096), "s"(so + 8192), "s"(so + 12288), "s"(go), "i"(1 - IFZ)
                 : "memory", "scc");
  }
}
DEV void conv_undef(ConvIn& in) { asm volatile("" : "=v"(in.x), "=v"(in.st[0]), "=v"(in.st[1]), "=v"(in.st[2]), "=v"(in.st[3])); }
template <int N> DEV void conv_wait(ConvIn& in) {
  asm volatile("s_waitcnt vmcnt(%5)" : "+v"(in.x), "+v"(in.st[0]), "+v"(in.st[1]), "+v"(in.st[2]), "+v"(in.st[3]) : "i"(N) : "memory");
}
template <int LAYER> DEV void conv_norm(const Ctx& c, int half, const ConvIn& in, u32 dsk, f32x4 (&base)[2]) {
  float sm[4], sq[4];
  if (LAYER == 0) {
#pragma unroll
    for (int i = 0; i < 2; ++i) {
      const f32x4 v = __builtin_bit_cast(f32x4, in.st[i]);
      sm[2 * i] = v.x; sq[2 * i] = v.y; sm[2 * i + 1] = v.z; sq[2 * i + 1] = v.w;
    }
  } else {
#pragma unroll
    for (int i = 0; i < 4; ++i) {
      const u32x2 lo = {in.st[i].x, in.st[i].y}, hi = {in.st[i].z, in.st[i].w};
      sm[i] = (float)__builtin_bit_cast(double, lo); sq[i] = (float)__builtin_bit_cast(double, hi);
    }
  }
  const f32x2 dd = {bf_lo(dsk), bf_hi(dsk)};
  f32x2 u[4];
#pragma unroll
  for (int i = 0; i < 4; ++i) {
    const float m = sm[i] * (-1.f / DM);
    const float r = __builtin_amdgcn_rsqf(fmaxf(__builtin_fmaf(-m, m, sq[i] * (1.f / DM)), 0.f) + 1e-5f);
    const f32x2 x = {bf_lo(in.x[i]), bf_hi(in.x[i])};
    u[i] = (x + m) * r;
    const f32x2 bs = __builtin_elementwise_fma(u[i], dd, x);
    base[0][i] = bs.x; base[1][i] = bs.y;
  }
  *(LAS u32x2*)(c.lds + L_U + 512 + half * 2048 + c.tid * 8) = u32x2{pk_bf16(u[0].x, u[1].x), pk_bf16(u[2].x, u[3].x)};
  *(LAS u32x2*)(c.lds + L_U + U_CH + 512 + half * 2048 + c.tid * 8) = u32x2{pk_bf16(u[0].y, u[1].y), pk_bf16(u[2].y, u[3].y)};
}
template <int H> DEV void lds8h(u32x4& d, LAS char* p) {
  const u32x2 r = *(LAS u32x2*)(p + 8 * H);
  if constexpr (H == 0) { d.x = r.x; d.y = r.y; } else { d.z = r.x; d.w = r.y; }
}
DEV void ldsA(u32x4& d, LAS char* p) { lds8h<0>(d, p); PIN(); lds8h<1>(d, p); PIN(); }
constexpr int tri_j(int m) { return m < 4 ? 0 : (m < 7 ? 1 : (m < 9 ? 2 : 3)); }
constexpr int tri_i(int m) { return m < 4 ? m : (m < 7 ? m - 3 : (m < 9 ? m - 5 : 3)); }
template <int B0, bool ZERO, class F>
DEV void conv_tri(LAS char* pa, LAS char* pb, u32x4 (&Ac)[4], f32x4 (&acc)[4], F&& side) {
  u32x4 Bc[4];
#pragma unroll
  for (int j = 0; j < 4; ++j) Bc[j] = lds16(pb + (B0 + j) * 512);
  PIN();
  sfor<4>([&](auto IT) {
    constexpr int it = IT;
    u32x4 An[4], Bn[4];
    sfor<10>([&](auto M) {
      constexpr int m = M, i = tri_i(m), j = tri_j(m);
      if constexpr (it < 3) {
        if constexpr (m < 8) lds8h<m & 1>(An[m >> 1], pa - 64 * (it + 1) + (7 - (m >> 1)) * 512);
        if constexpr (m == 0 || m == 5) { PIN(); Bn[m / 5] = lds16(pb - 64 * (it + 1) + (B0 + m / 5) * 512); }
        if constexpr (m >= 8) Bn[m - 6] = lds16(pb - 64 * (it + 1) + (B0 + m - 6) * 512);
      }
      side(std::integral_constant<int, it * 10 + m>{});
      mm_vv<ZERO && it == 0 && j == 0>(acc[i], Ac[i - j], Bc[j]);
      PIN();
    });
    if constexpr (it < 3) {
#pragma unroll
      for (int j = 0; j < 4; ++j) { Ac[j] = An[j]; Bc[j] = Bn[j]; }
    }
  });
  MM_DONE4(acc);
}
template <class F>
DEV void conv_cross(LAS char* pa, LAS char* pb, f32x4 (&acc)[4], u32x4 (&At)[4], F&& side) {
  u32x4 Ac[7], Bc[4];
  Bc[0] = lds16(pb);
#pragma unroll
  for (int k = 0; k < 4; ++k) ldsA(Ac[3 + k], pa + (3 - k) * 512);
#pragma unroll
  for (int k = 1; k < 4; ++k) { Bc[k] = lds16(pb + k * 512); ldsA(Ac[3 - k], pa + (3 + k) * 512); }
  sfor<4>([&](auto IT) {
    constexpr int it = IT;
    u32x4 An[7], Bn[4];
    sfor<16>([&](auto M) {
      constexpr int m = M, j = m >> 2, i = m & 3;
      if constexpr (it < 3) {
        if constexpr (m < 14) lds8h<m & 1>(An[6 - (m >> 1)], pa - 64 * (it + 1) + (m >> 1) * 512);
        if constexpr (m == 0 || m == 8) { PIN(); Bn[m / 8] = lds16(pb - 64 * (it + 1) + (m / 8) * 512); }
        if constexpr (m >= 14) Bn[m - 12] = lds16(pb - 64 * (it + 1) + (m - 12) * 512);
      } else if constexpr (m < 8) {
        lds8h<m & 1>(At[m >> 1], pa + (7 - (m >> 1)) * 512);
      }
      side(std::integral_constant<int, it * 16 + m>{});
      mm_vv<it == 0 && j == 0>(acc[i], Ac[3 + i - j], Bc[j]);
      PIN();
    });
    if constexpr (it < 3) {
#pragma unroll
      for (int k = 0; k < 7; ++k) Ac[k] = An[k];
#pragma unroll
      for (int k = 0; k < 4; ++k) Bc[k] = Bn[k];
    }
  });
}
DEV void conv_finish1(const Ctx& c, const f32x4 (&acc)[4]) {
  LAS char* pw = c.lds + L_P + c.w * 4096 + (c.q * 16 + (c.l15 ^ (c.q * 4))) * 16;
#pragma unroll
  for (int i = 0; i < 4; ++i) *(LAS f32x4*)(pw + i * 1024) = acc[i];
}
struct ConvP { f32x4 a[2], b[2]; };
DEV void conv_finish2(const Ctx& c, ConvP& r) {
  const int qq = c.tid & 3, ii = (c.tid >> 2) & 15;
  LAS char* pr = c.lds + L_P + c.w * 1024 + (qq * 16 + (ii ^ (qq * 4))) * 16;
#pragma unroll
  for (int ch = 0; ch < 2; ++ch) { r.a[ch] = *(LAS f32x4*)(pr + ch * 8192); r.b[ch] = *(LAS f32x4*)(pr + ch * 8192 + 4096); }
}
DEV void conv_finish3(const Ctx& c, int layer, int half, const ConvP& r, const f32x4 (&base)[2]) {
  u32x2 o[2];
#pragma unroll
  for (int ch = 0; ch < 2; ++ch) {
    const f32x4 t = (r.a[ch] + base[ch]) + r.b[ch];
    o[ch].x = pk_bf16(t.x, t.y); o[ch].y = pk_bf16(t.z, t.w);
  }
  const u32 r0 = dpp_u<0xB1>(selu(M_B0, o[1].x, o[0].x)), r1 = dpp_u<0xB1>(selu(M_B0, o[1].y, o[0].y));
  const u32x4 v = {selu(M_B0, o[0].x, r0), selu(M_B0, o[0].y, r1), selu(M_B0, r0, o[1].x), selu(M_B0, r1, o[1].y)};
  st16(c.rs, (c.tid >> 2) * 16384 + (c.tid & 1) * 32 + (c.tid & 2) * 8, OFF_XC + layer * 2097152 + half * 1048576 + c.cp * 64, v);
}
DEV u32 xa_ptr(const Ctx& c, int layer) { return OFF_XC + layer * 2097152 + (c.rb * 4 + c.w) * 16384; }
DEV u32 res_ptr(const Ctx& c, int layer) { return OFF_XC + layer * 2097152 + (c.rt * 2 + (c.w >> 1)) * 16384 + c.db * 2048 + (c.w & 1) * 1024; }

template <int LAYER> DEV void conv_phase(Ctx& c, int w0, u32 dsk, ConvIn& inA, ConvIn& inB) {
  refresh(c, w0);
  const int kch = c.w >> 1, khalf = c.w & 1;
  LAS char* pa = c.lds + L_KC + kch * KC_CH + (c.l15 & 3) * KC_COPY + 2 * (2044 - 16 * (8 * khalf + 1) - (c.l15 & 12) + 8 * c.q) - 7 * 512;
  LAS char* pb = c.lds + L_U + kch * U_CH + 2 * (256 + 16 * (c.l15 - 8 * khalf - 1) + 8 * c.q);
  const u32 xa = xa_ptr(c, LAYER);
  const u32 v16 = c.lane * 16, ldsw = c.lds0 + L_RING + c.w * 4096;
  f32x4 baseA[2], baseB[2];
  {
    u32 z = 0u;
    asm volatile("" : "+v"(z));
    *(LAS u32*)(c.lds + L_U + (c.tid >> 7) * U_CH + (c.tid & 127) * 4) = z;
  }
  u32x4 Ac[4];
  if constexpr (LAYER == 1) {
#pragma unroll
    for (int j = 0; j < 4; ++j) ldsA(Ac[j], pa + (7 - j) * 512);
  }
  conv_wait<LAYER == 0 ? 1 : 2>(inA);
  STAMP(4 + LAYER * 20);
  conv_norm<LAYER>(c, 0, inA, dsk, baseA);
  if constexpr (LAYER == 0) { conv_wait<1>(inB); conv_norm<LAYER>(c, 1, inB, dsk, baseB); poll_edge<false, 0>(c, E_W); }
  SYNC_LDS();
  if constexpr (LAYER == 0) {
#pragma unroll
    for (int j = 0; j < 4; ++j) ldsA(Ac[j], pa + (7 - j) * 512);
    sfor<2>([&](auto N) { ld_wgu<N>(c); });
  }
  STAMP(5 + LAYER * 20);
  f32x4 acc[4];
  ConvP pp;
  conv_tri<0, true>(pa, pb, Ac, acc, [&](auto K) {
    if constexpr (LAYER == 0 && K % 4 == 3) ld_wgu<2 + K / 4>(c);
    if constexpr (LAYER == 1 && K == 20) { vm_wait<0>(); BARRIER(); flag_all(c, E_DOWN + 1); }
  });
  conv_finish1(c, acc);
  if constexpr (LAYER == 0) sfor<2>([&](auto N) { ld_wgu<12 + N>(c); });
  SYNC_LDS();
  conv_finish2(c, pp);
  conv_finish3(c, LAYER, 0, pp, baseA);
  STAMP(6 + LAYER * 20);
  u64 miss = 0;
  conv_cross(pa, pb, acc, Ac, [&](auto K) {
    if constexpr (LAYER == 0) {
      if constexpr (K % 4 == 3) ld_wgu<14 + K / 4>(c);
      if constexpr (K == 36) { vm_wait<9>(); BARRIER(); flag_all(c, E_CONV); }
    } else {
      if constexpr (K == 4) peek<false>(c, E_DOWN + 1);
      if constexpr (K == 36) {
        post_verdict<0>(c, 1);
        vm_wait<0>(); SYNC_LDS();
        miss = read_verdict(c, 1);
        conv_load<1, 1>(c, 1, inB, miss);
        flag_all(c, E_CONV + 4);
      }
    }
  });
  STAMP(7 + LAYER * 20);
  if constexpr (LAYER == 1) {
    if (miss) { poll_edge<false, -1>(c, E_DOWN + 1); BARRIER(); FENCE(); }
    conv_load<1, 0>(c, 1, inB, miss);
    if (miss) vm_wait<0>();
    conv_wait<1>(inB);
    conv_norm<LAYER>(c, 1, inB, dsk, baseB);
    SYNC_LDS();
  }
  STAMP(8 + LAYER * 20);
  conv_tri<4, false>(pa, pb, Ac, acc, [&](auto K) {
    if constexpr (LAYER == 0) { if constexpr (K % 4 == 3) ld_wgu<30 + K / 4>(c); }
    else if constexpr (K == 2) peek<false>(c, E_CONV + 4);
  });
  conv_finish1(c, acc);
  if constexpr (LAYER == 0) sfor<2>([&](auto N) { ld_wgu<40 + N>(c); });
  else poll_edge<false, 0>(c, E_CONV + 4);
  SYNC_LDS();
  conv_finish2(c, pp);
  if constexpr (LAYER == 1) dma4<0, 0>(c.rs, v16, xa, ldsw);
  conv_finish3(c, LAYER, 1, pp, baseB);
  if constexpr (LAYER == 1) dma4<4096, 16384>(c.rs, v16, xa, ldsw);
  STAMP(9 + LAYER * 20);
  if constexpr (LAYER == 0) {
    peek<false>(c, E_CONV);
    sfor<6>([&](auto N) { ld_wgu<42 + N>(c); });
    f32x4 wsum[3];
    u32x4 ones = {0x3F803F80u, 0x3F803F80u, 0x3F803F80u, 0x3F803F80u};
    vm_wait<20>();
    asm volatile("s_nop 1" : "+v"(ones));
    sfor<24>([&](auto N) { mm_av<(N < 3), AW(N % 3, N / 3)>(wsum[N % 3], ones); });
    poll_edge<false, 6>(c, E_CONV);
    vm_wait<0>();
    BARRIER(); FENCE();
    dma4<0, 0>(c.rs, v16, xa, ldsw); dma4<4096, 16384>(c.rs, v16, xa, ldsw);
    flag_all(c, E_CONV + 1);
    sfor<24>([&](auto N) { mm_av<false, AW(N % 3, 8 + N / 3)>(wsum[N % 3], ones); });
    asm volatile("s_nop 7" : "+v"(wsum[0]), "+v"(wsum[1]), "+v"(wsum[2]));
    if (c.l15 == 0) {
#pragma unroll
      for (int i = 0; i < 3; ++i) *(LAS f32x4*)(c.lds + L_WS + c.w * 192 + i * 64 + c.q * 16) = wsum[i];
    }
  }
}

struct G1 {
  u32 xa;
  u32 res, ldsr;
  LAS char* ring[2];
  u32 ldsw;
  u32 v16;
  u32 wd0, wd1;
  u32 vst, sst;
  u32 hsrc, hsrc5;
  LAS char* pk;
  f32x4 wsum[3];
  int layer;
};
DEV void h_refetch(const Ctx& c, u32 vst, u32 sst) {
}
template <int HALF, bool STATS, int S>
DEV void g1_step(const Ctx& c, const G1& g, u32x4 (&WD)[NWDV], f32x4 (&acc)[3][4], u32x4 (&Bc)[4]) {
  if constexpr (S == 3) {
    vm_wait<4>(); BARRIER(); FENCE();
    if constexpr (STATS && HALF == 0) flag_all(c, E_CONV + 5, g.layer != 0);
    if constexpr (STATS && HALF == 1) flag_grp(c, 2 * g.layer);
    if constexpr (HALF == 1) h_refetch(c, g.vst, g.sst);
  }
  if constexpr (S == 7) { vm_wait<10>(); BARRIER(); FENCE(); }
  if constexpr (HALF == 0 && S == 11) { vm_wait<17>(); BARRIER(); FENCE(); }
  if constexpr (HALF == 1 && S == 10) { check_edge<true, 0>(c, 2 * g.layer); BARRIER(); FENCE(); }
  u32x4 Bn[4];
  sfor<12>([&](auto M) {
    constexpr int m = M, ct = m / 3, k = m % 3;
    if constexpr (S < 15 && k < 2) {
      constexpr int slot = (HALF * 4 + (S + 1) / 4) % 7;
      const u32x2 r = tr_read(g.ring[slot >> 2] + (slot & 3) * 16384 + ct * 4096 + ((S + 1) & 3) * 1024 + k * 512);
      if constexpr (k == 0) { Bn[ct].x = r.x; Bn[ct].y = r.y; } else { Bn[ct].z = r.x; Bn[ct].w = r.y; }
    }
    if constexpr (k == 2 && (ct & 1) == 0) {
      constexpr int n = 2 * (S - 1) + (ct >> 1);
      if constexpr (S >= 1 && S <= 4) dma1<HALF * 1048576 + 8192 + n * 1024, ((HALF * 4 + 2 + (n >> 2)) % 7) * 16384 + (n & 3) * 1024>(c.rs, g.v16, g.xa, g.ldsw);
      if constexpr (S == 5 && ct == 0) dma1<HALF * 1048576, HALF * 4096>(c.rs, g.v16, g.res, g.ldsr);
      if constexpr (HALF == 1 && S >= 10) dma1<((n - 18) >> 2) * 8192 + ((n - 18) & 3) * 1024, (((n - 18) >> 2) + 1) * 16384 + ((n - 18) & 3) * 1024>(c.rs, g.v16, g.hsrc, g.ldsw);
    }
    if constexpr (k == 2 && S >= 5 && S <= (HALF ? 7 : 15) && ct < 3 && 3 * (S - 5) + ct >= 1) ld_wd<HALF * 36 + 3 * (S - 5) + ct - 1>(c, g.v16, g.wd0, g.wd1, WD);
    if constexpr (HALF == 0 && S == 13 && m == 11) peek<false, true>(c, E_CONV + 4 * g.layer + 1);
    if constexpr (HALF == 1 && S == 7 && m == 11) peek<true, true>(c, 2 * g.layer);
    if constexpr (STATS && k == 2) mm_vv<S == 0>(acc[k][ct], Bc[ct], Bc[ct]);
    else mm_av<S == 0, AW(k, S)>(acc[k][ct], Bc[ct]);
    PIN();
  });
  if constexpr (S < 15) {
#pragma unroll
    for (int ct = 0; ct < 4; ++ct) Bc[ct] = Bn[ct];
    g1_step<HALF, STATS, S + 1>(c, g, WD, acc, Bc);
  } else {
    MM_DONE4(acc[2]); asm volatile("" : "+v"(acc[0][3]), "+v"(acc[1][3]), "+v"(acc[0][2]), "+v"(acc[1][2]), "+v"(acc[0][1]), "+v"(acc[1][1]));
  }
}
template <int HALF>
DEV void g1_main(const Ctx& c, const G1& g, u32x4 (&WD)[NWDV], f32x4 (&acc)[3][4], u32x4 (&Bc)[4]) {
  if (c.w == 3) {
    g1_step<HALF, true, 0>(c, g, WD, acc, Bc);
    float sm[4], sq[4];
#pragma unroll
    for (int ct = 0; ct < 4; ++ct) {
      sm[ct] = acc[1][ct][0];
      sq[ct] = sel(M_B1, sel(M_B0, acc[2][ct][0], acc[2][ct][1]), sel(M_B0, acc[2][ct][2], acc[2][ct][3]));
    }
    swap32f(sm[0], sm[2]); swap32f(sm[1], sm[3]); swap16f(sm[2], sm[3]);
    swap32f(sq[0], sq[2]); swap32f(sq[1], sq[3]); swap16f(sq[0], sq[1]); swap16f(sq[2], sq[3]);
    const float e2 = sel(M_B3, sel(M_B2, sq[0], sq[1]), sel(M_B2, sq[2], sq[3]));
    const float mu = sm[3] * (-1.f / DM);
    const float vr = fmaxf(__builtin_fmaf(-mu, mu, e2 * (1.f / DM)), 0.f) + 1e-5f;
    const float r = __builtin_amdgcn_rsqf(vr);
    const float c1 = (-2.f * 0.7978845608f * 1.44269504f) * r;
    *(LAS f32x4*)(c.lds + L_ST + g.v16) = f32x4{mu, vr, c1, c1 * 0.044715f * (r * r)};
  } else {
    g1_step<HALF, false, 0>(c, g, WD, acc, Bc);
  }
}
template <int HALF>
DEV void g1_epilogue(const Ctx& c, const G1& g, const f32x4 (&acc)[3][4], u32x4 (&WD)[NWDV]) {
  f32x4 k4[4];
  const f32x4 (&wsum)[3] = g.wsum;
  const u32 vst = g.vst, sst = g.sst + HALF * 64 * (KS2 * 1024);
  const u32 sst2 = c.w == 3 ? 0x7f000000u : sst;
  SYNC_LDS();
  STAMP(12 + 2 * HALF + g.layer * 20);
#pragma unroll
  for (int ct = 0; ct < 4; ++ct) k4[ct] = *(LAS f32x4*)(g.pk + ct * 256);
  auto stage = [&](auto N) {
    constexpr int n = N - 1;
    if constexpr (HALF == 0) {
      if constexpr (n < 0) check_edge<false, 6>(c, E_CONV + 4 * g.layer + 1);
      else if constexpr (n < 8) dma1<1048576 + n * 1024, (4 + (n >> 2)) * 16384 + (n & 3) * 1024>(c.rs, g.v16, g.xa, g.ldsw);
      else if constexpr (n >= 10 && n < 14) ld_wd<22 + n>(c, g.v16, g.wd0, g.wd1, WD);
    } else if constexpr (N % 3 != 1) {
      constexpr int j = (N / 3) * 2 + (N % 3 == 2);
      if constexpr (j < 8) dma1<(3 + (j >> 2)) * 8192 + (j & 3) * 1024, (4 + (j >> 2)) * 16384 + (j & 3) * 1024>(c.rs, g.v16, g.hsrc, g.ldsw);
      else if constexpr (j < 10) dma1<(j - 8) * 1024, 6 * 16384 + (j - 8) * 1024>(c.rs, g.v16, g.hsrc5, g.ldsw);
    }
    PIN();
  };
  sfor<3>([&](auto I) {
    constexpr int i = I;
    u32 hv[4];
    sfor<2>([&](auto P) {
      constexpr int pi = P;
      constexpr int n0 = 6 * i + 3 * pi - 1;
      f32x2 gg[2], gu[2], ex[2];
      if constexpr (n0 >= 0) stage(std::integral_constant<int, n0>{});
#pragma unroll
      for (int e = 0; e < 2; ++e) {
        const f32x4 k = k4[2 * pi + e];
        gg[e] = __builtin_elementwise_fma(k.xx, wsum[i].xy, acc[i][2 * pi + e].xy);
        const f32x2 ar = gg[e] * __builtin_elementwise_fma(gg[e] * gg[e], k.ww, k.zz);
        ex[e] = f32x2{__builtin_amdgcn_exp2f(ar.x), __builtin_amdgcn_exp2f(ar.y)};
      }
      PIN();
      stage(std::integral_constant<int, n0 + 1>{});
#pragma unroll
      for (int e = 0; e < 2; ++e) {
        const f32x4 k = k4[2 * pi + e];
        gu[e] = gg[e] * __builtin_elementwise_fma(k.xx, wsum[i].zw, acc[i][2 * pi + e].zw);
        const f32x2 dn = __builtin_elementwise_fma(ex[e], k.yy, k.yy);
        const f32x2 h = gu[e] * f32x2{__builtin_amdgcn_rcpf(dn.x), __builtin_amdgcn_rcpf(dn.y)};
        hv[2 * pi + e] = pk_bf16(h.x, h.y);
      }
      PIN();
      stage(std::integral_constant<int, n0 + 2>{});
    });
    swap32(hv[0], hv[2]); swap32(hv[1], hv[3]);
    swap16(hv[0], hv[1]); swap16(hv[2], hv[3]);
    st16(c.rs, vst + i * 256, i == 2 ? sst2 : sst, u32x4{hv[0], hv[1], hv[2], hv[3]});
    PIN();
  });
}

constexpr int HB_OFF = 64 * KS2 * 1024;
template <int HALF, int C>
DEV void g2_chunk(const Ctx& c, const G1& g, int layer, LAS char* const (&rd)[2], const u32x4 (&WD)[NWDV], f32x4 (&acc)[4],
                  u32x4 (&Bc)[8]) {
  if constexpr (HALF == 0) {
    if constexpr (C == 2) { vm_wait<0>(); BARRIER(); FENCE(); flag_grp(c, 2 * layer + 1); h_refetch(c, g.vst, g.sst + 64 * (KS2 * 1024)); }
  } else if constexpr (C < 5) {
    vm_wait<C == 0 ? 9 : (C == 1 ? 11 : (C == 2 ? 7 : (C == 3 ? 2 : 0)))>();
    BARRIER(); FENCE();
  }
  constexpr int NK = C < 5 ? 4 : 2, NKN = C < 4 ? 4 : 2;
  u32x4 Bn[8];
  sfor<NK * 4>([&](auto M) {
    constexpr int m = M, ks = m >> 2, ct = (m >> 1) & 1, dt = m & 1, n = (C * 4 + ks) * 2 + dt;
    if constexpr (C < 5 && m < NKN * 2) Bn[m] = lds16(rd[(C + 2) >> 2] + ((C + 2) & 3) * 16384 + (m & 1) * 4096 + (m >> 1) * 1024);
    if constexpr (HALF == 1 && C == 0 && m % 3 == 0) {
      if constexpr (m < 12) dma1<HB_OFF + 4 * 8192 + (m / 3) * 1024, 5 * 16384 + (m / 3) * 1024>(c.rs, g.v16, g.hsrc, g.ldsw);
      else dma1<HB_OFF + (m / 3 - 4) * 1024, 6 * 16384 + (m / 3 - 4) * 1024>(c.rs, g.v16, g.hsrc5, g.ldsw);
    }
    if constexpr (HALF == 1 && C == 4 && m == 4) peek<false>(c, E_DOWN);
    if constexpr (n < NWDA) mm_av<C == 0 && ks == 0, 192 + 4 * n>(acc[dt * 2 + ct], Bc[ks * 2 + ct]);
    else mm_vv<false>(acc[dt * 2 + ct], WD[n - NWDA], Bc[ks * 2 + ct]);
    PIN();
  });
  if constexpr (C < 5) {
#pragma unroll
    for (int m = 0; m < NKN * 2; ++m) Bc[m] = Bn[m];
    g2_chunk<HALF, C + 1>(c, g, layer, rd, WD, acc, Bc);
  }
}
template <int HALF>
DEV void g2_unit(const Ctx& c, const G1& g, const Params& p, int layer, u32x4 (&WD)[NWDV], ConvIn& inA) {
  const int kh = c.w >> 1, dfin = 2 * (c.w & 1) + kh;
  LAS char* rd0 = c.lds + L_RING + kh * 8192 + c.lane * 16;
  LAS char* rd1 = rd0 + 65536;
  asm volatile("" : "+v"(rd0), "+v"(rd1));
  LAS char* const rd[2] = {rd0, rd1};
  if constexpr (HALF == 1) {
    sfor<4>([&](auto I) { dma4<HB_OFF + I * 8192, (I + 1) * 16384>(c.rs, g.v16, g.hsrc, g.ldsw); });
  }
  u32x2 rres[2];
#pragma unroll
  for (int ct = 0; ct < 2; ++ct) rres[ct] = tr_read(c.lds + L_RES + HALF * 4096 + ct * 2048 + dfin * 512 + c.lane * 8);
  f32x4 acc[4];
  vm_wait<HALF == 0 ? 13 : 12>();
  BARRIER(); FENCE();
  if constexpr (HALF == 1) flag_all(c, E_DOWN, layer == 0);
  u32x4 Bc[8];
#pragma unroll
  for (int m = 0; m < 8; ++m) Bc[m] = lds16(rd0 + 16384 + (m & 1) * 4096 + (m >> 1) * 1024);
  g2_chunk<HALF, 0>(c, g, layer, rd, WD, acc, Bc);
  MM_DONE4(acc);
  STAMP(16 + 2 * HALF + layer * 20);
  LAS char* ex = c.lds + L_RING + HALF * 8192 + c.lane * 16;
  f32x4 o[2];
  if (kh) {
#pragma unroll
    for (int ct = 0; ct < 2; ++ct) { *(LAS f32x4*)(ex + c.w * 2048 + ct * 1024) = acc[ct]; o[ct] = acc[2 + ct]; }
  } else {
#pragma unroll
    for (int ct = 0; ct < 2; ++ct) { *(LAS f32x4*)(ex + c.w * 2048 + ct * 1024) = acc[2 + ct]; o[ct] = acc[ct]; }
  }
#pragma unroll
  for (int ct = 0; ct < 2; ++ct) o[ct] += f32x4{bf_lo(rres[ct].x), bf_hi(rres[ct].x), bf_lo(rres[ct].y), bf_hi(rres[ct].y)};
  if constexpr (HALF == 1) { if (layer == 0) poll_edge<false, 0>(c, E_DOWN); }
  SYNC_LDS();
  if constexpr (HALF == 1) conv_load<1, 1>(c, 0, inA, (u64)(u32)layer);
  else peek<true>(c, 2 * layer + 1);
  u32 v[4];
#pragma unroll
  for (int ct = 0; ct < 2; ++ct) {
    o[ct] += *(LAS f32x4*)(ex + (c.w ^ 2) * 2048 + ct * 1024);
    v[2 * ct] = pk_bf16(o[ct][0], o[ct][1]); v[2 * ct + 1] = pk_bf16(o[ct][2], o[ct][3]);
  }
  if (layer == 0) {
    float st[4];
#pragma unroll
    for (int ct = 0; ct < 2; ++ct) {
      st[2 * ct] = dot2(v[2 * ct], 0x3F803F80u, dot2(v[2 * ct + 1], 0x3F803F80u, 0.f));
      st[2 * ct + 1] = dot2(v[2 * ct], v[2 * ct], dot2(v[2 * ct + 1], v[2 * ct + 1], 0.f));
    }
    swap32f(st[0], st[2]); swap32f(st[1], st[3]);
    float sA = st[0] + st[2], sB = st[1] + st[3];
    swap16f(sA, sB);
    *(LAS float*)(c.lds + L_ST + 1024 + c.tid * 4) = sA + sB;
    u32 t0 = dpp_u<0xB1>(selu(M_B0, v[1], v[0])), t1 = dpp_u<0xB1>(selu(M_B0, v[3], v[2]));
    const u32 a0 = selu(M_B0, v[0], t0), a1 = selu(M_B0, t0, v[1]), a2 = selu(M_B0, v[2], t1), a3 = selu(M_B0, t1, v[3]);
    t0 = dpp_u<0x4E>(selu(M_B1, a2, a0)); t1 = dpp_u<0x4E>(selu(M_B1, a3, a1));
    const u32x4 o4 = {selu(M_B1, a0, t0), selu(M_B1, a1, t1), selu(M_B1, t0, a2), selu(M_B1, t1, a3)};
    st16(c.rs, (2 * c.q + (c.lane & 1)) * 8192 + (c.lane & 2) * 32 + (c.l15 >> 2) * 16,
         OFF_XP + 2097152 + (c.db * 32 + dfin * 8) * 8192 + HALF * 4096 + c.rt * 128, o4);
    SYNC_LDS();
    if (c.q == c.w) {
      LAS float* s = (LAS float*)(c.lds + L_ST + 1024) + c.lane;
      const float sv = (s[0] + s[64]) + (s[128] + s[192]);
      const int t = c.rt * 32 + (c.q >> 1) * 16 + c.l15;
      __hip_atomic_fetch_add((GAS double*)(c.ws + OFF_ST3 + HALF * 16384 + (t & 3) * 4096 + (t >> 2) * 16 + (c.q & 1) * 8), (double)sv, RLX_AGENT);
    }
    if constexpr (HALF == 0) poll_edge<true, 2>(c, 1);
  } else {
    swap16(v[0], v[2]); swap16(v[1], v[3]);
    st16<0>(mk_rsrc(p.out, SEQ * DM * 2), ((c.q & 1) * 16 + c.l15) * 1024 + (c.q >> 1) * 16, (HALF * 1024 + c.rt * 32) * 1024 + c.db * 128 + dfin * 32,
            u32x4{v[0], v[1], v[2], v[3]});
    if constexpr (HALF == 0) poll_edge<true, 1>(c, 3);
  }
  if constexpr (HALF == 0) { BARRIER(); FENCE(); }
  STAMP(17 + 2 * HALF + layer * 20);
}

__global__ void __launch_bounds__(256, 1) mega_kernel(Params p) {
  __shared__ __attribute__((aligned(16))) char smem[LDS_BYTES];
  const int w0 = __builtin_amdgcn_readfirstlane(threadIdx.x >> 6);
  Ctx c;
  c.ws = p.ws; c.lds = (LAS char*)smem; c.lds0 = (u32)(size_t)c.lds;
  c.rs = mk_rsrc(p.ws, WS_BYTES);
  refresh(c, w0);
  STAMP(0);
  u32 dsk;
  ConvIn inA, inB;
  conv_undef(inA); conv_undef(inB);
  u32 pf_sink;
  {
    u64 pc; u32 len;
    asm volatile("s_getpc_b64 %0\n1:\n\ts_mov_b32 %1, .Lmega_tail-1b" : "=s"(pc), "=s"(len));
    const u32 off = ((c.b >> 3) * 16 + (c.tid & 15)) * 128;
    pf_sink = 0;
    if (c.tid < 16 && off + 4 <= len) pf_sink = *(const GAS u32*)(pc + off);
  }

  {
    const int b = c.b, tid = c.tid, lane = c.lane, w = c.w, q = c.q, l15 = c.l15;
    LAS char* lds = c.lds;
    const int kch = w >> 1, khalf = w & 1;
    const rsrc_t rx = mk_rsrc(p.x0, SEQ * DM * 2), rg = mk_rsrc(p.wg, DM * DF * 2), ru = mk_rsrc(p.wu, DM * DF * 2), rd = mk_rsrc(p.wd, DM * DF * 2);
    const u32x4 r0 = ld16p(rx, lane * 16, (8 * b + 2 * w) * 1024), r1 = ld16p(rx, lane * 16 + 1024, (8 * b + 2 * w) * 1024);
    u32x4 xt[2];
#pragma unroll
    for (int i = 0; i < 2; ++i) xt[i] = ld16p(rx, (tid >> 3) * 1024 + (tid & 7) * 16 + i * 128, (b >> 2) * 32768 + (b & 3) * 256);
    const int dch = 2 * c.cp + kch, pn = dch * NS + 64 * khalf + lane;
    const u16 pA = p.logA[pn], pI = p.Aim[pn], pCr = p.Cre[pn], pCi = p.Cim[pn], pdt = p.logdt[dch];
    dsk = *(const u32*)(p.Dskip + 2 * c.cp);
    const u32 ep = __hip_atomic_load((GAS u32*)(p.ws + OFF_EPOCH), RLX_AGENT);
    const int cbR = b >> 4, sR = b & 15;
    u32x4 wg[2], wu[2];
    int wrow[2], wtau[2];
#pragma unroll
    for (int i = 0; i < 2; ++i) {
      const int id = tid + 256 * i;
      wrow[i] = (id * 5958) >> 16; wtau[i] = id - wrow[i] * 11;
      const u32 vo = wrow[i] * (DF * 2) + wtau[i] * 16 + (id < 352 ? 0u : 0x40000000u) + sR * (32 * DF * 2) + cbR * 172;
      wg[i] = ld16p(rg, vo, 0); wu[i] = ld16p(ru, vo, 0);
    }
    u32x4 dv[2];
    const int s2 = b >> 2, dq = b & 3;
#pragma unroll
    for (int i = 0; i < 2; ++i) {
      const int krow = (tid >> 4) + 16 * i, U = 4 * s2 + (krow >> 3), cbU = (U * 5958) >> 16, fl = 8 * (U - cbU * 11) + (krow & 7);
      dv[i] = ld16p(rd, (86 * cbU + fl) * 1024 + (tid & 15) * 16 + dq * 256 + (fl < 86 ? 0u : 0x40000000u), 0);
    }
    LAS char* wxt = lds + L_XT + (tid >> 3) * 272 + (tid & 7) * 16;
    LAS char* rxt = lds + L_XT + (tid & 7) * (4 * 272) + (tid >> 3) * 4;
    u32 vst = l15 == 0 ? OFF_ST1 + (b >> 7) * 8192 + (w & 1) * 4096 + ((2 * b + (w >> 1)) & 255) * 16 + q * 4
                       : (l15 < 3 ? OFF_ST3 + b * 128 + w * 32 + (l15 - 1) * 16 + q * 4 : 0x7f000000u);
    u32 vxp = (tid >> 3) * 8192 + (tid & 7) * 16;
    u32 sxp = OFF_XP + (b & 3) * (64 * 8192) + (b >> 2) * 128;
    asm volatile("" : "+v"(wxt), "+v"(rxt), "+v"(vst), "+v"(vxp));
    asm volatile("" : "+s"(sxp));
    PIN();
    STAMP(42);
    asm volatile("" :: "v"(pf_sink));
#pragma unroll
    for (int i = 0; i < 2; ++i) *(LAS u32x4*)(wxt + i * 128) = xt[i];
    {
      float s0 = 0.f, q0 = 0.f, s1 = 0.f, q1 = 0.f;
#pragma unroll
      for (int i = 0; i < 4; ++i) {
        s0 = dot2(r0[i], 0x3F803F80u, s0); q0 = dot2(r0[i], r0[i], q0);
        s1 = dot2(r1[i], 0x3F803F80u, s1); q1 = dot2(r1[i], r1[i], q1);
      }
      swap32f(s0, s1); swap32f(q0, q1);
      float sA = s0 + s1, sB = q0 + q1;
      swap16f(sA, sB);
      float sv = sA + sB;
      sv += dpp_f<0xB1>(sv); sv += dpp_f<0x4E>(sv); sv += dpp_f<0x141>(sv); sv += dpp_f<0x140>(sv);
      __builtin_amdgcn_raw_buffer_store_b32(l15 == 0 ? __builtin_bit_cast(u32, sv) : 0u, c.rs, vst, 0, 16);
    }
    SYNC_LDS();
    {
      u32x4 o[2];
#pragma unroll
      for (int i = 0; i < 2; ++i) o[i] = u32x4{*(LAS u32*)(rxt + i * 128), *(LAS u32*)(rxt + i * 128 + 272), *(LAS u32*)(rxt + i * 128 + 544), *(LAS u32*)(rxt + i * 128 + 816)};
      PIN();
      st16(c.rs, vxp, sxp, o[0]); st16(c.rs, vxp, sxp + 32 * 8192, o[1]);
    }
    STAMP(43);
    c.epoch = ep + 1u;
    int ci[11];
#pragma unroll
    for (int i = 0; i < 11; i += 2) {
      const int l0 = 2047 - 8 * tid - i, l1 = l0 - 1;
      const u32 pb = pk_bf16((float)(i >= 8 ? max(l0, 0) : l0), (float)(i >= 8 ? max(l1, 0) : l1));
      const int b0 = (int)(pb & 0xffffu) - (134 << 7) + 128, b1 = (int)(pb >> 16) - (134 << 7) + 128;
      ci[i] = 4 * max(b0, min(l0, 128));
      asm volatile("" : "+v"(ci[i]));
      if (i < 10) { ci[i + 1] = 4 * max(b1, min(l1, 128)); asm volatile("" : "+v"(ci[i + 1])); }
    }
#pragma unroll
    for (int i = 0; i < 2; ++i) {
      LAS char* d = (i == 0 || tid < 96) ? lds + L_IMG1 + wtau[i] * 1056 + wrow[i] * 32 : lds + L_KX + tid * 16;
      const bool last = wtau[i] == 10;
      const u32x4 lo = {wg[i].x, wu[i].x, wg[i].y, wu[i].y}, hi = {wg[i].z, wu[i].z, last ? 0x00003F80u : wg[i].w, last ? 0u : wu[i].w};
      *(LAS u32x4*)d = lo;
      *(LAS u32x4*)(d + 16) = hi;
    }
    {
      LAS char* wi2 = lds + L_IMG2 + ((tid & 15) >> 1) * 1056 + (4 * (tid >> 7) + ((tid >> 4) & 3) + ((tid & 64) ? 16 : 0)) * 32 + (tid & 1) * 16;
#pragma unroll
      for (int i = 0; i < 2; ++i) *(LAS u32x4*)(wi2 + i * 256) = dv[i];
    }

    const float dt = rnd_bf16(expf(bf2f(pdt)));
    const float a2 = -rnd_bf16(expf(bf2f(pA))) * dt * 1.44269504f;
    const float ai = bf2f(pI) * dt;
    const float i2p_hi = 0.15915494f, i2p_lo = (float)(0.15915494309189535 - (double)0.15915494f);
    const float fhi = ai * i2p_hi, flo = __builtin_fmaf(ai, i2p_hi, -fhi) + ai * i2p_lo;
    f32x2 wp[3];
#pragma unroll
    for (int i = 0; i < 3; ++i) {
      const float ps = i == 0 ? 1.f : (i == 1 ? 8.f : 128.f);
      const float mag = __builtin_amdgcn_exp2f(a2 * ps);
      const float rev = __builtin_fmaf(flo, ps, __builtin_amdgcn_fractf(fhi * ps));
      wp[i] = f32x2{mag * __builtin_amdgcn_cosf(rev), mag * __builtin_amdgcn_sinf(rev)};
    }
    const float cr = bf2f(pCr), cim = bf2f(pCi);
    int ce = 14 - __builtin_amdgcn_frexp_expf(wave_max(fmaxf(fabsf(cr), fabsf(cim))));
    ce = ce > 100 ? 100 : (ce < -100 ? -100 : ce);
    asm volatile("" : "+v"(wp[0]), "+v"(wp[1]), "+v"(wp[2]), "+v"(ce));
    vm_wait<0>();
    SYNC_LDS();
    flag_all(c, E_P0);
    STAMP(1);
    {
      LAS char* ri1 = lds + L_IMG1 + 3 * w * 1056 + lane * 8;
      LAS char* ri2 = lds + L_IMG2 + 2 * w * 1056 + lane * 8;
      const u32 sgu = OFF_WGU + ((cbR * 11 + 3 * w) * 16 + sR) * 1024, sgu2 = w == 3 ? 0x7f000000u : sgu + 32768;
      const u32 swd = b < 176 ? OFF_WD + ((dq * 8 + 2 * w) * KS2 + s2) * 1024 : 0x7f000000u;
      u32x4 o[5];
#pragma unroll
      for (int i = 0; i < 3; ++i) o[i] = tr_frag(ri1 + i * 1056);
#pragma unroll
      for (int i = 0; i < 2; ++i) o[3 + i] = tr_frag(ri2 + i * 1056);
      PIN();
      st16(c.rs, lane * 16, sgu, o[0]); st16(c.rs, lane * 16, sgu + 16384, o[1]); st16(c.rs, lane * 16, sgu2, o[2]);
      st16(c.rs, lane * 16, swd, o[3]); st16(c.rs, lane * 16, swd + KS2 * 1024, o[4]);
    }
    LAS char* tb = lds + w * TB_WAVE + lane * 4;
    auto put2 = [&](int ra, f32x2 va, int rb2, f32x2 vb) {
      *(LAS u32*)(tb + ra * TB_ROW) = __builtin_bit_cast(u32, __builtin_convertvector(va, f16x2));
      *(LAS u32*)(tb + rb2 * TB_ROW) = __builtin_bit_cast(u32, __builtin_convertvector(vb, f16x2));
    };
    {
      const f32x2 w8c = {wp[1].x, -wp[1].y}, w16c = cmul(w8c, w8c);
      f32x2 y0 = {ldexpf(cr, ce), -ldexpf(cim, ce)}, y1 = cmul(y0, w8c);
#pragma unroll
      for (int i = 0; i < 8; ++i) {
        put2(41 + 2 * i, y0, 42 + 2 * i, y1);
        if (i < 7) { y0 = cmul(y0, w16c); y1 = cmul(y1, w16c); }
      }
    }
    STAMP(2);
    {
      f32x2 e[8];
      e[1] = wp[0]; e[2] = cmul(e[1], e[1]); e[3] = cmul(e[2], e[1]); e[4] = cmul(e[2], e[2]);
      e[5] = cmul(e[4], e[1]); e[6] = cmul(e[4], e[2]); e[7] = cmul(e[4], e[3]);
      const f32x2 w256 = cmul(wp[2], wp[2]);
      f32x2 ta = {16384.f, 0.f}, tc = wp[2] * 16384.f;
      put2(0, ta, 8, tc);
#pragma unroll
      for (int i = 1; i < 8; ++i) put2(i, e[i] * 16384.f, 8 + i, cmul(tc, e[i]));
      vm_wait<0>();
      BARRIER();
      flag_all(c, E_W);
      ta = w256 * 16384.f; tc = cmul(tc, w256);
      put2(16, ta, 20, tc);
#pragma unroll
      for (int i = 1; i < 4; ++i) put2(16 + i, cmul(ta, e[2 * i]), 20 + i, cmul(tc, e[2 * i]));
#pragma unroll
      for (int i = 0; i < 2; ++i) {
        ta = cmul(ta, w256); tc = cmul(tc, w256);
        put2(24 + 4 * i, ta, 26 + 4 * i, tc);
        put2(25 + 4 * i, cmul(ta, e[4]), 27 + 4 * i, cmul(tc, e[4]));
      }
#pragma unroll
      for (int i = 0; i < 4; ++i) {
        ta = cmul(ta, w256); tc = cmul(tc, w256);
        put2(32 + 2 * i, ta, 33 + 2 * i, tc);
      }
      ta = cmul(ta, w256);
      put2(40, ta, 40, ta);
    }
    peek<false>(c, E_P0);
    f32x4 kk[3];
    {
      LAS char* tr = lds + w * TB_WAVE + l15 * TB_ROW + q * 16;
      u32x4 fa[4][3], fb[4];
#pragma unroll
      for (int G = 0; G < 4; ++G) {
        fb[G] = lds16(tr + 41 * TB_ROW + G * 64);
#pragma unroll
        for (int i = 0; i < 3; ++i) fa[G][i] = lds16(tr + i * 16 * TB_ROW + G * 64);
      }
      PIN();
      sfor<12>([&](auto N) { mm_f16<(N < 3)>(kk[N % 3], fa[N / 3][N % 3], fb[N / 3]); });
      asm volatile("s_nop 7" : "+v"(kk[0]), "+v"(kk[1]), "+v"(kk[2]));
      const float ks = ldexpf(2.f * dt, -14 - ce);
#pragma unroll
      for (int i = 0; i < 3; ++i) kk[i] *= ks;
    }
    STAMP(40);
    LAS char* kx = lds + L_KX + kch * 3072 + lane * 16;
    if (khalf) {
#pragma unroll
      for (int i = 0; i < 3; ++i) *(LAS f32x4*)(kx + i * 1024) = kk[i];
    }
    SYNC_LDS();
    if (!khalf) {
#pragma unroll
      for (int i = 0; i < 3; ++i) kk[i] += *(LAS f32x4*)(kx + i * 1024);
      LAS float* T = (LAS float*)(lds + L_KT + kch * KT_CH);
      const int i0 = 128 * (q >> 1) + 4 * (q & 1) + 8 * l15;
      *(LAS f32x4*)(T + i0) = kk[0];
      if (q < 2) {
        *(LAS f32x4*)(T + 256 + 64 * q + 4 * l15) = kk[1];
      } else {
        *(LAS f32x2*)(T + 384 + 64 * (q - 2) + 2 * l15) = kk[1].xy;
        *(LAS f32x2*)(T + 416 + 64 * (q - 2) + 2 * l15) = kk[1].zw;
      }
#pragma unroll
      for (int j = 0; j < 4; ++j) T[512 + 64 * q + 16 * j + l15] = kk[2][j];
      if (lane < 4) T[lane - 4] = 0.f;
    }
    poll_edge<false, 0>(c, E_P0);
    SYNC_LDS();
    STAMP(41);
    conv_load<0, 1>(c, 0, inA, 0ull); conv_load<0, 1>(c, 1, inB, 0ull);
    peek<false>(c, E_W);
    PIN();
#pragma unroll
    for (int ch = 0; ch < 2; ++ch) {
      float V[11];
#pragma unroll
      for (int i = 0; i < 11; ++i) V[i] = *(LAS float*)(lds + L_KT + ch * KT_CH + ci[i]);
#pragma unroll
      for (int s = 0; s < 4; ++s) {
        const u32x4 o = {pk_bf16(V[3 - s], V[4 - s]), pk_bf16(V[5 - s], V[6 - s]), pk_bf16(V[7 - s], V[8 - s]), pk_bf16(V[9 - s], V[10 - s])};
        *(LAS u32x4*)(lds + L_KC + ch * KC_CH + s * KC_COPY + tid * 16) = o;
      }
    }
    if (tid < 16) {
      const u32x4 z = {0u, 0u, 0u, 0u};
      *(LAS u32x4*)(lds + L_KC + (tid >> 1) * KC_COPY + 4096 + (tid & 1) * 16) = z;
    }
    if (b == 0 && tid == 0) __hip_atomic_store((GAS u32*)(c.ws + OFF_EPOCH), c.epoch, RLX_AGENT);
  }
  STAMP(3);

  conv_phase<0>(c, w0, dsk, inA, inB);

#pragma unroll 1
  for (int layer = 0; layer < 2; ++layer) {
    refresh(c, w0);
    STAMP(10 + layer * 20);
    G1 g;
    g.layer = layer;
    g.xa = xa_ptr(c, layer);
    g.res = res_ptr(c, layer);
    g.ring[0] = c.lds + L_RING + c.lane * 8;
    g.ring[1] = g.ring[0] + 65536;
    asm volatile("" : "+v"(g.ring[0]), "+v"(g.ring[1]));
    g.ldsw = c.lds0 + L_RING + c.w * 4096;
    g.v16 = c.lane * 16;
    g.wd0 = OFF_WD + (c.db * 4 + 2 * (c.w & 1)) * (KS2 * 1024) + (c.w >> 1) * 4096;
    g.wd1 = g.wd0 + KS2 * 1024;
    g.vst = c.q * (KS2 * 1024) + c.l15 * 16;
    g.sst = OFF_H + layer * H_BYTES + c.rb * 4 * (KS2 * 1024) + (c.cb * 11 + 3 * c.w) * 256;
    g.hsrc = OFF_H + layer * H_BYTES + (c.rt * 2 + (c.w & 1)) * (KS2 * 1024) + (c.w >> 1) * 4096;
    g.hsrc5 = g.hsrc + 5 * 8192 - (c.w >> 1) * 2048;
    g.ldsr = c.lds0 + L_RES + c.w * 1024;
    g.pk = c.lds + L_ST + c.l15 * 16;
#pragma unroll
    for (int i = 0; i < 3; ++i) g.wsum[i] = *(LAS f32x4*)(c.lds + L_WS + c.w * 192 + i * 64 + c.q * 16);
    asm volatile("" : "+s"(g.wd0), "+s"(g.wd1), "+s"(g.xa), "+s"(g.res), "+s"(g.ldsw), "+s"(g.sst), "+s"(g.hsrc), "+s"(g.hsrc5), "+s"(g.ldsr));
    asm volatile("" : "+v"(g.vst), "+v"(g.pk), "+v"(g.v16), "+v"(g.wsum[0]), "+v"(g.wsum[1]), "+v"(g.wsum[2]));
    u32x4 WD[NWDV], Bc[4];
    f32x4 acc[3][4];
    vm_wait<5>();
    BARRIER(); FENCE();
#pragma unroll
    for (int ct = 0; ct < 4; ++ct) Bc[ct] = tr_frag(g.ring[0] + ct * 4096);
    STAMP(11 + layer * 20);
    g1_main<0>(c, g, WD, acc, Bc);
    g1_epilogue<0>(c, g, acc, WD);
    vm_wait<11>();
    BARRIER(); FENCE();
#pragma unroll
    for (int ct = 0; ct < 4; ++ct) Bc[ct] = tr_frag(g.ring[1] + ct * 4096);
    STAMP(13 + layer * 20);
    g1_main<1>(c, g, WD, acc, Bc);
    g1_epilogue<1>(c, g, acc, WD);
    STAMP(15 + layer * 20);
    refresh(c, w0);
    ConvIn nA, nB;
    g2_unit<0>(c, g, p, layer, WD, nA);
    conv_undef(nA);
    g2_unit<1>(c, g, p, layer, WD, nA);
    if (layer == 0) { conv_undef(nB); conv_phase<1>(c, w0, dsk, nA, nB); }
  }
  asm volatile(".Lmega_tail:");
}

}

extern "C" void kernel_launch(void* const* d_in, const int* in_sizes, int n_in, void* d_out, int out_size, void* d_ws,
                              size_t ws_size, hipStream_t stream) {
  Params p;
  p.x0 = (const u16*)d_in[0];
  p.logA = (const u16*)d_in[1];
  p.Aim = (const u16*)d_in[2];
  p.Cre = (const u16*)d_in[3];
  p.Cim = (const u16*)d_in[4];
  p.logdt = (const u16*)d_in[5];
  p.Dskip = (const u16*)d_in[6];
  p.wg = (const u16*)d_in[8];
  p.wu = (const u16*)d_in[9];
  p.wd = (const u16*)d_in[10];
  p.out = (u16*)d_out;
  p.ws = (char*)d_ws;
  mega_kernel<<<NBLK, 256, 0, stream>>>(p);
}
```

```cpp
#include <hip/hip_runtime.h>
#include <hip/hip_bf16.h>
#include <utility>

typedef unsigned int u32;
typedef unsigned short u16;
typedef unsigned long long u64;
typedef u32 u32x4 __attribute__((ext_vector_type(4)));
typedef u32 u32x2 __attribute__((ext_vector_type(2)));
typedef float f32x4 __attribute__((ext_vector_type(4)));
typedef float f32x2 __attribute__((ext_vector_type(2)));
typedef short s16x4 __attribute__((ext_vector_type(4)));
typedef __bf16 bf16x8 __attribute__((ext_vector_type(8)));
typedef __bf16 bf16x2 __attribute__((ext_vector_type(2)));
typedef _Float16 f16x8 __attribute__((ext_vector_type(8)));
typedef _Float16 f16x2 __attribute__((ext_vector_type(2)));
#define LAS __attribute__((address_space(3)))
#define GAS __attribute__((address_space(1)))
#define RLX_AGENT __ATOMIC_RELAXED, __HIP_MEMORY_SCOPE_AGENT
#define DEV __device__ __forceinline__

#define CODE_PREFETCH 1
#define MAP_ROWS 0
#define H_REFETCH 0
#define PK_B 28
#define PK_A 4
#define FL_G 2
#define PK_G 84
#define FL_B 8
#define STAMP_WAVE 0

namespace {

constexpr int SEQ = 2048, DM = 512, NS = 128, DF = 1376;
constexpr int NBLK = 256;
constexpr int KS2 = 44;

constexpr u32 OFF_EPOCH = 0;
constexpr u32 OFF_FLAG = 4096;
constexpr u32 OFF_GFLAG = OFF_FLAG + 8 * 8192;
constexpr u32 OFF_ST1 = OFF_GFLAG + 8192;
constexpr u32 OFF_ST3 = OFF_ST1 + 16384;
constexpr u32 OFF_WGU = OFF_ST3 + 32768;
constexpr u32 OFF_WD = OFF_WGU + 16 * 11 * 16 * 1024;
constexpr u32 OFF_XC = OFF_WD + 32 * KS2 * 1024;
constexpr u32 OFF_XP = OFF_XC + 2 * 2097152;
constexpr u32 OFF_H = OFF_XP + 2 * 2097152;
constexpr u32 H_BYTES = 128 * KS2 * 1024;
constexpr u32 OFF_DBG = OFF_H + 2 * H_BYTES;
constexpr u32 WS_BYTES = OFF_DBG + 256 * 64 * 16;
constexpr int E_P0 = 0, E_W = 7, E_CONV = 1  , E_DOWN = 3  ;

constexpr int KC_COPY = 4160, KC_CH = 4 * KC_COPY;
constexpr int L_KC = 0;
constexpr int L_RING = 2 * KC_CH;
constexpr int L_RES = L_RING + 7 * 16384;
constexpr int L_ST = L_RES + 8192;
constexpr int L_WS = L_ST + 2048;
constexpr int L_VD = L_WS + 768;
constexpr int LDS_BYTES = 163840;
constexpr int L_U = L_RING + 5 * 16384;
constexpr int U_CH = 4608;
constexpr int L_P = L_RING + 6 * 16384;
constexpr int TB_ROW = 272, TB_WAVE = 57 * TB_ROW;
constexpr int L_TB = L_RING;
constexpr int L_IMG1 = L_TB + 4 * TB_WAVE;
constexpr int L_KT = L_IMG1 + 11 * 1056 + 16;
constexpr int KT_CH = 772 * 4;
constexpr int L_IMG2 = L_U + 2 * U_CH;
constexpr int L_XT = L_IMG2 + 8 * 1056;
constexpr int L_DUMMY = L_XT + 32 * 272;
static_assert(L_KT + 2 * KT_CH <= L_U && L_DUMMY + 4128 <= L_RES && L_VD + 16 <= LDS_BYTES, "lds");

struct Params {
  const u16 *x0, *logA, *Aim, *Cre, *Cim, *logdt, *Dskip, *wg, *wu, *wd;
  u16* out;
  char* ws;
};

DEV float bf_lo(u32 v) { return __builtin_bit_cast(float, v << 16); }
DEV float bf_hi(u32 v) { return __builtin_bit_cast(float, v & 0xffff0000u); }
DEV float bf2f(u16 v) { return __builtin_bit_cast(float, (u32)v << 16); }
DEV u32 pk_bf16(float lo, float hi) {
  f32x2 f = {lo, hi};
  return __builtin_bit_cast(u32, __builtin_convertvector(f, bf16x2));
}
DEV float rnd_bf16(float v) { return bf_lo(pk_bf16(v, 0.f)); }
DEV u32x2 tr_read(LAS char* p) {
  return __builtin_bit_cast(u32x2, __builtin_amdgcn_ds_read_tr16_b64_v4i16((LAS s16x4*)p));
}
DEV u32x4 tr_frag(LAS char* p) {
  u32x2 a = tr_read(p), b = tr_read(p + 512);
  u32x4 r = {a.x, a.y, b.x, b.y};
  return r;
}
DEV u32x4 lds16(LAS char* p) { return *(LAS u32x4*)p; }
template <int CTRL> DEV float dpp_f(float v) {
  return __builtin_bit_cast(float, __builtin_amdgcn_mov_dpp(__builtin_bit_cast(int, v), CTRL, 0xf, 0xf, true));
}
template <int CTRL> DEV u32 dpp_u(u32 v) { return (u32)__builtin_amdgcn_mov_dpp((int)v, CTRL, 0xf, 0xf, true); }
DEV void swap16(u32& a, u32& b) { auto r = __builtin_amdgcn_permlane16_swap(a, b, false, false); a = r[0]; b = r[1]; }
DEV void swap32(u32& a, u32& b) { auto r = __builtin_amdgcn_permlane32_swap(a, b, false, false); a = r[0]; b = r[1]; }
DEV void swap16f(float& a, float& b) { u32 x = __builtin_bit_cast(u32, a), y = __builtin_bit_cast(u32, b); swap16(x, y); a = __builtin_bit_cast(float, x); b = __builtin_bit_cast(float, y); }
DEV void swap32f(float& a, float& b) { u32 x = __builtin_bit_cast(u32, a), y = __builtin_bit_cast(u32, b); swap32(x, y); a = __builtin_bit_cast(float, x); b = __builtin_bit_cast(float, y); }
DEV float wave_sum(float v) {
  v += dpp_f<0xB1>(v);
  v += dpp_f<0x4E>(v);
  v += dpp_f<0x141>(v);
  v += dpp_f<0x140>(v);
  float a = v, b = v;
  swap16f(a, b); v = a + b;
  a = v; b = v;
  swap32f(a, b); v = a + b;
  return v;
}
DEV float wave_max(float v) {
  v = fmaxf(v, dpp_f<0xB1>(v)); v = fmaxf(v, dpp_f<0x4E>(v)); v = fmaxf(v, dpp_f<0x141>(v)); v = fmaxf(v, dpp_f<0x140>(v));
  float a = v, b = v;
  swap16f(a, b); v = fmaxf(a, b);
  a = v; b = v;
  swap32f(a, b); v = fmaxf(a, b);
  return v;
}
DEV float sel(u64 m, float a, float b) { float r; asm("v_cndmask_b32_e64 %0, %1, %2, %3" : "=v"(r) : "v"(a), "v"(b), "s"(m)); return r; }
DEV u32 selu(u64 m, u32 a, u32 b) { u32 r; asm("v_cndmask_b32_e64 %0, %1, %2, %3" : "=v"(r) : "v"(a), "v"(b), "s"(m)); return r; }
constexpr u64 M_B0 = 0xAAAAAAAAAAAAAAAAull, M_B1 = 0xCCCCCCCCCCCCCCCCull, M_B2 = 0xF0F0F0F0F0F0F0F0ull, M_B3 = 0xFF00FF00FF00FF00ull;
DEV float dot2(u32 a, u32 b, float acc) {
  return __builtin_amdgcn_fdot2_f32_bf16(__builtin_bit_cast(bf16x2, a), __builtin_bit_cast(bf16x2, b), acc, false);
}
DEV f32x2 cmul(f32x2 a, f32x2 b) {
  const f32x2 t = a.yy * f32x2{-b.y, b.x};
  return __builtin_elementwise_fma(a.xx, b, t);
}
#define ACLOB "a0","a1","a2","a3","a4","a5","a6","a7","a8","a9","a10","a11","a12","a13","a14","a15","a16","a17","a18","a19","a20","a21","a22","a23","a24","a25","a26","a27","a28","a29","a30","a31","a32","a33","a34","a35","a36","a37","a38","a39","a40","a41","a42","a43","a44","a45","a46","a47","a48","a49","a50","a51","a52","a53","a54","a55","a56","a57","a58","a59","a60","a61","a62","a63","a64","a65","a66","a67","a68","a69","a70","a71","a72","a73","a74","a75","a76","a77","a78","a79","a80","a81","a82","a83","a84","a85","a86","a87","a88","a89","a90","a91","a92","a93","a94","a95","a96","a97","a98","a99","a100","a101","a102","a103","a104","a105","a106","a107","a108","a109","a110","a111","a112","a113","a114","a115","a116","a117","a118","a119","a120","a121","a122","a123","a124","a125","a126","a127","a128","a129","a130","a131","a132","a133","a134","a135","a136","a137","a138","a139","a140","a141","a142","a143","a144","a145","a146","a147","a148","a149","a150","a151","a152","a153","a154","a155","a156","a157","a158","a159","a160","a161","a162","a163","a164","a165","a166","a167","a168","a169","a170","a171","a172","a173","a174","a175","a176","a177","a178","a179","a180","a181","a182","a183","a184","a185","a186","a187","a188","a189","a190","a191","a192","a193","a194","a195","a196","a197","a198","a199","a200","a201","a202","a203","a204","a205","a206","a207","a208","a209","a210","a211","a212","a213","a214","a215","a216","a217","a218","a219","a220","a221","a222","a223","a224","a225","a226","a227","a228","a229","a230","a231","a232","a233","a234","a235","a236","a237","a238","a239","a240","a241","a242","a243","a244","a245","a246","a247","a248","a249","a250","a251","a252","a253","a254","a255"
#define BARRIER() __builtin_amdgcn_s_barrier()
#define FENCE() asm volatile("" ::: "memory")
#define PIN() __builtin_amdgcn_sched_barrier(0)
#define LGKM0() asm volatile("s_waitcnt lgkmcnt(0)" ::: "memory", ACLOB)
#define SYNC_LDS() do { LGKM0(); BARRIER(); FENCE(); } while (0)
template <int N> DEV void vm_wait() { asm volatile("s_waitcnt vmcnt(%0)" ::"i"(N) : "memory", ACLOB); }

typedef __amdgpu_buffer_rsrc_t rsrc_t;
DEV rsrc_t mk_rsrc(const void* p, u32 bytes) { return __builtin_amdgcn_make_buffer_rsrc((void*)p, 0, bytes, 0x00020000); }
DEV u32x4 ld16(rsrc_t r, u32 voff, u32 soff) { return __builtin_amdgcn_raw_buffer_load_b128(r, voff, soff, 16); }
DEV u32x4 ld16p(rsrc_t r, u32 voff, u32 soff) { return __builtin_amdgcn_raw_buffer_load_b128(r, voff, soff, 0); }
template <int AUX = 16> DEV void st16(rsrc_t r, u32 voff, u32 soff, u32x4 v) {
  __builtin_amdgcn_raw_buffer_store_b128(v, r, voff, soff, AUX);
  asm volatile("s_nop 1" ::"v"(v) : "memory");
}

template <int... Is, class F> DEV void sfor_impl(std::integer_sequence<int, Is...>, F&& f) { (f(std::integral_constant<int, Is>{}), ...); }
template <int N, class F> DEV void sfor(F&& f) { sfor_impl(std::make_integer_sequence<int, N>{}, f); }
template <bool FIRST> DEV void mm_vv(f32x4& acc, const u32x4& a, const u32x4& b) {
  if constexpr (FIRST) asm volatile("v_mfma_f32_16x16x32_bf16 %0, %1, %2, 0" : "=&v"(acc) : "v"(a), "v"(b));
  else asm volatile("v_mfma_f32_16x16x32_bf16 %0, %1, %2, %0" : "+v"(acc) : "v"(a), "v"(b));
}
template <bool FIRST> DEV void mm_f16(f32x4& acc, const u32x4& a, const u32x4& b) {
  if constexpr (FIRST) asm volatile("v_mfma_f32_16x16x32_f16 %0, %1, %2, 0" : "=&v"(acc) : "v"(a), "v"(b));
  else asm volatile("v_mfma_f32_16x16x32_f16 %0, %1, %2, %0" : "+v"(acc) : "v"(a), "v"(b));
}
template <bool FIRST, int A> DEV void mm_av(f32x4& acc, const u32x4& b) {
  if constexpr (FIRST) asm volatile("v_mfma_f32_16x16x32_bf16 %0, a[%c2:%c3], %1, 0" : "=&v"(acc) : "v"(b), "i"(A), "i"(A + 3));
  else asm volatile("v_mfma_f32_16x16x32_bf16 %0, a[%c2:%c3], %1, %0" : "+v"(acc) : "v"(b), "i"(A), "i"(A + 3));
}
template <int A, int OFF> DEV void ld16_a(rsrc_t r, u32 voff, u32 soff) {
  asm volatile("buffer_load_dwordx4 a[%c3:%c4], %0, %1, %2 offen offset:%c5 sc1" ::"v"(voff), "s"(r), "s"(soff), "i"(A), "i"(A + 3), "i"(OFF) : "memory");
}
#define MM_DONE4(a) asm volatile("s_nop 7" : "+v"((a)[0]), "+v"((a)[1]), "+v"((a)[2]), "+v"((a)[3]))

template <int G, int L> DEV void dma1(rsrc_t r, u32 voff, u32 soff, u32 lds) {
  u32 t;
  asm volatile("s_add_u32 m0, %4, %6\n\ts_add_u32 %0, %3, %5\n\tbuffer_load_dwordx4 %1, %2, %0 offen sc1 lds"
               : "=&s"(t) : "v"(voff), "s"(r), "s"(soff), "s"(lds), "i"(G), "i"(L) : "memory", "scc");
}
template <int G, int L> DEV void dma4(rsrc_t r, u32 voff, u32 soff, u32 lds) {
  u32 t;
  asm volatile("s_add_u32 m0, %4, %6\n\ts_add_u32 %0, %3, %5\n\tbuffer_load_dwordx4 %1, %2, %0 offen sc1 lds\n\t"
               "buffer_load_dwordx4 %1, %2, %0 offen offset:1024 sc1 lds\n\tbuffer_load_dwordx4 %1, %2, %0 offen offset:2048 sc1 lds\n\t"
               "buffer_load_dwordx4 %1, %2, %0 offen offset:3072 sc1 lds"
               : "=&s"(t) : "v"(voff), "s"(r), "s"(soff), "s"(lds), "i"(G), "i"(L) : "memory", "scc");
}

constexpr u32 SPIN_CAP = 1u << 16;
constexpr int SYNC_WAVE = 2;

struct Ctx {
  char* ws;
  rsrc_t rs;
  LAS char* lds;
  u32 lds0, epoch;
  int b, tid, lane, w, q, l15, cp, cb, rb, db, rt;
  mutable u32 junk;
  u32 vflag, vpeek, sflag, sflagg, speek, speekg;
};

DEV void flag_setup(Ctx& c) {
  c.vflag = c.tid == 192 ? 0u : 0x7f000000u;
  c.vpeek = c.lane * 64;
  c.sflag = OFF_FLAG + (c.b >> 2) * 64 + (c.b & 3) * 4;
  c.sflagg = OFF_FLAG + 8 * 4096 + c.rb * 256 + (c.cb >> 2) * 64 + (c.cb & 3) * 4;
  c.speek = c.w == SYNC_WAVE ? OFF_FLAG : 0x7f000000u;
  c.speekg = c.w == SYNC_WAVE ? OFF_FLAG + 8 * 4096 + (c.rt >> 1) * 256 : 0x7f000000u;
  asm volatile("" : "+v"(c.vflag), "+v"(c.vpeek));
  asm volatile("" : "+s"(c.sflag), "+s"(c.sflagg), "+s"(c.speek), "+s"(c.speekg));
}
template <int E> DEV void flag_st(const Ctx& c, u32 base) {
  u32 t;
  asm volatile("s_add_u32 %0, %4, %5\n\tbuffer_store_dword %1, %2, %3, %0 offen sc1" : "=&s"(t) : "v"(c.epoch), "v"(c.vflag), "s"(c.rs), "s"(base), "i"(E * 4096) : "memory", "scc");
}
template <bool GRP, int E> DEV void peek(const Ctx& c, u32 base) {
  u32 t;
  if constexpr (GRP)
    asm volatile("s_mov_b64 exec, 15\n\ts_add_u32 %0, %3, %4\n\tbuffer_load_dwordx4 a[252:255], %1, %2, %0 offen sc1\n\ts_mov_b64 exec, -1"
                 : "=&s"(t) : "v"(c.vpeek), "s"(c.rs), "s"(base), "i"(E * 4096) : "memory", "scc");
  else
    asm volatile("s_add_u32 %0, %3, %4\n\tbuffer_load_dwordx4 a[252:255], %1, %2, %0 offen sc1"
                 : "=&s"(t) : "v"(c.vpeek), "s"(c.rs), "s"(base), "i"(E * 4096) : "memory", "scc");
}
template <bool GRP, int N> DEV u64 flags_miss(const Ctx& c) {
  u32 f0, f1, f2, f3;
  asm volatile("s_waitcnt vmcnt(%4)\n\tv_accvgpr_read_b32 %0, a252\n\tv_accvgpr_read_b32 %1, a253\n\tv_accvgpr_read_b32 %2, a254\n\tv_accvgpr_read_b32 %3, a255"
               : "=v"(f0), "=v"(f1), "=v"(f2), "=v"(f3) : "i"(N) : "memory");
  const u32 d = ((f0 ^ c.epoch) | (f1 ^ c.epoch)) | ((f2 ^ c.epoch) | (f3 ^ c.epoch));
  return __builtin_amdgcn_ballot_w64(d != 0u) & (GRP ? 15ull : ~0ull);
}
template <bool GRP, int N, int E> DEV void poll_edge(const Ctx& c, u32 base) {
  if (c.w == SYNC_WAVE) {
    if (flags_miss<GRP, N>(c)) {
#pragma unroll 1
      for (u32 sp = 0; sp < SPIN_CAP; ++sp) {
        peek<GRP, E>(c, base);
        if (flags_miss<GRP, 0>(c) == 0ull) break;
        __builtin_amdgcn_s_sleep(4);
      }
    }
  }
  FENCE();
}

#define STAMP(i) do { } while (0)

DEV void refresh(Ctx& c, int w0) {
  int tid = threadIdx.x, b = blockIdx.x, w = w0;
  asm volatile("" : "+v"(tid)); asm volatile("" : "+s"(b)); asm volatile("" : "+s"(w));
  c.tid = tid; c.b = b; c.w = w; c.lane = tid & 63; c.q = c.lane >> 4; c.l15 = tid & 15;
  const int xcd = b & 7, rr = b >> 3;
  c.cp = xcd * 32 + rr;
  c.cb = (xcd & 3) * 4 + (rr & 3); c.rb = (xcd >> 2) * 8 + (rr >> 2);
  c.db = (xcd & 1) * 4 + (rr & 3); c.rt = (xcd >> 1) * 8 + (rr >> 2);
}

constexpr int AW(int tile, int s) { return (tile * 16 + s) * 4; }
template <int N> DEV void ld_wgu(const Ctx& c) {
  constexpr int tl = N % 3, s = N / 3;
  const u32 v = c.lane * 16 + ((tl == 2 && c.w == 3) ? 0x40000000u : 0u);
  ld16_a<AW(tl, s), (s & 3) * 1024>(c.rs, v, OFF_WGU + (c.cb * 11 + 3 * c.w + tl) * 16384 + (s >> 2) * 4096);
}
constexpr int NWDA = 15, VWD = 140;
DEV u32 wd_ptr(const Ctx& c, int other) { return OFF_WD + (c.db * 4 + 2 * (c.w & 1) + ((c.w >> 1) ^ other)) * (KS2 * 1024) + (c.w >> 1) * 4096; }
template <int N> DEV void ld_wd(const Ctx& c, u32 v16, u32 wd0, u32 wd1, u64 lanes) {
  constexpr int OFF = ((N >> 1) & 3) * 1024;
  if constexpr (N < NWDA)
    asm volatile("s_mov_b64 exec, %3\n\tbuffer_load_dwordx4 a[%c5:%c6], %0, %1, %2 offen offset:%c4 sc1\n\ts_mov_b64 exec, -1"
                 :: "v"(v16), "s"(c.rs), "s"((N & 1) ? wd1 : wd0), "s"(lanes), "i"(OFF), "i"(192 + 4 * N), "i"(195 + 4 * N) : "memory");
  else
    asm volatile("s_mov_b64 exec, %3\n\tbuffer_load_dwordx4 v[%c5:%c6], %0, %1, %2 offen offset:%c4 sc1\n\ts_mov_b64 exec, -1"
                 :: "v"(v16), "s"(c.rs), "s"((N & 1) ? wd1 : wd0), "s"(lanes), "i"(OFF), "i"(VWD + 4 * (N - NWDA)), "i"(VWD + 3 + 4 * (N - NWDA)) : "memory");
}
template <int V> DEV void mm_wv(f32x4& acc, const u32x4& b) {
  asm volatile("v_mfma_f32_16x16x32_bf16 %0, v[%c2:%c3], %1, %0" : "+v"(acc) : "v"(b), "i"(V), "i"(V + 3));
}

struct ConvIn { u32x4 x; u32x4 st[4]; };
template <int LAYER> DEV void conv_load(const Ctx& c, int half, ConvIn& in, u32 skip = 0) {
  const u32 v = c.tid * 16;
  if constexpr (LAYER == 0) {
    asm volatile("buffer_load_dwordx4 %0, %3, %4, %5 offen sc1\n\tbuffer_load_dwordx4 %1, %3, %4, %6 offen sc1\n\tbuffer_load_dwordx4 %2, %3, %4, %7 offen sc1"
                 : "=&v"(in.x), "=&v"(in.st[0]), "=&v"(in.st[1])
                 : "v"(v), "s"(c.rs), "s"(OFF_XP + c.cp * 8192 + half * 4096), "s"(OFF_ST1 + half * 8192), "s"(OFF_ST1 + half * 8192 + 4096)
                 : "memory");
  } else {
    const u32 so = OFF_ST3 + half * 16384;
    asm volatile("s_cmp_lg_u32 %12, 0\n\ts_cbranch_scc1 1f\n\t"
                 "buffer_load_dwordx4 %1, %5, %6, %8 offen sc1\n\tbuffer_load_dwordx4 %2, %5, %6, %9 offen sc1\n\t"
                 "buffer_load_dwordx4 %3, %5, %6, %10 offen sc1\n\tbuffer_load_dwordx4 %4, %5, %6, %11 offen sc1\n\tbuffer_load_dwordx4 %0, %5, %6, %7 offen sc1\n1:"
                 : "=&v"(in.x), "=&v"(in.st[0]), "=&v"(in.st[1]), "=&v"(in.st[2]), "=&v"(in.st[3])
                 : "v"(v), "s"(c.rs), "s"(OFF_XP + 2097152 + c.cp * 8192 + half * 4096), "s"(so), "s"(so + 4096), "s"(so + 8192), "s"(so + 12288), "s"(skip)
                 : "memory", "scc");
  }
}
template <int LAYER, int N> DEV void conv_wait(ConvIn& in) {
  if constexpr (LAYER == 0) asm volatile("s_waitcnt vmcnt(%3)" : "+v"(in.x), "+v"(in.st[0]), "+v"(in.st[1]) : "i"(N) : "memory");
  else asm volatile("s_waitcnt vmcnt(%5)" : "+v"(in.x), "+v"(in.st[0]), "+v"(in.st[1]), "+v"(in.st[2]), "+v"(in.st[3]) : "i"(N) : "memory");
}
template <int LAYER> DEV void conv_norm(LAS char* pu, const ConvIn& in, u32 dsk, f32x4 (&base)[2]) {
  f32x2 st[4];
  if (LAYER == 0) {
#pragma unroll
    for (int i = 0; i < 2; ++i) { const f32x4 v = __builtin_bit_cast(f32x4, in.st[i]); st[2 * i] = v.xy; st[2 * i + 1] = v.zw; }
  } else {
#pragma unroll
    for (int i = 0; i < 4; ++i) {
      const u32x2 lo = {in.st[i].x, in.st[i].y}, hi = {in.st[i].z, in.st[i].w};
      st[i] = f32x2{(float)__builtin_bit_cast(double, lo), (float)__builtin_bit_cast(double, hi)};
    }
  }
  const f32x2 dd = {bf_lo(dsk), bf_hi(dsk)};
  f32x2 u[4], x[4];
#pragma unroll
  for (int i = 0; i < 4; ++i) {
    const f32x2 me = __builtin_elementwise_fma(st[i], f32x2{-1.f / DM, 1.f / DM}, f32x2{0.f, 1e-5f});
    const float r = __builtin_amdgcn_rsqf(fmaxf(__builtin_fmaf(-me.x, me.x, me.y), 1e-5f));
    x[i] = f32x2{bf_lo(in.x[i]), bf_hi(in.x[i])};
    u[i] = (x[i] + me.x) * r;
  }
  *(LAS u32x2*)pu = u32x2{pk_bf16(u[0].x, u[1].x), pk_bf16(u[2].x, u[3].x)};
  *(LAS u32x2*)(pu + U_CH) = u32x2{pk_bf16(u[0].y, u[1].y), pk_bf16(u[2].y, u[3].y)};
#pragma unroll
  for (int i = 0; i < 4; ++i) { const f32x2 bs = __builtin_elementwise_fma(u[i], dd, x[i]); base[0][i] = bs.x; base[1][i] = bs.y; }
  asm volatile("" : "+v"(base[0]), "+v"(base[1]));
}
template <int H> DEV void lds8h(u32x4& d, LAS char* p) {
  const u32x2 r = *(LAS u32x2*)(p + 8 * H);
  if constexpr (H == 0) { d.x = r.x; d.y = r.y; } else { d.z = r.x; d.w = r.y; }
}
DEV void ldsA(u32x4& d, LAS char* p) { lds8h<0>(d, p); PIN(); lds8h<1>(d, p); PIN(); }
constexpr int tri_j(int m) { return m < 4 ? 0 : (m < 7 ? 1 : (m < 9 ? 2 : 3)); }
constexpr int tri_i(int m) { return m < 4 ? m : (m < 7 ? m - 3 : (m < 9 ? m - 5 : 3)); }
template <int B0, bool ZERO, class F>
DEV void conv_tri(LAS char* pa, LAS char* pb, u32x4 (&Ac)[4], f32x4 (&acc)[4], F&& side) {
  u32x4 Bc[4];
#pragma unroll
  for (int j = 0; j < 4; ++j) Bc[j] = lds16(pb + (B0 + j) * 512);
  PIN();
  sfor<4>([&](auto IT) {
    constexpr int it = IT;
    u32x4 An[4], Bn[4];
    sfor<10>([&](auto M) {
      constexpr int m = M, i = tri_i(m), j = tri_j(m);
      if constexpr (it < 3) {
        if constexpr (m < 8) lds8h<m & 1>(An[m >> 1], pa - 64 * (it + 1) + (7 - (m >> 1)) * 512);
        if constexpr (m == 0 || m == 5) { PIN(); Bn[m / 5] = lds16(pb - 64 * (it + 1) + (B0 + m / 5) * 512); }
        if constexpr (m >= 8) Bn[m - 6] = lds16(pb - 64 * (it + 1) + (B0 + m - 6) * 512);
      }
      side(std::integral_constant<int, it * 10 + m>{});
      mm_vv<ZERO && it == 0 && j == 0>(acc[i], Ac[i - j], Bc[j]);
      PIN();
    });
    if constexpr (it < 3) {
#pragma unroll
      for (int j = 0; j < 4; ++j) { Ac[j] = An[j]; Bc[j] = Bn[j]; }
    }
  });
}
template <int R> DEV void cross_first(LAS char* pa, LAS char* pb, u32x4 (&Ac)[7], u32x4 (&Bc)[4]) {
  if constexpr (R == 0) Bc[0] = lds16(pb);
  else if constexpr (R < 9) lds8h<(R - 1) & 1>(Ac[3 + (R - 1) / 2], pa + (3 - (R - 1) / 2) * 512);
  else if constexpr ((R - 9) % 3 == 0) Bc[1 + (R - 9) / 3] = lds16(pb + (1 + (R - 9) / 3) * 512);
  else lds8h<(R - 9) % 3 - 1>(Ac[2 - (R - 9) / 3], pa + (4 + (R - 9) / 3) * 512);
}
template <class F>
DEV void conv_cross(LAS char* pa, LAS char* pb, f32x4 (&acc)[4], u32x4 (&Ac)[7], u32x4 (&Bc)[4], u32x4 (&At)[4], F&& side) {
  sfor<4>([&](auto IT) {
    constexpr int it = IT;
    u32x4 An[7], Bn[4];
    sfor<16>([&](auto M) {
      constexpr int m = M, j = m >> 2, i = m & 3;
      if constexpr (it < 3) {
        if constexpr (m < 14) lds8h<m & 1>(An[6 - (m >> 1)], pa - 64 * (it + 1) + (m >> 1) * 512);
        if constexpr (m == 0 || m == 8) { PIN(); Bn[m / 8] = lds16(pb - 64 * (it + 1) + (m / 8) * 512); }
        if constexpr (m >= 14) Bn[m - 12] = lds16(pb - 64 * (it + 1) + (m - 12) * 512);
      } else if constexpr (m < 8) {
        lds8h<m & 1>(At[m >> 1], pa + (7 - (m >> 1)) * 512);
      }
      side(std::integral_constant<int, it * 16 + m>{});
      mm_vv<it == 0 && j == 0>(acc[i], Ac[3 + i - j], Bc[j]);
      PIN();
    });
    if constexpr (it < 3) {
#pragma unroll
      for (int k = 0; k < 7; ++k) Ac[k] = An[k];
#pragma unroll
      for (int k = 0; k < 4; ++k) Bc[k] = Bn[k];
    }
  });
}
struct ConvF { LAS char *pa, *pb, *pu, *pw, *pr; u32 vo, so, xa, ldsw, v16; f32x4 a[2], b[2]; };
DEV u32 xa_ptr(const Ctx& c, int layer) { return OFF_XC + layer * 2097152 + (c.rb * 4 + c.w) * 16384; }
DEV void conv_setup(const Ctx& c, int layer, ConvF& r) {
  const int qq = c.tid & 3, ii = (c.tid >> 2) & 15, kch = c.w >> 1, khalf = c.w & 1;
  r.pa = c.lds + L_KC + kch * KC_CH + (c.l15 & 3) * KC_COPY + 2 * (2044 - 16 * (8 * khalf + 1) - (c.l15 & 12) + 8 * c.q) - 7 * 512 - 192;
  r.pb = c.lds + L_U + kch * U_CH + 2 * (256 + 16 * (c.l15 - 8 * khalf - 1) + 8 * c.q) - 192;
  r.pu = c.lds + L_U + 512 + c.tid * 8;
  r.pw = c.lds + L_P + c.w * 4096 + (c.q * 16 + (c.l15 ^ (c.q * 4))) * 16;
  r.pr = c.lds + L_P + c.w * 1024 + (qq * 16 + (ii ^ (qq * 4))) * 16;
  r.vo = (c.tid >> 2) * 16384 + (c.tid & 1) * 32 + (c.tid & 2) * 8;
  r.so = OFF_XC + layer * 2097152 + c.cp * 64;
  r.xa = xa_ptr(c, layer); r.ldsw = c.lds0 + L_RING + c.w * 4096; r.v16 = c.lane * 16;
  asm volatile("" : "+v"(r.pa), "+v"(r.pb), "+v"(r.pu), "+v"(r.pw), "+v"(r.pr), "+v"(r.vo), "+v"(r.v16));
  asm volatile("" : "+s"(r.so), "+s"(r.xa), "+s"(r.ldsw));
}
DEV void conv_pad(const Ctx& c) {
  u32x2 z = {0u, 0u};
  asm volatile("" : "+v"(z));
  *(LAS u32x2*)(c.lds + L_U + c.lane * 8) = z; *(LAS u32x2*)(c.lds + L_U + U_CH + c.lane * 8) = z;
}
DEV void conv_finish2(ConvF& r) {
#pragma unroll
  for (int ch = 0; ch < 2; ++ch) { r.a[ch] = *(LAS f32x4*)(r.pr + ch * 8192); r.b[ch] = *(LAS f32x4*)(r.pr + ch * 8192 + 4096); }
}
DEV void conv_finish3(const Ctx& c, int half, const ConvF& r, const f32x4 (&base)[2]) {
  u32x2 o[2];
#pragma unroll
  for (int ch = 0; ch < 2; ++ch) {
    const f32x4 t = (r.a[ch] + base[ch]) + r.b[ch];
    o[ch].x = pk_bf16(t.x, t.y); o[ch].y = pk_bf16(t.z, t.w);
  }
  const u32 r0 = dpp_u<0xB1>(selu(M_B0, o[1].x, o[0].x)), r1 = dpp_u<0xB1>(selu(M_B0, o[1].y, o[0].y));
  const u32x4 v = {selu(M_B0, o[0].x, r0), selu(M_B0, o[0].y, r1), selu(M_B0, r0, o[1].x), selu(M_B0, r1, o[1].y)};
  st16(c.rs, r.vo, r.so + half * 1048576, v);
}
DEV u32 res_ptr(const Ctx& c, int layer) { return OFF_XC + layer * 2097152 + (c.rt * 2 + (c.w >> 1)) * 16384 + c.db * 2048 + (c.w & 1) * 1024; }

template <int S0, int S1, class F> DEV void wsum_part(LAS char* pws, F&& side) {
  f32x4 ws[3];
  u32x4 ones = {0x3F803F80u, 0x3F803F80u, 0x3F803F80u, 0x3F803F80u};
  if constexpr (S0 > 0) {
#pragma unroll
    for (int i = 0; i < 3; ++i) ws[i] = *(LAS f32x4*)(pws + i * 64);
  }
  asm volatile("s_nop 1" : "+v"(ones));
  sfor<3 * (S1 - S0)>([&](auto N) { mm_av<(S0 == 0 && N < 3), AW(N % 3, S0 + N / 3)>(ws[N % 3], ones); side(N); });
  asm volatile("s_nop 7" : "+v"(ws[0]), "+v"(ws[1]), "+v"(ws[2]));
#pragma unroll
  for (int i = 0; i < 3; ++i) *(LAS f32x4*)(pws + i * 64) = ws[i];
}
template <int LAYER> DEV void conv_phase(Ctx& c, ConvF& cf, u32 dsk, ConvIn& inA, ConvIn& inB) {
  LAS char* const pa = cf.pa + 192;
  LAS char* const pb = cf.pb + 192;
  f32x4 baseA[2], baseB[2];
  u32x4 Ac[4];
  if constexpr (LAYER == 1) {
#pragma unroll
    for (int j = 0; j < 4; ++j) ldsA(Ac[j], pa + (7 - j) * 512);
  }
  conv_wait<LAYER, LAYER == 0 ? 3 : 2>(inA);
  STAMP(4 + LAYER * 20);
  conv_norm<LAYER>(cf.pu, inA, dsk, baseA);
  SYNC_LDS();
  if constexpr (LAYER == 0) {
#pragma unroll
    for (int j = 0; j < 4; ++j) ldsA(Ac[j], pa + (7 - j) * 512);
  }
  STAMP(5 + LAYER * 20);
  f32x4 accA[4], acc[4];
  u32x4 Ax[7], Bx[4];
  conv_tri<0, true>(pa, pb, Ac, accA, [&](auto K) {
    if constexpr (LAYER == 0 && K == 0) peek<false, E_W>(c, c.speek);
    if constexpr (LAYER == 1 && K == FL_B) { vm_wait<0>(); BARRIER(); flag_st<E_DOWN + 1>(c, c.sflag); }
    if constexpr (K >= 30 && K < 39) { cross_first<2 * (K - 30)>(pa, pb, Ax, Bx); PIN(); cross_first<2 * (K - 30) + 1>(pa, pb, Ax, Bx); }
  });
  STAMP(6 + LAYER * 20);
  conv_cross(pa, pb, acc, Ax, Bx, Ac, [&](auto K) {
    if constexpr (K == 3) asm volatile("s_nop 3" : "+v"(accA[0]), "+v"(accA[1]), "+v"(accA[2]), "+v"(accA[3]));
    if constexpr (K < 3) *(LAS f32x4*)(cf.pw + K * 1024) = accA[K];
    if constexpr (K == 3) *(LAS f32x4*)(cf.pw + K * 1024) = accA[K];
    if constexpr (K == 9) { if constexpr (LAYER == 0) poll_edge<false, 0, E_W>(c, c.speek); SYNC_LDS(); conv_finish2(cf); }
    if constexpr (K == 14) conv_finish3(c, 0, cf, baseA);
    if constexpr (LAYER == 0) {
      if constexpr (K >= 16 && K % 4 == 0) ld_wgu<K / 4 - 4>(c);
    } else {
      if constexpr (K == PK_B) peek<false, E_DOWN + 1>(c, c.speek);
    }
  });
  if constexpr (LAYER == 1) poll_edge<false, 0, E_DOWN + 1>(c, c.speek);
  vm_wait<LAYER == 0 ? 12 : 1>();
  BARRIER(); FENCE();
  if constexpr (LAYER == 1) conv_load<1>(c, 1, inB);
  flag_st<E_CONV + 4 * LAYER>(c, c.sflag);
  STAMP(7 + LAYER * 20);
  if constexpr (LAYER == 0) sfor<4>([&](auto N) { ld_wgu<12 + N>(c); });
  conv_wait<LAYER, LAYER == 0 ? 17 : 1>(inB);
  conv_norm<LAYER>(cf.pu + 2048, inB, dsk, baseB);
  SYNC_LDS();
  STAMP(8 + LAYER * 20);
  conv_tri<4, false>(pa, pb, Ac, acc, [&](auto K) {
    if constexpr (LAYER == 0) { if constexpr (K % 4 == 0) ld_wgu<16 + K / 4>(c); }
    else if constexpr (K == PK_A) peek<false, E_CONV + 4>(c, c.speek);
  });
  MM_DONE4(acc);
#pragma unroll
  for (int i = 0; i < 4; ++i) *(LAS f32x4*)(cf.pw + i * 1024) = acc[i];
  if constexpr (LAYER == 0) { peek<false, E_CONV>(c, c.speek); sfor<4>([&](auto N) { ld_wgu<26 + N>(c); }); }
  else poll_edge<false, 0, E_CONV + 4>(c, c.speek);
  SYNC_LDS();
  conv_finish2(cf);
  if constexpr (LAYER == 1) dma4<0, 0>(c.rs, cf.v16, cf.xa, cf.ldsw);
  conv_finish3(c, 1, cf, baseB);
  STAMP(9 + LAYER * 20);
  if constexpr (LAYER == 0) {
    sfor<3>([&](auto N) { ld_wgu<30 + N>(c); });
    poll_edge<false, 8, E_CONV>(c, c.speek);
    BARRIER(); FENCE();
    dma4<0, 0>(c.rs, cf.v16, cf.xa, cf.ldsw); dma4<4096, 16384>(c.rs, cf.v16, cf.xa, cf.ldsw);
    vm_wait<17>();
    wsum_part<0, 8>(c.lds + L_WS + c.w * 192 + c.q * 16, [&](auto N) { if constexpr (N < 15) ld_wgu<33 + N>(c); });
  } else {
    dma4<4096, 16384>(c.rs, cf.v16, cf.xa, cf.ldsw);
  }
}

struct G1 {
  u32 xa;
  u32 res, ldsr;
  LAS char* ring[2];
  u32 ldsw;
  u32 v16;
  u64 lanes;
  u32 wd[6][2];
  u32 vst, sst;
  u32 hsrc, hsrc5;
  LAS char* pk;
  LAS char* pws;
  LAS char *rd[2], *prs, *exw, *exr, *sts;
  u32 vo, so, soh, va, sa, eoff, fA, fG, pA, pG;
  int layer;
};
DEV void h_refetch(const Ctx& c, u32 vst, u32 sst) {
}
template <int N> DEV void ld_wd(const Ctx& c, const G1& g) { ld_wd<N>(c, g.v16, g.wd[N >> 3][0], g.wd[N >> 3][1], g.lanes); }
template <int HALF, bool STATS, int S>
DEV void g1_step(const Ctx& c, const G1& g, f32x4 (&acc)[3][4], u32x4 (&Bc)[4]) {
  if constexpr (S == 3) {
    vm_wait<4>(); BARRIER(); FENCE();
    if constexpr (STATS && HALF == 0) flag_st<E_CONV + 1>(c, g.fA);
    if constexpr (STATS && HALF == 1) flag_st<0>(c, g.fG);
    if constexpr (HALF == 1) h_refetch(c, g.vst, g.sst);
  }
  if constexpr (S == 7) { vm_wait<HALF ? 8 : 9>(); BARRIER(); FENCE(); }
  if constexpr (HALF == 0 && S == 11) { vm_wait<16>(); BARRIER(); FENCE(); }
  if constexpr (HALF == 1 && S == 11) { if constexpr (!STATS) poll_edge<true, 6, 0>(c, g.pG); vm_wait<11>(); BARRIER(); FENCE(); }
  u32x4 Bn[4];
  sfor<12>([&](auto M) {
    constexpr int m = M, ct = m / 3, k = m % 3;
    if constexpr (S < 15 && k < 2) {
      constexpr int slot = (HALF * 4 + (S + 1) / 4) % 7;
      const u32x2 r = tr_read(g.ring[slot >> 2] + (slot & 3) * 16384 + ct * 4096 + ((S + 1) & 3) * 1024 + k * 512);
      if constexpr (k == 0) { Bn[ct].x = r.x; Bn[ct].y = r.y; } else { Bn[ct].z = r.x; Bn[ct].w = r.y; }
    }
    if constexpr (k == 2 && (ct & 1) == 0) {
      constexpr int n = 2 * (S - 1) + (ct >> 1);
      if constexpr (S >= 1 && S <= 4) dma1<HALF * 1048576 + 8192 + n * 1024, ((HALF * 4 + 2 + (n >> 2)) % 7) * 16384 + (n & 3) * 1024>(c.rs, g.v16, g.xa, g.ldsw);
      if constexpr (S == 5 && ct == 0) dma1<HALF * 1048576, HALF * 4096>(c.rs, g.v16, g.res, g.ldsr);
      if constexpr (HALF == 1 && S >= 11) dma1<((n - 20) >> 2) * 8192 + ((n - 20) & 3) * 1024, (((n - 20) >> 2) + 1) * 16384 + ((n - 20) & 3) * 1024>(c.rs, g.v16, g.hsrc, g.ldsw);
      if constexpr (HALF == 0 && n >= 9) ld_wd<n - 9>(c, g);
      if constexpr (HALF == 1 && n >= 9 && n <= 19) ld_wd<24 + n>(c, g);
    }
    if constexpr (HALF == 0 && m == 5 && S >= 6 && S <= 12) ld_wd<15 + S>(c, g);
    if constexpr (!STATS && HALF == 0 && S == 12 && m == 11) peek<false, E_CONV + 1>(c, g.pA);
    if constexpr (!STATS && HALF == 1 && S == 7 && m == 11) peek<true, 0>(c, g.pG);
    if constexpr (STATS && k == 2) mm_vv<S == 0>(acc[k][ct], Bc[ct], Bc[ct]);
    else mm_av<S == 0, AW(k, S)>(acc[k][ct], Bc[ct]);
    PIN();
  });
  if constexpr (S < 15) {
#pragma unroll
    for (int ct = 0; ct < 4; ++ct) Bc[ct] = Bn[ct];
    g1_step<HALF, STATS, S + 1>(c, g, acc, Bc);
  } else {
    MM_DONE4(acc[2]); asm volatile("" : "+v"(acc[0][3]), "+v"(acc[1][3]), "+v"(acc[0][2]), "+v"(acc[1][2]), "+v"(acc[0][1]), "+v"(acc[1][1]));
  }
}
template <int HALF>
DEV void g1_main(const Ctx& c, const G1& g, f32x4 (&acc)[3][4], u32x4 (&Bc)[4]) {
  if (c.w == 3) {
    g1_step<HALF, true, 0>(c, g, acc, Bc);
    float sm[4], sq[4];
#pragma unroll
    for (int ct = 0; ct < 4; ++ct) {
      sm[ct] = acc[1][ct][0];
      sq[ct] = sel(M_B1, sel(M_B0, acc[2][ct][0], acc[2][ct][1]), sel(M_B0, acc[2][ct][2], acc[2][ct][3]));
    }
    swap32f(sm[0], sm[2]); swap32f(sm[1], sm[3]); swap16f(sm[2], sm[3]);
    swap32f(sq[0], sq[2]); swap32f(sq[1], sq[3]); swap16f(sq[0], sq[1]); swap16f(sq[2], sq[3]);
    const float e2 = sel(M_B3, sel(M_B2, sq[0], sq[1]), sel(M_B2, sq[2], sq[3]));
    const float mu = sm[3] * (-1.f / DM);
    const float vr = fmaxf(__builtin_fmaf(-mu, mu, e2 * (1.f / DM)), 0.f) + 1e-5f;
    const float r = __builtin_amdgcn_rsqf(vr);
    const float c1 = (-2.f * 0.7978845608f * 1.44269504f) * r;
    *(LAS f32x4*)(c.lds + L_ST + g.v16) = f32x4{mu, vr, c1, c1 * 0.044715f * (r * r)};
  } else {
    g1_step<HALF, false, 0>(c, g, acc, Bc);
    if constexpr (HALF == 0) poll_edge<false, 6, E_CONV + 1>(c, g.pA);
  }
}
template <int HALF, class F>
DEV void g1_epilogue(const Ctx& c, const G1& g, const f32x4 (&acc)[3][4], F&& next) {
  f32x4 k4[4], wsum[3];
#pragma unroll
  for (int i = 0; i < 3; ++i) wsum[i] = *(LAS f32x4*)(g.pws + i * 64);
  const u32 vst = g.vst, sst = g.sst + HALF * 64 * (KS2 * 1024);
  const u32 sst2 = c.w == 3 ? 0x7f000000u : sst;
  SYNC_LDS();
  STAMP(12 + 2 * HALF + g.layer * 20);
#pragma unroll
  for (int ct = 0; ct < 4; ++ct) k4[ct] = *(LAS f32x4*)(g.pk + ct * 256);
  auto stage = [&](auto N) {
    if constexpr (HALF == 0) {
      if constexpr (N < 8) dma1<1048576 + N * 1024, (4 + (N >> 2)) * 16384 + (N & 3) * 1024>(c.rs, g.v16, g.xa, g.ldsw);
      else if constexpr (N >= 9 && N < 14) ld_wd<19 + N>(c, g);
    } else if constexpr (N < 12 && N % 2 == 0) {
      constexpr int j = 10 + N / 2;
      dma1<(j >> 2) * 8192 + (j & 3) * 1024, ((j >> 2) + 1) * 16384 + (j & 3) * 1024>(c.rs, g.v16, g.hsrc, g.ldsw);
    }
    PIN();
  };
  sfor<3>([&](auto I) {
    constexpr int i = I;
    u32 hv[4];
    sfor<2>([&](auto P) {
      constexpr int pi = P;
      constexpr int n0 = 6 * i + 3 * pi;
      f32x2 gg[2], gu[2], ex[2];
      if constexpr (n0 == 15) { next(); PIN(); }
      stage(std::integral_constant<int, n0>{});
#pragma unroll
      for (int e = 0; e < 2; ++e) {
        const f32x4 k = k4[2 * pi + e];
        gg[e] = __builtin_elementwise_fma(k.xx, wsum[i].xy, acc[i][2 * pi + e].xy);
        const f32x2 ar = gg[e] * __builtin_elementwise_fma(gg[e] * gg[e], k.ww, k.zz);
        ex[e] = f32x2{__builtin_amdgcn_exp2f(ar.x), __builtin_amdgcn_exp2f(ar.y)};
      }
      PIN();
      stage(std::integral_constant<int, n0 + 1>{});
#pragma unroll
      for (int e = 0; e < 2; ++e) {
        const f32x4 k = k4[2 * pi + e];
        gu[e] = gg[e] * __builtin_elementwise_fma(k.xx, wsum[i].zw, acc[i][2 * pi + e].zw);
        const f32x2 dn = __builtin_elementwise_fma(ex[e], k.yy, k.yy);
        const f32x2 h = gu[e] * f32x2{__builtin_amdgcn_rcpf(dn.x), __builtin_amdgcn_rcpf(dn.y)};
        hv[2 * pi + e] = pk_bf16(h.x, h.y);
      }
      PIN();
      stage(std::integral_constant<int, n0 + 2>{});
    });
    swap32(hv[0], hv[2]); swap32(hv[1], hv[3]);
    swap16(hv[0], hv[1]); swap16(hv[2], hv[3]);
    st16(c.rs, vst + i * 256, i == 2 ? sst2 : sst, u32x4{hv[0], hv[1], hv[2], hv[3]});
    PIN();
  });
}

constexpr int HB_OFF = 64 * KS2 * 1024;
template <int HALF, int C>
DEV void g2_chunk(const Ctx& c, const G1& g, int layer, f32x4 (&acc)[4], u32x4 (&Bc)[8]) {
  if constexpr (HALF == 0) {
    if constexpr (C == FL_G) { vm_wait<6>(); BARRIER(); FENCE(); flag_st<1>(c, g.fG); h_refetch(c, g.vst, g.sst + 64 * (KS2 * 1024)); }
    if constexpr (C == 3) { vm_wait<1 + H_REFETCH>(); BARRIER(); FENCE(); }
  } else if constexpr (C < 5) {
    vm_wait<C == 0 ? 9 : (C == 1 ? 11 : (C == 2 ? 7 : (C == 3 ? 2 : 0)))>();
    BARRIER(); FENCE();
  }
  constexpr int NK = C < 5 ? 4 : 2, NKN = C < 4 ? 4 : 2;
  u32x4 Bn[8];
  sfor<NK * 4>([&](auto M) {
    constexpr int m = M, ks = m >> 2, ct = (m >> 1) & 1, dt = m & 1, n = (C * 4 + ks) * 2 + dt;
    if constexpr (C < 5 && m < NKN * 2) Bn[m] = lds16(g.rd[(C + 2) >> 2] + ((C + 2) & 3) * 16384 + (m & 1) * 4096 + (m >> 1) * 1024);
    if constexpr (C == 0 && m % 3 == 0) {
      if constexpr (m < 12) dma1<HALF * HB_OFF + 4 * 8192 + (m / 3) * 1024, 5 * 16384 + (m / 3) * 1024>(c.rs, g.v16, g.hsrc, g.ldsw);
      else dma1<HALF * HB_OFF + (m / 3 - 4) * 1024, 6 * 16384 + (m / 3 - 4) * 1024>(c.rs, g.v16, g.hsrc5, g.ldsw);
    }
    if constexpr (HALF == 1 && C == 4 && m == 0) peek<false, E_DOWN>(c, c.speek);
    if constexpr (HALF == 0 && C * 16 + m == PK_G) peek<true, 1>(c, g.pG);
    if constexpr (n < NWDA) mm_av<C == 0 && ks == 0 && dt == 1, 192 + 4 * n>(acc[dt * 2 + ct], Bc[ks * 2 + ct]);
    else mm_wv<VWD + 4 * (n - NWDA)>(acc[dt * 2 + ct], Bc[ks * 2 + ct]);
    PIN();
  });
  if constexpr (C < 5) {
#pragma unroll
    for (int m = 0; m < NKN * 2; ++m) Bc[m] = Bn[m];
    g2_chunk<HALF, C + 1>(c, g, layer, acc, Bc);
  }
}
template <int HALF> DEV void g2_res(const G1& g, f32x4 (&acc)[4]) {
#pragma unroll
  for (int ct = 0; ct < 2; ++ct) {
    const u32x2 r = tr_read(g.prs + HALF * 4096 + ct * 2048);
    acc[ct] = f32x4{bf_lo(r.x), bf_hi(r.x), bf_lo(r.y), bf_hi(r.y)};
  }
  asm volatile("" : "+v"(acc[0]), "+v"(acc[1]));
}
DEV void g2_first(const G1& g, u32x4 (&Bc)[8]) {
#pragma unroll
  for (int m = 0; m < 8; ++m) Bc[m] = lds16(g.rd[0] + 16384 + (m & 1) * 4096 + (m >> 1) * 1024);
}
template <int HALF>
DEV void g2_unit(const Ctx& c, const G1& g, const Params& p, int layer, f32x4 (&acc)[4], u32x4 (&Bc)[8], ConvIn& inA) {
  g2_chunk<HALF, 0>(c, g, layer, acc, Bc);
  MM_DONE4(acc);
  STAMP(16 + 2 * HALF + layer * 20);
#pragma unroll
  for (int ct = 0; ct < 2; ++ct) *(LAS f32x4*)(g.exw + HALF * 8192 + ct * 1024) = acc[2 + ct];
  if constexpr (HALF == 1) { if (layer == 0) poll_edge<false, 0, E_DOWN>(c, c.speek); }
  SYNC_LDS();
  if constexpr (HALF == 1) conv_load<1>(c, 0, inA, layer);
  u32 v[4];
#pragma unroll
  for (int ct = 0; ct < 2; ++ct) {
    const f32x4 o = acc[ct] + *(LAS f32x4*)(g.exr + HALF * 8192 + ct * 1024);
    v[2 * ct] = pk_bf16(o[0], o[1]); v[2 * ct + 1] = pk_bf16(o[2], o[3]);
  }
  if (layer == 0) {
    float st[4];
#pragma unroll
    for (int ct = 0; ct < 2; ++ct) {
      st[2 * ct] = dot2(v[2 * ct], 0x3F803F80u, dot2(v[2 * ct + 1], 0x3F803F80u, 0.f));
      st[2 * ct + 1] = dot2(v[2 * ct], v[2 * ct], dot2(v[2 * ct + 1], v[2 * ct + 1], 0.f));
    }
    swap32f(st[0], st[2]); swap32f(st[1], st[3]);
    float sA = st[0] + st[2], sB = st[1] + st[3];
    swap16f(sA, sB);
    *(LAS float*)(g.sts + c.w * 4) = sA + sB;
    u32 t0 = dpp_u<0xB1>(selu(M_B0, v[1], v[0])), t1 = dpp_u<0xB1>(selu(M_B0, v[3], v[2]));
    const u32 a0 = selu(M_B0, v[0], t0), a1 = selu(M_B0, t0, v[1]), a2 = selu(M_B0, v[2], t1), a3 = selu(M_B0, t1, v[3]);
    t0 = dpp_u<0x4E>(selu(M_B1, a2, a0)); t1 = dpp_u<0x4E>(selu(M_B1, a3, a1));
    SYNC_LDS();
    const f32x4 s4 = *(LAS f32x4*)g.sts;
    st16(c.rs, g.vo, HALF ? g.soh : g.so, u32x4{selu(M_B1, a0, t0), selu(M_B1, a1, t1), selu(M_B1, t0, a2), selu(M_B1, t1, a3)});
    const double sv = (double)((s4.x + s4.y) + (s4.z + s4.w));
    asm volatile("buffer_atomic_add_f64 %0, %1, %2, %3 offen" ::"v"(sv), "v"(g.va), "s"(c.rs), "s"(g.sa + HALF * 16384) : "memory");
    if constexpr (HALF == 0) poll_edge<true, 2, 1>(c, g.pG);
  } else {
    swap16(v[0], v[2]); swap16(v[1], v[3]);
    st16<0>(mk_rsrc(p.out, SEQ * DM * 2), g.vo, HALF ? g.soh : g.so, u32x4{v[0], v[1], v[2], v[3]});
    if constexpr (HALF == 0) poll_edge<true, 1, 1>(c, g.pG);
  }
  if constexpr (HALF == 0) { BARRIER(); FENCE(); }
  STAMP(17 + 2 * HALF + layer * 20);
}

__global__ void __launch_bounds__(256, 1) __attribute__((amdgpu_num_vgpr(140))) mega_kernel(Params p) {
  asm volatile("" ::: "v255", "a255");
  __shared__ __attribute__((aligned(16))) char smem[LDS_BYTES];
  const int w0 = __builtin_amdgcn_readfirstlane(threadIdx.x >> 6);
  Ctx c;
  c.ws = p.ws; c.lds = (LAS char*)smem; c.lds0 = (u32)(size_t)c.lds;
  c.rs = mk_rsrc(p.ws, WS_BYTES);
  refresh(c, w0);
  STAMP(0);
  u32 dsk;
  ConvIn inA, inB;
  ConvF cf;
  u32 pf_sink;
  {
    u64 pc; u32 len;
    asm volatile("s_getpc_b64 %0\n1:\n\ts_mov_b32 %1, .Lmega_tail-1b" : "=s"(pc), "=s"(len));
    const u32 off = ((c.b >> 3) * 16 + (c.tid & 15)) * 128;
    pf_sink = 0;
    if (c.tid < 16 && off + 4 <= len) pf_sink = *(const GAS u32*)(pc + off);
  }

  {
    const int b = c.b, tid = c.tid, lane = c.lane, w = c.w, q = c.q, l15 = c.l15;
    LAS char* lds = c.lds;
    const int kch = w >> 1, khalf = w & 1;
    const rsrc_t rx = mk_rsrc(p.x0, SEQ * DM * 2), rg = mk_rsrc(p.wg, DM * DF * 2), ru = mk_rsrc(p.wu, DM * DF * 2), rd = mk_rsrc(p.wd, DM * DF * 2);
    const u32x4 r0 = ld16p(rx, lane * 16, (8 * b + 2 * w) * 1024), r1 = ld16p(rx, lane * 16 + 1024, (8 * b + 2 * w) * 1024);
    u32x4 xt[2];
#pragma unroll
    for (int i = 0; i < 2; ++i) xt[i] = ld16p(rx, (tid >> 3) * 1024 + (tid & 7) * 16 + i * 128, (b >> 2) * 32768 + (b & 3) * 256);
    const int dch = 2 * c.cp + kch, pn = dch * NS + 64 * khalf + lane;
    const u16 pA = p.logA[pn], pI = p.Aim[pn], pCr = p.Cre[pn], pCi = p.Cim[pn], pdt = p.logdt[dch];
    const u16 pCr2 = p.Cre[pn ^ 64], pCi2 = p.Cim[pn ^ 64];
    dsk = *(const u32*)(p.Dskip + 2 * c.cp);
    const u32 ep = __hip_atomic_load((GAS u32*)(p.ws + OFF_EPOCH), RLX_AGENT);
    const int cbR = b >> 4, sR = b & 15;
    u32x4 wg[2], wu[2];
    int wrow[2], wtau[2];
#pragma unroll
    for (int i = 0; i < 2; ++i) {
      const int id = tid + 256 * i;
      wrow[i] = (id * 5958) >> 16; wtau[i] = id - wrow[i] * 11;
      const u32 vo = wrow[i] * (DF * 2) + wtau[i] * 16 + (id < 352 ? 0u : 0x40000000u) + sR * (32 * DF * 2) + cbR * 172;
      wg[i] = ld16p(rg, vo, 0); wu[i] = ld16p(ru, vo, 0);
    }
    u32x4 dv[2];
    const int s2 = b >> 2, dq = b & 3;
#pragma unroll
    for (int i = 0; i < 2; ++i) {
      const int krow = (tid >> 4) + 16 * i, U = 4 * s2 + (krow >> 3), cbU = (U * 5958) >> 16, fl = 8 * (U - cbU * 11) + (krow & 7);
      dv[i] = ld16p(rd, (86 * cbU + fl) * 1024 + (tid & 15) * 16 + dq * 256 + (fl < 86 ? 0u : 0x40000000u), 0);
    }
    LAS char* wxt = lds + L_XT + (tid >> 3) * 272 + (tid & 7) * 16;
    LAS char* rxt = lds + L_XT + (tid & 7) * (4 * 272) + (tid >> 3) * 4;
    u32 vst = l15 == 0 ? OFF_ST1 + (b >> 7) * 8192 + (w & 1) * 4096 + ((2 * b + (w >> 1)) & 255) * 16 + q * 4
                       : (l15 < 3 ? OFF_ST3 + b * 128 + w * 32 + (l15 - 1) * 16 + q * 4 : 0x7f000000u);
    u32 vxp = (tid >> 3) * 8192 + (tid & 7) * 16;
    u32 sxp = OFF_XP + (b & 3) * (64 * 8192) + (b >> 2) * 128;
    LAS char* wi1[2] = {lds + L_IMG1 + wtau[0] * 1056 + wrow[0] * 32, tid < 96 ? lds + L_IMG1 + wtau[1] * 1056 + wrow[1] * 32 : lds + L_DUMMY + tid * 16};
    LAS char* wi2 = lds + L_IMG2 + ((tid & 15) >> 1) * 1056 + (4 * (tid >> 7) + ((tid >> 4) & 3) + ((tid & 64) ? 16 : 0)) * 32 + (tid & 1) * 16;
    LAS char* ta0 = lds + L_KT + kch * KT_CH + (khalf ? 512 + 64 * q + l15 : 128 * (q >> 1) + 4 * (q & 1) + 8 * l15) * 4;
    LAS char* ta1 = lds + L_KT + kch * KT_CH + (q < 2 ? 256 + 64 * q + 4 * l15 : 384 + 64 * (q - 2) + 2 * l15) * 4;
    LAS char* ta1b = ta1 + (q < 2 ? 2 : 32) * 4;
    asm volatile("" : "+v"(wxt), "+v"(rxt), "+v"(vst), "+v"(vxp), "+v"(wi1[0]), "+v"(wi1[1]), "+v"(wi2), "+v"(ta0), "+v"(ta1), "+v"(ta1b));
    asm volatile("" : "+s"(sxp));
    LAS char* ci[11];
#pragma unroll
    for (int i = 0; i < 11; i += 2) {
      const int l0 = 2047 - 8 * tid - i, l1 = l0 - 1;
      const u32 pb = pk_bf16((float)(i >= 8 ? max(l0, 0) : l0), (float)(i >= 8 ? max(l1, 0) : l1));
      const int b0 = (int)(pb & 0xffffu) - (134 << 7) + 128, b1 = (int)(pb >> 16) - (134 << 7) + 128;
      ci[i] = lds + L_KT + 4 * max(b0, min(l0, 128));
      asm volatile("" : "+v"(ci[i]));
      if (i < 10) { ci[i + 1] = lds + L_KT + 4 * max(b1, min(l1, 128)); asm volatile("" : "+v"(ci[i + 1])); }
    }
    {
      const u32x4 z = {0u, 0u, 0u, 0u};
      *(LAS u32x4*)(tid < 16 ? lds + L_KC + (tid >> 1) * KC_COPY + 4096 + (tid & 1) * 16 : (tid < 18 ? lds + L_KT - 16 + (tid & 1) * KT_CH : lds + L_DUMMY + tid * 16)) = z;
    }
    conv_setup(c, 0, cf);
    conv_pad(c);
    flag_setup(c);
    PIN();
    STAMP(42);
    asm volatile("" :: "v"(pf_sink));
#pragma unroll
    for (int i = 0; i < 2; ++i) *(LAS u32x4*)(wxt + i * 128) = xt[i];
    {
      float s0 = 0.f, q0 = 0.f, s1 = 0.f, q1 = 0.f;
#pragma unroll
      for (int i = 0; i < 4; ++i) {
        s0 = dot2(r0[i], 0x3F803F80u, s0); q0 = dot2(r0[i], r0[i], q0);
        s1 = dot2(r1[i], 0x3F803F80u, s1); q1 = dot2(r1[i], r1[i], q1);
      }
      swap32f(s0, s1); swap32f(q0, q1);
      float sA = s0 + s1, sB = q0 + q1;
      swap16f(sA, sB);
      float sv = sA + sB;
      sv += dpp_f<0xB1>(sv); sv += dpp_f<0x4E>(sv); sv += dpp_f<0x141>(sv); sv += dpp_f<0x140>(sv);
      __builtin_amdgcn_raw_buffer_store_b32(l15 == 0 ? __builtin_bit_cast(u32, sv) : 0u, c.rs, vst, 0, 16);
    }
    SYNC_LDS();
    {
      u32x4 o[2];
#pragma unroll
      for (int i = 0; i < 2; ++i) o[i] = u32x4{*(LAS u32*)(rxt + i * 128), *(LAS u32*)(rxt + i * 128 + 272), *(LAS u32*)(rxt + i * 128 + 544), *(LAS u32*)(rxt + i * 128 + 816)};
      PIN();
      st16(c.rs, vxp, sxp, o[0]); st16(c.rs, vxp, sxp + 32 * 8192, o[1]);
    }
    STAMP(43);
    c.epoch = ep + 1u;

    const float dt = rnd_bf16(expf(bf2f(pdt)));
    const float a2 = -rnd_bf16(expf(bf2f(pA))) * dt * 1.44269504f;
    const float ai = bf2f(pI) * dt;
    const float i2p_hi = 0.15915494f, i2p_lo = (float)(0.15915494309189535 - (double)0.15915494f);
    const float fhi = ai * i2p_hi, flo = __builtin_fmaf(ai, i2p_hi, -fhi) + ai * i2p_lo;
    f32x2 wp[3];
#pragma unroll
    for (int i = 0; i < 3; ++i) {
      const float ps = i == 0 ? 1.f : (i == 1 ? 8.f : 128.f);
      const float mag = __builtin_amdgcn_exp2f(a2 * ps);
      const float rev = __builtin_fmaf(flo, ps, __builtin_amdgcn_fractf(fhi * ps));
      wp[i] = f32x2{mag * __builtin_amdgcn_cosf(rev), mag * __builtin_amdgcn_sinf(rev)};
    }
    const float cr = bf2f(pCr), cim = bf2f(pCi);
    int ce = 14 - __builtin_amdgcn_frexp_expf(wave_max(fmaxf(fmaxf(fabsf(cr), fabsf(cim)), fmaxf(fabsf(bf2f(pCr2)), fabsf(bf2f(pCi2))))));
    ce = ce > 100 ? 100 : (ce < -100 ? -100 : ce);
    LAS char* tb = lds + L_TB + w * TB_WAVE + lane * 4;
    auto put2 = [&](int ra, f32x2 va, int rb2, f32x2 vb) {
      *(LAS u32*)(tb + ra * TB_ROW) = __builtin_bit_cast(u32, __builtin_convertvector(va, f16x2));
      *(LAS u32*)(tb + rb2 * TB_ROW) = __builtin_bit_cast(u32, __builtin_convertvector(vb, f16x2));
    };
    {
      const f32x2 w8c = {wp[1].x, -wp[1].y}, w16c = cmul(w8c, w8c);
      f32x2 y0 = {ldexpf(cr, ce), -ldexpf(cim, ce)}, y1 = cmul(y0, w8c);
#pragma unroll
      for (int i = 0; i < 8; ++i) {
        put2(41 + 2 * i, y0, 42 + 2 * i, y1);
        if (i < 7) { y0 = cmul(y0, w16c); y1 = cmul(y1, w16c); }
      }
    }
    FENCE();
#pragma unroll
    for (int i = 0; i < 2; ++i) {
      const bool last = wtau[i] == 10;
      *(LAS u32x4*)wi1[i] = u32x4{wg[i].x, wu[i].x, wg[i].y, wu[i].y};
      *(LAS u32x4*)(wi1[i] + 16) = u32x4{wg[i].z, wu[i].z, last ? 0x00003F80u : wg[i].w, last ? 0u : wu[i].w};
      *(LAS u32x4*)(wi2 + i * 256) = dv[i];
    }
    vm_wait<0>();
    SYNC_LDS();
    flag_st<E_P0>(c, c.sflag);
    STAMP(1);
    {
      LAS char* ri1 = lds + L_IMG1 + 3 * w * 1056 + lane * 8;
      LAS char* ri2 = lds + L_IMG2 + 2 * w * 1056 + lane * 8;
      const u32 sgu = OFF_WGU + ((cbR * 11 + 3 * w) * 16 + sR) * 1024, sgu2 = w == 3 ? 0x7f000000u : sgu + 32768;
      const u32 swd = b < 176 ? OFF_WD + ((dq * 8 + 2 * w) * KS2 + s2) * 1024 : 0x7f000000u;
      u32x4 o[5];
#pragma unroll
      for (int i = 0; i < 3; ++i) o[i] = tr_frag(ri1 + i * 1056);
#pragma unroll
      for (int i = 0; i < 2; ++i) o[3 + i] = tr_frag(ri2 + i * 1056);
      PIN();
      st16(c.rs, lane * 16, sgu, o[0]); st16(c.rs, lane * 16, sgu + 16384, o[1]); st16(c.rs, lane * 16, sgu2, o[2]);
      st16(c.rs, lane * 16, swd, o[3]); st16(c.rs, lane * 16, swd + KS2 * 1024, o[4]);
    }
    STAMP(2);
    {
      f32x2 e[8];
      e[1] = wp[0]; e[2] = cmul(e[1], e[1]); e[3] = cmul(e[2], e[1]); e[4] = cmul(e[2], e[2]);
      e[5] = cmul(e[4], e[1]); e[6] = cmul(e[4], e[2]); e[7] = cmul(e[4], e[3]);
      const f32x2 w256 = cmul(wp[2], wp[2]);
      f32x2 ta = {16384.f, 0.f}, tc = wp[2] * 16384.f;
      put2(0, ta, 8, tc);
#pragma unroll
      for (int i = 1; i < 8; ++i) put2(i, e[i] * 16384.f, 8 + i, cmul(tc, e[i]));
      ta = w256 * 16384.f; tc = cmul(tc, w256);
      put2(16, ta, 20, tc);
#pragma unroll
      for (int i = 1; i < 4; ++i) put2(16 + i, cmul(ta, e[2 * i]), 20 + i, cmul(tc, e[2 * i]));
#pragma unroll
      for (int i = 0; i < 2; ++i) {
        ta = cmul(ta, w256); tc = cmul(tc, w256);
        put2(24 + 4 * i, ta, 26 + 4 * i, tc);
        put2(25 + 4 * i, cmul(ta, e[4]), 27 + 4 * i, cmul(tc, e[4]));
      }
#pragma unroll
      for (int i = 0; i < 4; ++i) {
        ta = cmul(ta, w256); tc = cmul(tc, w256);
        put2(32 + 2 * i, ta, 33 + 2 * i, tc);
      }
      ta = cmul(ta, w256);
      put2(40, ta, 40, ta);
    }
    vm_wait<0>();
    SYNC_LDS();
    peek<false, E_P0>(c, c.speek);
    flag_st<E_W>(c, c.sflag);
    {
      LAS char* tr = lds + L_TB + 2 * kch * TB_WAVE + l15 * TB_ROW + q * 16;
      LAS char* tr0 = tr + khalf * 32 * TB_ROW;
      f32x4 kk[2];
      sfor<2>([&](auto H) {
        u32x4 fa[4][2], fb[4];
#pragma unroll
        for (int G = 0; G < 4; ++G) {
          fb[G] = lds16(tr + H * TB_WAVE + 41 * TB_ROW + G * 64);
          fa[G][0] = lds16(tr0 + H * TB_WAVE + G * 64);
          fa[G][1] = lds16(tr + H * TB_WAVE + 16 * TB_ROW + G * 64);
        }
        sfor<8>([&](auto N) { mm_f16<(H == 0 && N < 2)>(kk[N & 1], fa[N >> 1][N & 1], fb[N >> 1]); });
      });
      const float ks = ldexpf(2.f * dt, -14 - ce);
      asm volatile("s_nop 7" : "+v"(kk[0]), "+v"(kk[1]));
      kk[0] *= ks; kk[1] *= ks;
      STAMP(40);
      if (khalf) {
#pragma unroll
        for (int j = 0; j < 4; ++j) *(LAS float*)(ta0 + j * 64) = kk[0][j];
      } else {
        *(LAS f32x4*)ta0 = kk[0];
        *(LAS f32x2*)ta1 = kk[1].xy; *(LAS f32x2*)ta1b = kk[1].zw;
      }
    }
    poll_edge<false, 1, E_P0>(c, c.speek);
    SYNC_LDS();
    STAMP(41);
    conv_load<0>(c, 0, inA); conv_load<0>(c, 1, inB);
    PIN();
    {
      f32x2 V[11];
#pragma unroll
      for (int i = 0; i < 11; ++i) V[i] = f32x2{*(LAS float*)ci[i], *(LAS float*)(ci[i] + KT_CH)};
      PIN();
#pragma unroll
      for (int ch = 0; ch < 2; ++ch)
#pragma unroll
        for (int s = 0; s < 4; ++s) {
          const u32x4 o = {pk_bf16(V[3 - s][ch], V[4 - s][ch]), pk_bf16(V[5 - s][ch], V[6 - s][ch]), pk_bf16(V[7 - s][ch], V[8 - s][ch]), pk_bf16(V[9 - s][ch], V[10 - s][ch])};
          *(LAS u32x4*)(lds + L_KC + ch * KC_CH + s * KC_COPY + tid * 16) = o;
        }
    }
  }
  STAMP(3);

  conv_phase<0>(c, cf, dsk, inA, inB);

#pragma unroll 1
  for (int layer = 0; layer < 2; ++layer) {
    refresh(c, w0);
    STAMP(10 + layer * 20);
    G1 g;
    g.layer = layer;
    g.xa = xa_ptr(c, layer);
    g.res = res_ptr(c, layer);
    g.ring[0] = c.lds + L_RING + c.lane * 8;
    g.ring[1] = g.ring[0] + 65536;
    asm volatile("" : "+v"(g.ring[0]), "+v"(g.ring[1]));
    g.ldsw = c.lds0 + L_RING + c.w * 4096;
    g.v16 = c.lane * 16;
    g.lanes = (u64)(long long)(layer - 1) | 1ull;
#pragma unroll
    for (int i = 0; i < 6; ++i) {
      g.wd[i][0] = wd_ptr(c, 0) + i * 8192 - (i == 5 ? (c.w >> 1) * 2048 : 0); g.wd[i][1] = wd_ptr(c, 1) + i * 8192 - (i == 5 ? (c.w >> 1) * 2048 : 0);
      asm volatile("" : "+s"(g.wd[i][0]), "+s"(g.wd[i][1]));
    }
    g.vst = c.q * (KS2 * 1024) + c.l15 * 16;
    g.sst = OFF_H + layer * H_BYTES + c.rb * 4 * (KS2 * 1024) + (c.cb * 11 + 3 * c.w) * 256;
    g.hsrc = OFF_H + layer * H_BYTES + (c.rt * 2 + (c.w & 1)) * (KS2 * 1024) + (c.w >> 1) * 4096;
    g.hsrc5 = g.hsrc + 5 * 8192 - (c.w >> 1) * 2048;
    g.ldsr = c.lds0 + L_RES + c.w * 1024;
    g.pk = c.lds + L_ST + c.l15 * 16;
    g.pws = c.lds + L_WS + c.w * 192 + c.q * 16;
    {
      const int dfin = 2 * (c.w & 1) + (c.w >> 1);
      g.rd[0] = c.lds + L_RING + (c.w >> 1) * 8192 + c.lane * 16;
      g.rd[1] = g.rd[0] + 65536;
      g.prs = c.lds + L_RES + dfin * 512 + c.lane * 8;
      g.exw = c.lds + L_RING + c.w * 2048 + c.lane * 16;
      g.exr = c.lds + L_RING + (c.w ^ 2) * 2048 + c.lane * 16;
      g.sts = c.lds + L_ST + 1024 + c.lane * 16;
      g.vo = layer == 0 ? (2 * c.q + (c.lane & 1)) * 8192 + (c.lane & 2) * 32 + (c.l15 >> 2) * 16 : ((c.q & 1) * 16 + c.l15) * 1024 + (c.q >> 1) * 16;
      g.so = layer == 0 ? OFF_XP + 2097152 + (c.db * 32 + dfin * 8) * 8192 + c.rt * 128 : c.rt * 32768 + c.db * 128 + dfin * 32;
      g.soh = g.so + (layer == 0 ? 4096 : 1048576);
      g.va = c.q == c.w ? (c.l15 & 3) * 4096 + (c.l15 >> 2) * 16 : 0x7f000000u;
      g.sa = OFF_ST3 + (c.rt * 8 + (c.w >> 1) * 4) * 16 + (c.w & 1) * 8;
      g.eoff = layer == 0 ? c.sflag : 0x7f000000u;
      g.fA = c.sflag + layer * 16384; g.fG = c.sflagg + layer * 8192; g.pA = c.speek + layer * 16384; g.pG = c.speekg + layer * 8192;
    }
    asm volatile("" : "+s"(g.lanes), "+s"(g.xa), "+s"(g.res), "+s"(g.ldsw), "+s"(g.sst), "+s"(g.hsrc), "+s"(g.hsrc5), "+s"(g.ldsr));
    asm volatile("" : "+s"(g.so), "+s"(g.soh), "+s"(g.sa), "+s"(g.eoff), "+s"(g.fA), "+s"(g.fG), "+s"(g.pA), "+s"(g.pG));
    asm volatile("" : "+v"(g.vst), "+v"(g.pk), "+v"(g.v16), "+v"(g.pws), "+v"(g.rd[0]), "+v"(g.rd[1]), "+v"(g.prs), "+v"(g.exw), "+v"(g.exr), "+v"(g.sts), "+v"(g.vo), "+v"(g.va));
    u32x4 Bc[4];
    f32x4 acc[3][4];
    if (layer == 0) vm_wait<19>(); else vm_wait<5>();
    BARRIER(); FENCE();
#pragma unroll
    for (int ct = 0; ct < 4; ++ct) Bc[ct] = tr_frag(g.ring[0] + ct * 4096);
    STAMP(11 + layer * 20);
    g1_main<0>(c, g, acc, Bc);
    if (layer == 0) wsum_part<8, 16>(g.pws, [](auto) {});
    g1_epilogue<0>(c, g, acc, [&]() {
      vm_wait<11>();
      BARRIER(); FENCE();
#pragma unroll
      for (int ct = 0; ct < 4; ++ct) Bc[ct] = tr_frag(g.ring[1] + ct * 4096);
    });
    STAMP(13 + layer * 20);
    g1_main<1>(c, g, acc, Bc);
    f32x4 acc2[4];
    u32x4 Bd[8];
    g1_epilogue<1>(c, g, acc, [&]() {
      vm_wait<6>();
      BARRIER(); FENCE();
      g2_first(g, Bd);
      g2_res<0>(g, acc2);
    });
    STAMP(15 + layer * 20);
    ConvIn nA, nB;
    ConvF c2;
    g2_unit<0>(c, g, p, layer, acc2, Bd, nA);
    sfor<4>([&](auto I) { dma4<HB_OFF + I * 8192, (I + 1) * 16384>(c.rs, g.v16, g.hsrc, g.ldsw); });
    g2_res<1>(g, acc2);
    refresh(c, w0);
    conv_setup(c, 1, c2);
    vm_wait<12>();
    BARRIER(); FENCE();
    g2_first(g, Bd);
    flag_st<E_DOWN>(c, g.eoff);
    g2_unit<1>(c, g, p, layer, acc2, Bd, nA);
    if (layer == 0) { conv_pad(c); conv_phase<1>(c, c2, dsk, nA, nB); }
  }
  if (blockIdx.x == 0 && threadIdx.x == 0) __hip_atomic_store((GAS u32*)(c.ws + OFF_EPOCH), c.epoch, RLX_AGENT);
  asm volatile(".Lmega_tail:");
}

}

extern "C" void kernel_launch(void* const* d_in, const int* in_sizes, int n_in, void* d_out, int out_size, void* d_ws,
                              size_t ws_size, hipStream_t stream) {
  Params p;
  p.x0 = (const u16*)d_in[0];
  p.logA = (const u16*)d_in[1];
  p.Aim = (const u16*)d_in[2];
  p.Cre = (const u16*)d_in[3];
  p.Cim = (const u16*)d_in[4];
  p.logdt = (const u16*)d_in[5];
  p.Dskip = (const u16*)d_in[6];
  p.wg = (const u16*)d_in[8];
  p.wu = (const u16*)d_in[9];
  p.wd = (const u16*)d_in[10];
  p.out = (u16*)d_out;
  p.ws = (char*)d_ws;
  mega_kernel<<<NBLK, 256, 0, stream>>>(p);
}
```

```cpp
#include <hip/hip_runtime.h>
#include <hip/hip_bf16.h>
#include <utility>

typedef unsigned int u32;
typedef unsigned short u16;
typedef unsigned long long u64;
typedef u32 u32x4 __attribute__((ext_vector_type(4)));
typedef u32 u32x2 __attribute__((ext_vector_type(2)));
typedef float f32x4 __attribute__((ext_vector_type(4)));
typedef float f32x2 __attribute__((ext_vector_type(2)));
typedef short s16x4 __attribute__((ext_vector_type(4)));
typedef __bf16 bf16x8 __attribute__((ext_vector_type(8)));
typedef __bf16 bf16x2 __attribute__((ext_vector_type(2)));
typedef _Float16 f16x8 __attribute__((ext_vector_type(8)));
typedef _Float16 f16x2 __attribute__((ext_vector_type(2)));
#define LAS __attribute__((address_space(3)))
#define GAS __attribute__((address_space(1)))
#define RLX_AGENT __ATOMIC_RELAXED, __HIP_MEMORY_SCOPE_AGENT
#define DEV __device__ __forceinline__

#define CODE_PREFETCH 1
#define MAP_ROWS 0
#define H_REFETCH 0
#define PK_B 28
#define PK_A 4
#define FL_B 8
#define STAMP_WAVE 0

namespace {

constexpr int SEQ = 2048, DM = 512, NS = 128, DF = 1376;
constexpr int NBLK = 256;
constexpr int KS2 = 44;

constexpr u32 OFF_EPOCH = 0;
constexpr u32 OFF_FLAG = 4096;
constexpr u32 OFF_GFLAG = OFF_FLAG + 8 * 8192;
constexpr u32 OFF_ST1 = OFF_GFLAG + 8192;
constexpr u32 OFF_ST3 = OFF_ST1 + 16384;
constexpr u32 OFF_WGU = OFF_ST3 + 32768;
constexpr u32 OFF_WD = OFF_WGU + 16 * 11 * 16 * 1024;
constexpr u32 OFF_XC = OFF_WD + 32 * KS2 * 1024;
constexpr u32 OFF_XP = OFF_XC + 2 * 2097152;
constexpr u32 OFF_H = OFF_XP + 2 * 2097152;
constexpr u32 H_BYTES = 128 * KS2 * 1024;
constexpr u32 OFF_DBG = OFF_H + 2 * H_BYTES;
constexpr u32 WS_BYTES = OFF_DBG + 256 * 64 * 16;
constexpr int E_P0 = 0, E_W = 7, E_CONV = 1  , E_DOWN = 3  ;

constexpr int KC_COPY = 4160, KC_CH = 4 * KC_COPY;
constexpr int L_KC = 0;
constexpr int L_RING = 2 * KC_CH;
constexpr int L_RES = L_RING + 7 * 16384;
constexpr int L_ST = L_RES + 8192;
constexpr int L_WS = L_ST + 2048;
constexpr int L_VD = L_WS + 768;
constexpr int LDS_BYTES = 163840;
constexpr int L_U = L_RING + 5 * 16384;
constexpr int U_CH = 4608;
constexpr int L_P = L_RING + 6 * 16384;
constexpr int TB_ROW = 272, TB_WAVE = 57 * TB_ROW;
constexpr int L_TB = L_RING;
constexpr int L_IMG1 = L_TB + 4 * TB_WAVE;
constexpr int L_KT = L_IMG1 + 11 * 1056 + 16;
constexpr int KT_CH = 772 * 4;
constexpr int L_IMG2 = L_U + 2 * U_CH;
constexpr int L_XT = L_IMG2 + 8 * 1056;
constexpr int L_DUMMY = L_XT + 32 * 272;
static_assert(L_KT + 2 * KT_CH <= L_U && L_DUMMY + 4128 <= L_RES && L_VD + 16 <= LDS_BYTES, "lds");

struct Params {
  const u16 *x0, *logA, *Aim, *Cre, *Cim, *logdt, *Dskip, *wg, *wu, *wd;
  u16* out;
  char* ws;
};

DEV float bf_lo(u32 v) { return __builtin_bit_cast(float, v << 16); }
DEV float bf_hi(u32 v) { return __builtin_bit_cast(float, v & 0xffff0000u); }
DEV float bf2f(u16 v) { return __builtin_bit_cast(float, (u32)v << 16); }
DEV u32 pk_bf16(float lo, float hi) {
  f32x2 f = {lo, hi};
  return __builtin_bit_cast(u32, __builtin_convertvector(f, bf16x2));
}
DEV float rnd_bf16(float v) { return bf_lo(pk_bf16(v, 0.f)); }
DEV u32x2 tr_read(LAS char* p) {
  return __builtin_bit_cast(u32x2, __builtin_amdgcn_ds_read_tr16_b64_v4i16((LAS s16x4*)p));
}
DEV u32x4 tr_frag(LAS char* p) {
  u32x2 a = tr_read(p), b = tr_read(p + 512);
  u32x4 r = {a.x, a.y, b.x, b.y};
  return r;
}
DEV u32x4 lds16(LAS char* p) { return *(LAS u32x4*)p; }
template <int CTRL> DEV float dpp_f(float v) {
  return __builtin_bit_cast(float, __builtin_amdgcn_mov_dpp(__builtin_bit_cast(int, v), CTRL, 0xf, 0xf, true));
}
template <int CTRL> DEV u32 dpp_u(u32 v) { return (u32)__builtin_amdgcn_mov_dpp((int)v, CTRL, 0xf, 0xf, true); }
DEV void swap16(u32& a, u32& b) { auto r = __builtin_amdgcn_permlane16_swap(a, b, false, false); a = r[0]; b = r[1]; }
DEV void swap32(u32& a, u32& b) { auto r = __builtin_amdgcn_permlane32_swap(a, b, false, false); a = r[0]; b = r[1]; }
DEV void swap16f(float& a, float& b) { u32 x = __builtin_bit_cast(u32, a), y = __builtin_bit_cast(u32, b); swap16(x, y); a = __builtin_bit_cast(float, x); b = __builtin_bit_cast(float, y); }
DEV void swap32f(float& a, float& b) { u32 x = __builtin_bit_cast(u32, a), y = __builtin_bit_cast(u32, b); swap32(x, y); a = __builtin_bit_cast(float, x); b = __builtin_bit_cast(float, y); }
DEV float wave_sum(float v) {
  v += dpp_f<0xB1>(v);
  v += dpp_f<0x4E>(v);
  v += dpp_f<0x141>(v);
  v += dpp_f<0x140>(v);
  float a = v, b = v;
  swap16f(a, b); v = a + b;
  a = v; b = v;
  swap32f(a, b); v = a + b;
  return v;
}
DEV float wave_max(float v) {
  v = fmaxf(v, dpp_f<0xB1>(v)); v = fmaxf(v, dpp_f<0x4E>(v)); v = fmaxf(v, dpp_f<0x141>(v)); v = fmaxf(v, dpp_f<0x140>(v));
  float a = v, b = v;
  swap16f(a, b); v = fmaxf(a, b);
  a = v; b = v;
  swap32f(a, b); v = fmaxf(a, b);
  return v;
}
DEV float sel(u64 m, float a, float b) { float r; asm("v_cndmask_b32_e64 %0, %1, %2, %3" : "=v"(r) : "v"(a), "v"(b), "s"(m)); return r; }
DEV u32 selu(u64 m, u32 a, u32 b) { u32 r; asm("v_cndmask_b32_e64 %0, %1, %2, %3" : "=v"(r) : "v"(a), "v"(b), "s"(m)); return r; }
constexpr u64 M_B0 = 0xAAAAAAAAAAAAAAAAull, M_B1 = 0xCCCCCCCCCCCCCCCCull, M_B2 = 0xF0F0F0F0F0F0F0F0ull, M_B3 = 0xFF00FF00FF00FF00ull;
DEV float dot2(u32 a, u32 b, float acc) {
  return __builtin_amdgcn_fdot2_f32_bf16(__builtin_bit_cast(bf16x2, a), __builtin_bit_cast(bf16x2, b), acc, false);
}
DEV f32x2 cmul(f32x2 a, f32x2 b) {
  const f32x2 t = a.yy * f32x2{-b.y, b.x};
  return __builtin_elementwise_fma(a.xx, b, t);
}
#define ACLOB "a0","a1","a2","a3","a4","a5","a6","a7","a8","a9","a10","a11","a12","a13","a14","a15","a16","a17","a18","a19","a20","a21","a22","a23","a24","a25","a26","a27","a28","a29","a30","a31","a32","a33","a34","a35","a36","a37","a38","a39","a40","a41","a42","a43","a44","a45","a46","a47","a48","a49","a50","a51","a52","a53","a54","a55","a56","a57","a58","a59","a60","a61","a62","a63","a64","a65","a66","a67","a68","a69","a70","a71","a72","a73","a74","a75","a76","a77","a78","a79","a80","a81","a82","a83","a84","a85","a86","a87","a88","a89","a90","a91","a92","a93","a94","a95","a96","a97","a98","a99","a100","a101","a102","a103","a104","a105","a106","a107","a108","a109","a110","a111","a112","a113","a114","a115","a116","a117","a118","a119","a120","a121","a122","a123","a124","a125","a126","a127","a128","a129","a130","a131","a132","a133","a134","a135","a136","a137","a138","a139","a140","a141","a142","a143","a144","a145","a146","a147","a148","a149","a150","a151","a152","a153","a154","a155","a156","a157","a158","a159","a160","a161","a162","a163","a164","a165","a166","a167","a168","a169","a170","a171","a172","a173","a174","a175","a176","a177","a178","a179","a180","a181","a182","a183","a184","a185","a186","a187","a188","a189","a190","a191","a192","a193","a194","a195","a196","a197","a198","a199","a200","a201","a202","a203","a204","a205","a206","a207","a208","a209","a210","a211","a212","a213","a214","a215","a216","a217","a218","a219","a220","a221","a222","a223","a224","a225","a226","a227","a228","a229","a230","a231","a232","a233","a234","a235","a236","a237","a238","a239","a240","a241","a242","a243","a244","a245","a246","a247","a248","a249","a250","a251","a252","a253","a254","a255"
#define BARRIER() __builtin_amdgcn_s_barrier()
#define FENCE() asm volatile("" ::: "memory")
#define PIN() __builtin_amdgcn_sched_barrier(0)
#define LGKM0() asm volatile("s_waitcnt lgkmcnt(0)" ::: "memory", ACLOB)
#define SYNC_LDS() do { LGKM0(); BARRIER(); FENCE(); } while (0)
template <int N> DEV void vm_wait() { asm volatile("s_waitcnt vmcnt(%0)" ::"i"(N) : "memory", ACLOB); }

typedef __amdgpu_buffer_rsrc_t rsrc_t;
DEV rsrc_t mk_rsrc(const void* p, u32 bytes) { return __builtin_amdgcn_make_buffer_rsrc((void*)p, 0, bytes, 0x00020000); }
DEV u32x4 ld16(rsrc_t r, u32 voff, u32 soff) { return __builtin_amdgcn_raw_buffer_load_b128(r, voff, soff, 16); }
DEV u32x4 ld16p(rsrc_t r, u32 voff, u32 soff) { return __builtin_amdgcn_raw_buffer_load_b128(r, voff, soff, 0); }
template <int AUX = 16> DEV void st16(rsrc_t r, u32 voff, u32 soff, u32x4 v) {
  __builtin_amdgcn_raw_buffer_store_b128(v, r, voff, soff, AUX);
  asm volatile("s_nop 1" ::"v"(v) : "memory");
}

template <int... Is, class F> DEV void sfor_impl(std::integer_sequence<int, Is...>, F&& f) { (f(std::integral_constant<int, Is>{}), ...); }
template <int N, class F> DEV void sfor(F&& f) { sfor_impl(std::make_integer_sequence<int, N>{}, f); }
template <bool FIRST> DEV void mm_vv(f32x4& acc, const u32x4& a, const u32x4& b) {
  if constexpr (FIRST) asm volatile("v_mfma_f32_16x16x32_bf16 %0, %1, %2, 0" : "=&v"(acc) : "v"(a), "v"(b));
  else asm volatile("v_mfma_f32_16x16x32_bf16 %0, %1, %2, %0" : "+v"(acc) : "v"(a), "v"(b));
}
template <bool FIRST> DEV void mm_f16(f32x4& acc, const u32x4& a, const u32x4& b) {
  if constexpr (FIRST) asm volatile("v_mfma_f32_16x16x32_f16 %0, %1, %2, 0" : "=&v"(acc) : "v"(a), "v"(b));
  else asm volatile("v_mfma_f32_16x16x32_f16 %0, %1, %2, %0" : "+v"(acc) : "v"(a), "v"(b));
}
template <bool FIRST, int A> DEV void mm_av(f32x4& acc, const u32x4& b) {
  if constexpr (FIRST) asm volatile("v_mfma_f32_16x16x32_bf16 %0, a[%c2:%c3], %1, 0" : "=&v"(acc) : "v"(b), "i"(A), "i"(A + 3));
  else asm volatile("v_mfma_f32_16x16x32_bf16 %0, a[%c2:%c3], %1, %0" : "+v"(acc) : "v"(b), "i"(A), "i"(A + 3));
}
template <int A, int OFF> DEV void ld16_a(rsrc_t r, u32 voff, u32 soff) {
  asm volatile("buffer_load_dwordx4 a[%c3:%c4], %0, %1, %2 offen offset:%c5 sc1" ::"v"(voff), "s"(r), "s"(soff), "i"(A), "i"(A + 3), "i"(OFF) : "memory");
}
#define MM_DONE4(a) asm volatile("s_nop 7" : "+v"((a)[0]), "+v"((a)[1]), "+v"((a)[2]), "+v"((a)[3]))

template <int G, int L> DEV void dma1(rsrc_t r, u32 voff, u32 soff, u32 lds) {
  u32 t;
  asm volatile("s_add_u32 m0, %4, %6\n\ts_add_u32 %0, %3, %5\n\tbuffer_load_dwordx4 %1, %2, %0 offen sc1 lds"
               : "=&s"(t) : "v"(voff), "s"(r), "s"(soff), "s"(lds), "i"(G), "i"(L) : "memory", "scc");
}
template <int G, int L> DEV void dma4(rsrc_t r, u32 voff, u32 soff, u32 lds) {
  u32 t;
  asm volatile("s_add_u32 m0, %4, %6\n\ts_add_u32 %0, %3, %5\n\tbuffer_load_dwordx4 %1, %2, %0 offen sc1 lds\n\t"
               "buffer_load_dwordx4 %1, %2, %0 offen offset:1024 sc1 lds\n\tbuffer_load_dwordx4 %1, %2, %0 offen offset:2048 sc1 lds\n\t"
               "buffer_load_dwordx4 %1, %2, %0 offen offset:3072 sc1 lds"
               : "=&s"(t) : "v"(voff), "s"(r), "s"(soff), "s"(lds), "i"(G), "i"(L) : "memory", "scc");
}

constexpr u32 SPIN_CAP = 1u << 16;
constexpr int SYNC_WAVE = 2;

struct Ctx {
  char* ws;
  rsrc_t rs;
  LAS char* lds;
  u32 lds0, epoch;
  int b, tid, lane, w, q, l15, cp, cb, rb, db, rt;
  mutable u32 junk;
};

DEV void flag_all(const Ctx& c, int edge, bool on = true) {
  __builtin_amdgcn_raw_buffer_store_b32(c.epoch, c.rs, c.tid == 192 ? 0u : 0x7f000000u, on ? OFF_FLAG + edge * 4096 + (c.b >> 2) * 64 + (c.b & 3) * 4 : 0x7f000000u, 16);
  FENCE();
}
DEV void flag_grp(const Ctx& c, int edge) {
  __builtin_amdgcn_raw_buffer_store_b32(c.epoch, c.rs, c.tid == 192 ? 0u : 0x7f000000u, OFF_FLAG + (8 + edge) * 4096 + c.rb * 256 + (c.cb >> 2) * 64 + (c.cb & 3) * 4, 16);
  FENCE();
}
template <bool GRP, bool ANY = false> DEV void peek(const Ctx& c, int edge) {
  if constexpr (GRP) ld16_a<252, 0>(c.rs, (c.lane & 3) * 64, (ANY || c.w == SYNC_WAVE) ? OFF_FLAG + (8 + edge) * 4096 + (c.rt >> 1) * 256 : 0x7f000000u);
  else ld16_a<252, 0>(c.rs, c.lane * 64, (ANY || c.w == SYNC_WAVE) ? OFF_FLAG + edge * 4096 : 0x7f000000u);
}
template <int N> DEV u64 flags_miss(const Ctx& c) {
  u32 f0, f1, f2, f3;
  asm volatile("s_waitcnt vmcnt(%4)\n\tv_accvgpr_read_b32 %0, a252\n\tv_accvgpr_read_b32 %1, a253\n\tv_accvgpr_read_b32 %2, a254\n\tv_accvgpr_read_b32 %3, a255"
               : "=v"(f0), "=v"(f1), "=v"(f2), "=v"(f3) : "i"(N) : "memory");
  const u32 d = ((f0 ^ c.epoch) | (f1 ^ c.epoch)) | ((f2 ^ c.epoch) | (f3 ^ c.epoch));
  return __builtin_amdgcn_ballot_w64(d != 0u);
}
template <bool GRP> DEV void spin(const Ctx& c, int edge) {
#pragma unroll 1
  for (u32 sp = 0; sp < SPIN_CAP; ++sp) {
    peek<GRP, true>(c, edge);
    if (flags_miss<0>(c) == 0ull) break;
    __builtin_amdgcn_s_sleep(4);
  }
}
template <bool GRP, int N> DEV void check_edge(const Ctx& c, int edge) { if (flags_miss<N>(c)) spin<GRP>(c, edge); FENCE(); }
template <bool GRP, int N> DEV void poll_edge(const Ctx& c, int edge) {
  if (c.w == SYNC_WAVE) { if constexpr (N >= 0) check_edge<GRP, N>(c, edge); else spin<GRP>(c, edge); }
  FENCE();
}
template <int N> DEV void post_verdict(const Ctx& c, int k) {
  if (c.w == SYNC_WAVE) { const u64 m = flags_miss<N>(c); *(LAS u32*)(c.lds + L_VD + 4 * k) = (u32)m | (u32)(m >> 32); }
}
DEV u64 read_verdict(const Ctx& c, int k) { return (u64)(u32)__builtin_amdgcn_readfirstlane(*(LAS u32*)(c.lds + L_VD + 4 * k)); }

#define STAMP(i) do { } while (0)

DEV void refresh(Ctx& c, int w0) {
  int tid = threadIdx.x, b = blockIdx.x, w = w0;
  asm volatile("" : "+v"(tid)); asm volatile("" : "+s"(b)); asm volatile("" : "+s"(w));
  c.tid = tid; c.b = b; c.w = w; c.lane = tid & 63; c.q = c.lane >> 4; c.l15 = tid & 15;
  const int xcd = b & 7, rr = b >> 3;
  c.cp = xcd * 32 + rr;
  c.cb = (xcd & 3) * 4 + (rr & 3); c.rb = (xcd >> 2) * 8 + (rr >> 2);
  c.db = (xcd & 1) * 4 + (rr & 3); c.rt = (xcd >> 1) * 8 + (rr >> 2);
}

constexpr int AW(int tile, int s) { return (tile * 16 + s) * 4; }
template <int N> DEV void ld_wgu(const Ctx& c) {
  constexpr int tl = N % 3, s = N / 3;
  const u32 v = c.lane * 16 + ((tl == 2 && c.w == 3) ? 0x40000000u : 0u);
  ld16_a<AW(tl, s), (s & 3) * 1024>(c.rs, v, OFF_WGU + (c.cb * 11 + 3 * c.w + tl) * 16384 + (s >> 2) * 4096);
}
constexpr int NWDA = 15, VWD = 140;
DEV u32 wd_ptr(const Ctx& c, int other) { return OFF_WD + (c.db * 4 + 2 * (c.w & 1) + ((c.w >> 1) ^ other)) * (KS2 * 1024) + (c.w >> 1) * 4096; }
template <int N> DEV void ld_wd(const Ctx& c, u32 v16, u32 wd0, u32 wd1) {
  constexpr int j = N >> 1, G = (j >> 2) * 8192 + (j & 3) * 1024;
  u32 t;
  if constexpr (N < NWDA)
    asm volatile("s_add_u32 %0, %3, %4\n\tbuffer_load_dwordx4 a[%c5:%c6], %1, %2, %0 offen sc1"
                 : "=&s"(t) : "v"(v16), "s"(c.rs), "s"((N & 1) ? wd1 : wd0), "i"(G), "i"(192 + 4 * N), "i"(195 + 4 * N) : "memory", "scc");
  else
    asm volatile("s_add_u32 %0, %3, %4\n\tbuffer_load_dwordx4 v[%c5:%c6], %1, %2, %0 offen sc1"
                 : "=&s"(t) : "v"(v16), "s"(c.rs), "s"((N & 1) ? wd1 : wd0), "i"(G), "i"(VWD + 4 * (N - NWDA)), "i"(VWD + 3 + 4 * (N - NWDA)) : "memory", "scc");
}
template <int V> DEV void mm_wv(f32x4& acc, const u32x4& b) {
  asm volatile("v_mfma_f32_16x16x32_bf16 %0, v[%c2:%c3], %1, %0" : "+v"(acc) : "v"(b), "i"(V), "i"(V + 3));
}

struct ConvIn { u32x4 x; u32x4 st[4]; };
template <int LAYER> DEV void conv_load(const Ctx& c, int half, ConvIn& in, u32 skip = 0) {
  const u32 v = c.tid * 16;
  if constexpr (LAYER == 0) {
    asm volatile("buffer_load_dwordx4 %0, %3, %4, %5 offen sc1\n\tbuffer_load_dwordx4 %1, %3, %4, %6 offen sc1\n\tbuffer_load_dwordx4 %2, %3, %4, %7 offen sc1"
                 : "=&v"(in.x), "=&v"(in.st[0]), "=&v"(in.st[1])
                 : "v"(v), "s"(c.rs), "s"(OFF_XP + c.cp * 8192 + half * 4096), "s"(OFF_ST1 + half * 8192), "s"(OFF_ST1 + half * 8192 + 4096)
                 : "memory");
  } else {
    const u32 so = OFF_ST3 + half * 16384;
    asm volatile("s_cmp_lg_u32 %12, 0\n\ts_cbranch_scc1 1f\n\t"
                 "buffer_load_dwordx4 %1, %5, %6, %8 offen sc1\n\tbuffer_load_dwordx4 %2, %5, %6, %9 offen sc1\n\t"
                 "buffer_load_dwordx4 %3, %5, %6, %10 offen sc1\n\tbuffer_load_dwordx4 %4, %5, %6, %11 offen sc1\n\tbuffer_load_dwordx4 %0, %5, %6, %7 offen sc1\n1:"
                 : "=&v"(in.x), "=&v"(in.st[0]), "=&v"(in.st[1]), "=&v"(in.st[2]), "=&v"(in.st[3])
                 : "v"(v), "s"(c.rs), "s"(OFF_XP + 2097152 + c.cp * 8192 + half * 4096), "s"(so), "s"(so + 4096), "s"(so + 8192), "s"(so + 12288), "s"(skip)
                 : "memory", "scc");
  }
}
template <int LAYER, int N> DEV void conv_wait(ConvIn& in) {
  if constexpr (LAYER == 0) asm volatile("s_waitcnt vmcnt(%3)" : "+v"(in.x), "+v"(in.st[0]), "+v"(in.st[1]) : "i"(N) : "memory");
  else asm volatile("s_waitcnt vmcnt(%5)" : "+v"(in.x), "+v"(in.st[0]), "+v"(in.st[1]), "+v"(in.st[2]), "+v"(in.st[3]) : "i"(N) : "memory");
}
template <int LAYER> DEV void conv_norm(LAS char* pu, const ConvIn& in, u32 dsk, f32x4 (&base)[2]) {
  f32x2 st[4];
  if (LAYER == 0) {
#pragma unroll
    for (int i = 0; i < 2; ++i) { const f32x4 v = __builtin_bit_cast(f32x4, in.st[i]); st[2 * i] = v.xy; st[2 * i + 1] = v.zw; }
  } else {
#pragma unroll
    for (int i = 0; i < 4; ++i) {
      const u32x2 lo = {in.st[i].x, in.st[i].y}, hi = {in.st[i].z, in.st[i].w};
      st[i] = f32x2{(float)__builtin_bit_cast(double, lo), (float)__builtin_bit_cast(double, hi)};
    }
  }
  const f32x2 dd = {bf_lo(dsk), bf_hi(dsk)};
  f32x2 u[4], x[4];
#pragma unroll
  for (int i = 0; i < 4; ++i) {
    const f32x2 me = __builtin_elementwise_fma(st[i], f32x2{-1.f / DM, 1.f / DM}, f32x2{0.f, 1e-5f});
    const float r = __builtin_amdgcn_rsqf(fmaxf(__builtin_fmaf(-me.x, me.x, me.y), 1e-5f));
    x[i] = f32x2{bf_lo(in.x[i]), bf_hi(in.x[i])};
    u[i] = (x[i] + me.x) * r;
  }
  *(LAS u32x2*)pu = u32x2{pk_bf16(u[0].x, u[1].x), pk_bf16(u[2].x, u[3].x)};
  *(LAS u32x2*)(pu + U_CH) = u32x2{pk_bf16(u[0].y, u[1].y), pk_bf16(u[2].y, u[3].y)};
#pragma unroll
  for (int i = 0; i < 4; ++i) { const f32x2 bs = __builtin_elementwise_fma(u[i], dd, x[i]); base[0][i] = bs.x; base[1][i] = bs.y; }
  asm volatile("" : "+v"(base[0]), "+v"(base[1]));
}
template <int H> DEV void lds8h(u32x4& d, LAS char* p) {
  const u32x2 r = *(LAS u32x2*)(p + 8 * H);
  if constexpr (H == 0) { d.x = r.x; d.y = r.y; } else { d.z = r.x; d.w = r.y; }
}
DEV void ldsA(u32x4& d, LAS char* p) { lds8h<0>(d, p); PIN(); lds8h<1>(d, p); PIN(); }
constexpr int tri_j(int m) { return m < 4 ? 0 : (m < 7 ? 1 : (m < 9 ? 2 : 3)); }
constexpr int tri_i(int m) { return m < 4 ? m : (m < 7 ? m - 3 : (m < 9 ? m - 5 : 3)); }
template <int B0, bool ZERO, class F>
DEV void conv_tri(LAS char* pa, LAS char* pb, u32x4 (&Ac)[4], f32x4 (&acc)[4], F&& side) {
  u32x4 Bc[4];
#pragma unroll
  for (int j = 0; j < 4; ++j) Bc[j] = lds16(pb + (B0 + j) * 512);
  PIN();
  sfor<4>([&](auto IT) {
    constexpr int it = IT;
    u32x4 An[4], Bn[4];
    sfor<10>([&](auto M) {
      constexpr int m = M, i = tri_i(m), j = tri_j(m);
      if constexpr (it < 3) {
        if constexpr (m < 8) lds8h<m & 1>(An[m >> 1], pa - 64 * (it + 1) + (7 - (m >> 1)) * 512);
        if constexpr (m == 0 || m == 5) { PIN(); Bn[m / 5] = lds16(pb - 64 * (it + 1) + (B0 + m / 5) * 512); }
        if constexpr (m >= 8) Bn[m - 6] = lds16(pb - 64 * (it + 1) + (B0 + m - 6) * 512);
      }
      side(std::integral_constant<int, it * 10 + m>{});
      mm_vv<ZERO && it == 0 && j == 0>(acc[i], Ac[i - j], Bc[j]);
      PIN();
    });
    if constexpr (it < 3) {
#pragma unroll
      for (int j = 0; j < 4; ++j) { Ac[j] = An[j]; Bc[j] = Bn[j]; }
    }
  });
  MM_DONE4(acc);
}
template <class F>
DEV void conv_cross(LAS char* pa, LAS char* pb, f32x4 (&acc)[4], u32x4 (&At)[4], F&& side) {
  u32x4 Ac[7], Bc[4];
  Bc[0] = lds16(pb);
#pragma unroll
  for (int k = 0; k < 4; ++k) ldsA(Ac[3 + k], pa + (3 - k) * 512);
#pragma unroll
  for (int k = 1; k < 4; ++k) { Bc[k] = lds16(pb + k * 512); ldsA(Ac[3 - k], pa + (3 + k) * 512); }
  sfor<4>([&](auto IT) {
    constexpr int it = IT;
    u32x4 An[7], Bn[4];
    sfor<16>([&](auto M) {
      constexpr int m = M, j = m >> 2, i = m & 3;
      if constexpr (it < 3) {
        if constexpr (m < 14) lds8h<m & 1>(An[6 - (m >> 1)], pa - 64 * (it + 1) + (m >> 1) * 512);
        if constexpr (m == 0 || m == 8) { PIN(); Bn[m / 8] = lds16(pb - 64 * (it + 1) + (m / 8) * 512); }
        if constexpr (m >= 14) Bn[m - 12] = lds16(pb - 64 * (it + 1) + (m - 12) * 512);
      } else if constexpr (m < 8) {
        lds8h<m & 1>(At[m >> 1], pa + (7 - (m >> 1)) * 512);
      }
      side(std::integral_constant<int, it * 16 + m>{});
      mm_vv<it == 0 && j == 0>(acc[i], Ac[3 + i - j], Bc[j]);
      PIN();
    });
    if constexpr (it < 3) {
#pragma unroll
      for (int k = 0; k < 7; ++k) Ac[k] = An[k];
#pragma unroll
      for (int k = 0; k < 4; ++k) Bc[k] = Bn[k];
    }
  });
}
struct ConvF { LAS char *pa, *pb, *pu, *pw, *pr; u32 vo, so, xa, ldsw, v16; f32x4 a[2], b[2]; };
DEV u32 xa_ptr(const Ctx& c, int layer) { return OFF_XC + layer * 2097152 + (c.rb * 4 + c.w) * 16384; }
DEV void conv_setup(const Ctx& c, int layer, ConvF& r) {
  const int qq = c.tid & 3, ii = (c.tid >> 2) & 15, kch = c.w >> 1, khalf = c.w & 1;
  r.pa = c.lds + L_KC + kch * KC_CH + (c.l15 & 3) * KC_COPY + 2 * (2044 - 16 * (8 * khalf + 1) - (c.l15 & 12) + 8 * c.q) - 7 * 512 - 192;
  r.pb = c.lds + L_U + kch * U_CH + 2 * (256 + 16 * (c.l15 - 8 * khalf - 1) + 8 * c.q) - 192;
  r.pu = c.lds + L_U + 512 + c.tid * 8;
  r.pw = c.lds + L_P + c.w * 4096 + (c.q * 16 + (c.l15 ^ (c.q * 4))) * 16;
  r.pr = c.lds + L_P + c.w * 1024 + (qq * 16 + (ii ^ (qq * 4))) * 16;
  r.vo = (c.tid >> 2) * 16384 + (c.tid & 1) * 32 + (c.tid & 2) * 8;
  r.so = OFF_XC + layer * 2097152 + c.cp * 64;
  r.xa = xa_ptr(c, layer); r.ldsw = c.lds0 + L_RING + c.w * 4096; r.v16 = c.lane * 16;
  u32 z = 0u;
  asm volatile("" : "+v"(r.pa), "+v"(r.pb), "+v"(r.pu), "+v"(r.pw), "+v"(r.pr), "+v"(r.vo), "+v"(r.v16), "+v"(z));
  asm volatile("" : "+s"(r.so), "+s"(r.xa), "+s"(r.ldsw));
  *(LAS u32*)(c.lds + L_U + (c.tid >> 7) * U_CH + (c.tid & 127) * 4) = z;
}
DEV void conv_finish2(ConvF& r) {
#pragma unroll
  for (int ch = 0; ch < 2; ++ch) { r.a[ch] = *(LAS f32x4*)(r.pr + ch * 8192); r.b[ch] = *(LAS f32x4*)(r.pr + ch * 8192 + 4096); }
}
DEV void conv_finish3(const Ctx& c, int half, const ConvF& r, const f32x4 (&base)[2]) {
  u32x2 o[2];
#pragma unroll
  for (int ch = 0; ch < 2; ++ch) {
    const f32x4 t = (r.a[ch] + base[ch]) + r.b[ch];
    o[ch].x = pk_bf16(t.x, t.y); o[ch].y = pk_bf16(t.z, t.w);
  }
  const u32 r0 = dpp_u<0xB1>(selu(M_B0, o[1].x, o[0].x)), r1 = dpp_u<0xB1>(selu(M_B0, o[1].y, o[0].y));
  const u32x4 v = {selu(M_B0, o[0].x, r0), selu(M_B0, o[0].y, r1), selu(M_B0, r0, o[1].x), selu(M_B0, r1, o[1].y)};
  st16(c.rs, r.vo, r.so + half * 1048576, v);
}
DEV u32 res_ptr(const Ctx& c, int layer) { return OFF_XC + layer * 2097152 + (c.rt * 2 + (c.w >> 1)) * 16384 + c.db * 2048 + (c.w & 1) * 1024; }

template <int S0, int S1> DEV void wsum_part(LAS char* pws) {
  f32x4 ws[3];
  u32x4 ones = {0x3F803F80u, 0x3F803F80u, 0x3F803F80u, 0x3F803F80u};
  if constexpr (S0 > 0) {
#pragma unroll
    for (int i = 0; i < 3; ++i) ws[i] = *(LAS f32x4*)(pws + i * 64);
  }
  asm volatile("s_nop 1" : "+v"(ones));
  sfor<3 * (S1 - S0)>([&](auto N) { mm_av<(S0 == 0 && N < 3), AW(N % 3, S0 + N / 3)>(ws[N % 3], ones); });
  asm volatile("s_nop 7" : "+v"(ws[0]), "+v"(ws[1]), "+v"(ws[2]));
#pragma unroll
  for (int i = 0; i < 3; ++i) *(LAS f32x4*)(pws + i * 64) = ws[i];
}
template <int LAYER> DEV void conv_phase(Ctx& c, ConvF& cf, u32 dsk, ConvIn& inA, ConvIn& inB) {
  LAS char* const pa = cf.pa + 192;
  LAS char* const pb = cf.pb + 192;
  f32x4 baseA[2], baseB[2];
  u32x4 Ac[4];
  if constexpr (LAYER == 1) {
#pragma unroll
    for (int j = 0; j < 4; ++j) ldsA(Ac[j], pa + (7 - j) * 512);
  }
  conv_wait<LAYER, LAYER == 0 ? 4 : 2>(inA);
  STAMP(4 + LAYER * 20);
  conv_norm<LAYER>(cf.pu, inA, dsk, baseA);
  if constexpr (LAYER == 0) poll_edge<false, 0>(c, E_W);
  SYNC_LDS();
  if constexpr (LAYER == 0) {
#pragma unroll
    for (int j = 0; j < 4; ++j) ldsA(Ac[j], pa + (7 - j) * 512);
    ld_wgu<0>(c);
  }
  STAMP(5 + LAYER * 20);
  f32x4 accA[4], acc[4];
  conv_tri<0, true>(pa, pb, Ac, accA, [&](auto K) {
    if constexpr (LAYER == 0 && K % 6 == 1) ld_wgu<1 + K / 6>(c);
    if constexpr (LAYER == 1 && K == FL_B) { vm_wait<0>(); BARRIER(); flag_all(c, E_DOWN + 1); }
  });
  STAMP(6 + LAYER * 20);
  conv_cross(pa, pb, acc, Ac, [&](auto K) {
    if constexpr (K < 4) *(LAS f32x4*)(cf.pw + K * 1024) = accA[K];
    if constexpr (K == 8) { SYNC_LDS(); conv_finish2(cf); }
    if constexpr (K == 13) conv_finish3(c, 0, cf, baseA);
    if constexpr (LAYER == 0) {
      if constexpr (K % 5 == 0) ld_wgu<8 + K / 5>(c);
    } else {
      if constexpr (K == PK_B) peek<false>(c, E_DOWN + 1);
    }
  });
  if constexpr (LAYER == 1) poll_edge<false, 0>(c, E_DOWN + 1);
  vm_wait<LAYER == 0 ? 10 : 1>();
  BARRIER(); FENCE();
  if constexpr (LAYER == 1) conv_load<1>(c, 1, inB);
  flag_all(c, E_CONV + 4 * LAYER);
  STAMP(7 + LAYER * 20);
  if constexpr (LAYER == 0) sfor<3>([&](auto N) { ld_wgu<21 + N>(c); });
  conv_wait<LAYER, LAYER == 0 ? 14 : 1>(inB);
  conv_norm<LAYER>(cf.pu + 2048, inB, dsk, baseB);
  SYNC_LDS();
  STAMP(8 + LAYER * 20);
  conv_tri<4, false>(pa, pb, Ac, acc, [&](auto K) {
    if constexpr (LAYER == 0) { if constexpr (K % 5 == 0) ld_wgu<24 + K / 5>(c); }
    else if constexpr (K == PK_A) peek<false>(c, E_CONV + 4);
  });
#pragma unroll
  for (int i = 0; i < 4; ++i) *(LAS f32x4*)(cf.pw + i * 1024) = acc[i];
  if constexpr (LAYER == 0) sfor<3>([&](auto N) { ld_wgu<32 + N>(c); });
  else poll_edge<false, 0>(c, E_CONV + 4);
  SYNC_LDS();
  conv_finish2(cf);
  if constexpr (LAYER == 1) dma4<0, 0>(c.rs, cf.v16, cf.xa, cf.ldsw);
  conv_finish3(c, 1, cf, baseB);
  STAMP(9 + LAYER * 20);
  if constexpr (LAYER == 0) {
    peek<false>(c, E_CONV);
    sfor<6>([&](auto N) { ld_wgu<35 + N>(c); });
    poll_edge<false, 6>(c, E_CONV);
    BARRIER(); FENCE();
    dma4<0, 0>(c.rs, cf.v16, cf.xa, cf.ldsw); dma4<4096, 16384>(c.rs, cf.v16, cf.xa, cf.ldsw);
    sfor<7>([&](auto N) { ld_wgu<41 + N>(c); });
    vm_wait<23>();
    wsum_part<0, 11>(c.lds + L_WS + c.w * 192 + c.q * 16);
  } else {
    dma4<4096, 16384>(c.rs, cf.v16, cf.xa, cf.ldsw);
  }
}

struct G1 {
  u32 xa;
  u32 res, ldsr;
  LAS char* ring[2];
  u32 ldsw;
  u32 v16;
  u32 wd0, wd1, wd0s, wd1s;
  u32 vst, sst;
  u32 hsrc, hsrc5;
  LAS char* pk;
  LAS char* pws;
  int layer;
};
DEV void h_refetch(const Ctx& c, u32 vst, u32 sst) {
}
template <int HALF, bool STATS, int S>
DEV void g1_step(const Ctx& c, const G1& g, f32x4 (&acc)[3][4], u32x4 (&Bc)[4]) {
  if constexpr (S == 3) {
    vm_wait<4>(); BARRIER(); FENCE();
    if constexpr (STATS && HALF == 0) flag_all(c, E_CONV + 4 * g.layer + 1);
    if constexpr (STATS && HALF == 1) flag_grp(c, 2 * g.layer);
    if constexpr (HALF == 1) h_refetch(c, g.vst, g.sst);
  }
  if constexpr (S == 7) { vm_wait<HALF ? 8 : 9>(); BARRIER(); FENCE(); }
  if constexpr (HALF == 0 && S == 11) { vm_wait<14>(); BARRIER(); FENCE(); }
  if constexpr (HALF == 1 && S == 11) { if constexpr (!STATS) poll_edge<true, 6>(c, 2 * g.layer); vm_wait<11>(); BARRIER(); FENCE(); }
  u32x4 Bn[4];
  sfor<12>([&](auto M) {
    constexpr int m = M, ct = m / 3, k = m % 3;
    if constexpr (S < 15 && k < 2) {
      constexpr int slot = (HALF * 4 + (S + 1) / 4) % 7;
      const u32x2 r = tr_read(g.ring[slot >> 2] + (slot & 3) * 16384 + ct * 4096 + ((S + 1) & 3) * 1024 + k * 512);
      if constexpr (k == 0) { Bn[ct].x = r.x; Bn[ct].y = r.y; } else { Bn[ct].z = r.x; Bn[ct].w = r.y; }
    }
    if constexpr (k == 2 && (ct & 1) == 0) {
      constexpr int n = 2 * (S - 1) + (ct >> 1);
      if constexpr (S >= 1 && S <= 4) dma1<HALF * 1048576 + 8192 + n * 1024, ((HALF * 4 + 2 + (n >> 2)) % 7) * 16384 + (n & 3) * 1024>(c.rs, g.v16, g.xa, g.ldsw);
      if constexpr (S == 5 && ct == 0) dma1<HALF * 1048576, HALF * 4096>(c.rs, g.v16, g.res, g.ldsr);
      if constexpr (HALF == 1 && S >= 11) dma1<((n - 20) >> 2) * 8192 + ((n - 20) & 3) * 1024, (((n - 20) >> 2) + 1) * 16384 + ((n - 20) & 3) * 1024>(c.rs, g.v16, g.hsrc, g.ldsw);
      if constexpr (HALF == 0 && n >= 9) ld_wd<n - 9>(c, g.v16, g.wd0, g.wd1);
      if constexpr (HALF == 1 && n >= 9 && n <= 15) ld_wd<24 + n>(c, g.v16, g.wd0, g.wd1);
      if constexpr (HALF == 1 && n >= 16 && n <= 19) ld_wd<24 + n>(c, g.v16, g.wd0s, g.wd1s);
    }
    if constexpr (HALF == 0 && m == 5 && S >= 6 && S <= 12 && (S & 1) == 0) ld_wd<18 + S / 2>(c, g.v16, g.wd0, g.wd1);
    if constexpr (!STATS && HALF == 0 && S == 12 && m == 11) peek<false>(c, E_CONV + 4 * g.layer + 1);
    if constexpr (!STATS && HALF == 1 && S == 7 && m == 11) peek<true>(c, 2 * g.layer);
    if constexpr (STATS && k == 2) mm_vv<S == 0>(acc[k][ct], Bc[ct], Bc[ct]);
    else mm_av<S == 0, AW(k, S)>(acc[k][ct], Bc[ct]);
    PIN();
  });
  if constexpr (S < 15) {
#pragma unroll
    for (int ct = 0; ct < 4; ++ct) Bc[ct] = Bn[ct];
    g1_step<HALF, STATS, S + 1>(c, g, acc, Bc);
  } else {
    MM_DONE4(acc[2]); asm volatile("" : "+v"(acc[0][3]), "+v"(acc[1][3]), "+v"(acc[0][2]), "+v"(acc[1][2]), "+v"(acc[0][1]), "+v"(acc[1][1]));
  }
}
template <int HALF>
DEV void g1_main(const Ctx& c, const G1& g, f32x4 (&acc)[3][4], u32x4 (&Bc)[4]) {
  if (c.w == 3) {
    g1_step<HALF, true, 0>(c, g, acc, Bc);
    float sm[4], sq[4];
#pragma unroll
    for (int ct = 0; ct < 4; ++ct) {
      sm[ct] = acc[1][ct][0];
      sq[ct] = sel(M_B1, sel(M_B0, acc[2][ct][0], acc[2][ct][1]), sel(M_B0, acc[2][ct][2], acc[2][ct][3]));
    }
    swap32f(sm[0], sm[2]); swap32f(sm[1], sm[3]); swap16f(sm[2], sm[3]);
    swap32f(sq[0], sq[2]); swap32f(sq[1], sq[3]); swap16f(sq[0], sq[1]); swap16f(sq[2], sq[3]);
    const float e2 = sel(M_B3, sel(M_B2, sq[0], sq[1]), sel(M_B2, sq[2], sq[3]));
    const float mu = sm[3] * (-1.f / DM);
    const float vr = fmaxf(__builtin_fmaf(-mu, mu, e2 * (1.f / DM)), 0.f) + 1e-5f;
    const float r = __builtin_amdgcn_rsqf(vr);
    const float c1 = (-2.f * 0.7978845608f * 1.44269504f) * r;
    *(LAS f32x4*)(c.lds + L_ST + g.v16) = f32x4{mu, vr, c1, c1 * 0.044715f * (r * r)};
  } else {
    g1_step<HALF, false, 0>(c, g, acc, Bc);
    if constexpr (HALF == 0) poll_edge<false, 6>(c, E_CONV + 4 * g.layer + 1);
  }
}
template <int HALF>
DEV void g1_epilogue(const Ctx& c, const G1& g, const f32x4 (&acc)[3][4]) {
  f32x4 k4[4], wsum[3];
#pragma unroll
  for (int i = 0; i < 3; ++i) wsum[i] = *(LAS f32x4*)(g.pws + i * 64);
  const u32 vst = g.vst, sst = g.sst + HALF * 64 * (KS2 * 1024);
  const u32 sst2 = c.w == 3 ? 0x7f000000u : sst;
  SYNC_LDS();
  STAMP(12 + 2 * HALF + g.layer * 20);
#pragma unroll
  for (int ct = 0; ct < 4; ++ct) k4[ct] = *(LAS f32x4*)(g.pk + ct * 256);
  auto stage = [&](auto N) {
    if constexpr (HALF == 0) {
      if constexpr (N < 8) dma1<1048576 + N * 1024, (4 + (N >> 2)) * 16384 + (N & 3) * 1024>(c.rs, g.v16, g.xa, g.ldsw);
      else if constexpr (N >= 9 && N < 17) ld_wd<16 + N>(c, g.v16, g.wd0, g.wd1);
    } else if constexpr (N % 3 != 2) {
      constexpr int j = 10 + (N / 3) * 2 + N % 3;
      if constexpr (j < 20) dma1<(j >> 2) * 8192 + (j & 3) * 1024, ((j >> 2) + 1) * 16384 + (j & 3) * 1024>(c.rs, g.v16, g.hsrc, g.ldsw);
      else dma1<(j - 20) * 1024, 6 * 16384 + (j - 20) * 1024>(c.rs, g.v16, g.hsrc5, g.ldsw);
    }
    PIN();
  };
  sfor<3>([&](auto I) {
    constexpr int i = I;
    u32 hv[4];
    sfor<2>([&](auto P) {
      constexpr int pi = P;
      constexpr int n0 = 6 * i + 3 * pi;
      f32x2 gg[2], gu[2], ex[2];
      stage(std::integral_constant<int, n0>{});
#pragma unroll
      for (int e = 0; e < 2; ++e) {
        const f32x4 k = k4[2 * pi + e];
        gg[e] = __builtin_elementwise_fma(k.xx, wsum[i].xy, acc[i][2 * pi + e].xy);
        const f32x2 ar = gg[e] * __builtin_elementwise_fma(gg[e] * gg[e], k.ww, k.zz);
        ex[e] = f32x2{__builtin_amdgcn_exp2f(ar.x), __builtin_amdgcn_exp2f(ar.y)};
      }
      PIN();
      stage(std::integral_constant<int, n0 + 1>{});
#pragma unroll
      for (int e = 0; e < 2; ++e) {
        const f32x4 k = k4[2 * pi + e];
        gu[e] = gg[e] * __builtin_elementwise_fma(k.xx, wsum[i].zw, acc[i][2 * pi + e].zw);
        const f32x2 dn = __builtin_elementwise_fma(ex[e], k.yy, k.yy);
        const f32x2 h = gu[e] * f32x2{__builtin_amdgcn_rcpf(dn.x), __builtin_amdgcn_rcpf(dn.y)};
        hv[2 * pi + e] = pk_bf16(h.x, h.y);
      }
      PIN();
      stage(std::integral_constant<int, n0 + 2>{});
    });
    swap32(hv[0], hv[2]); swap32(hv[1], hv[3]);
    swap16(hv[0], hv[1]); swap16(hv[2], hv[3]);
    st16(c.rs, vst + i * 256, i == 2 ? sst2 : sst, u32x4{hv[0], hv[1], hv[2], hv[3]});
    PIN();
  });
}

constexpr int HB_OFF = 64 * KS2 * 1024;
template <int HALF, int C>
DEV void g2_chunk(const Ctx& c, const G1& g, int layer, LAS char* const (&rd)[2], f32x4 (&acc)[4],
                  u32x4 (&Bc)[8], ConvIn& inA) {
  if constexpr (HALF == 0) {
    if constexpr (C == 2) { vm_wait<0>(); BARRIER(); FENCE(); flag_grp(c, 2 * layer + 1); h_refetch(c, g.vst, g.sst + 64 * (KS2 * 1024)); }
  } else if constexpr (C < 5) {
    vm_wait<C == 0 ? 9 : (C == 1 ? 11 : (C == 2 ? 7 : (C == 3 ? 2 : 0)))>();
    BARRIER(); FENCE();
  }
  constexpr int NK = C < 5 ? 4 : 2, NKN = C < 4 ? 4 : 2;
  u32x4 Bn[8];
  sfor<NK * 4>([&](auto M) {
    constexpr int m = M, ks = m >> 2, ct = (m >> 1) & 1, dt = m & 1, n = (C * 4 + ks) * 2 + dt;
    if constexpr (C < 5 && m < NKN * 2) Bn[m] = lds16(rd[(C + 2) >> 2] + ((C + 2) & 3) * 16384 + (m & 1) * 4096 + (m >> 1) * 1024);
    if constexpr (HALF == 1 && C == 0 && m % 3 == 0) {
      if constexpr (m < 12) dma1<HB_OFF + 4 * 8192 + (m / 3) * 1024, 5 * 16384 + (m / 3) * 1024>(c.rs, g.v16, g.hsrc, g.ldsw);
      else dma1<HB_OFF + (m / 3 - 4) * 1024, 6 * 16384 + (m / 3 - 4) * 1024>(c.rs, g.v16, g.hsrc5, g.ldsw);
    }
    if constexpr (HALF == 1 && C == 4 && m == 0) peek<false>(c, E_DOWN);
    if constexpr (n < NWDA) mm_av<C == 0 && ks == 0 && dt == 1, 192 + 4 * n>(acc[dt * 2 + ct], Bc[ks * 2 + ct]);
    else mm_wv<VWD + 4 * (n - NWDA)>(acc[dt * 2 + ct], Bc[ks * 2 + ct]);
    PIN();
  });
  if constexpr (C < 5) {
#pragma unroll
    for (int m = 0; m < NKN * 2; ++m) Bc[m] = Bn[m];
    g2_chunk<HALF, C + 1>(c, g, layer, rd, acc, Bc, inA);
  }
}
template <int HALF>
DEV void g2_unit(const Ctx& c, const G1& g, const Params& p, int layer, ConvIn& inA) {
  const int kh = c.w >> 1, dfin = 2 * (c.w & 1) + kh;
  LAS char* rd0 = c.lds + L_RING + kh * 8192 + c.lane * 16;
  LAS char* rd1 = rd0 + 65536;
  asm volatile("" : "+v"(rd0), "+v"(rd1));
  LAS char* const rd[2] = {rd0, rd1};
  if constexpr (HALF == 1) {
    sfor<4>([&](auto I) { dma4<HB_OFF + I * 8192, (I + 1) * 16384>(c.rs, g.v16, g.hsrc, g.ldsw); });
  }
  f32x4 acc[4];
#pragma unroll
  for (int ct = 0; ct < 2; ++ct) {
    const u32x2 r = tr_read(c.lds + L_RES + HALF * 4096 + ct * 2048 + dfin * 512 + c.lane * 8);
    acc[ct] = f32x4{bf_lo(r.x), bf_hi(r.x), bf_lo(r.y), bf_hi(r.y)};
  }
  LAS char* exw = c.lds + L_RING + HALF * 8192 + c.w * 2048 + c.lane * 16;
  LAS char* exr = c.lds + L_RING + HALF * 8192 + (c.w ^ 2) * 2048 + c.lane * 16;
  LAS char* sts = c.lds + L_ST + 1024 + c.lane * 16;
  u32 vo = layer == 0 ? (2 * c.q + (c.lane & 1)) * 8192 + (c.lane & 2) * 32 + (c.l15 >> 2) * 16 : ((c.q & 1) * 16 + c.l15) * 1024 + (c.q >> 1) * 16;
  u32 so = layer == 0 ? OFF_XP + 2097152 + (c.db * 32 + dfin * 8) * 8192 + HALF * 4096 + c.rt * 128 : (HALF * 1024 + c.rt * 32) * 1024 + c.db * 128 + dfin * 32;
  u32 va = c.q == c.w ? (c.l15 & 3) * 4096 + (c.l15 >> 2) * 16 : 0x7f000000u;
  u32 sa = OFF_ST3 + HALF * 16384 + (c.rt * 8 + (c.w >> 1) * 4) * 16 + (c.w & 1) * 8;
  asm volatile("" : "+v"(acc[0]), "+v"(acc[1]), "+v"(exw), "+v"(exr), "+v"(sts), "+v"(vo), "+v"(va));
  asm volatile("" : "+s"(so), "+s"(sa));
  vm_wait<HALF == 0 ? 13 : 12>();
  BARRIER(); FENCE();
  if constexpr (HALF == 1) flag_all(c, E_DOWN, layer == 0);
  u32x4 Bc[8];
#pragma unroll
  for (int m = 0; m < 8; ++m) Bc[m] = lds16(rd0 + 16384 + (m & 1) * 4096 + (m >> 1) * 1024);
  g2_chunk<HALF, 0>(c, g, layer, rd, acc, Bc, inA);
  if constexpr (HALF == 0) peek<true>(c, 2 * layer + 1);
  MM_DONE4(acc);
  STAMP(16 + 2 * HALF + layer * 20);
#pragma unroll
  for (int ct = 0; ct < 2; ++ct) *(LAS f32x4*)(exw + ct * 1024) = acc[2 + ct];
  if constexpr (HALF == 1) { if (layer == 0) poll_edge<false, 0>(c, E_DOWN); }
  SYNC_LDS();
  if constexpr (HALF == 1) conv_load<1>(c, 0, inA, layer);
  u32 v[4];
#pragma unroll
  for (int ct = 0; ct < 2; ++ct) {
    const f32x4 o = acc[ct] + *(LAS f32x4*)(exr + ct * 1024);
    v[2 * ct] = pk_bf16(o[0], o[1]); v[2 * ct + 1] = pk_bf16(o[2], o[3]);
  }
  if (layer == 0) {
    float st[4];
#pragma unroll
    for (int ct = 0; ct < 2; ++ct) {
      st[2 * ct] = dot2(v[2 * ct], 0x3F803F80u, dot2(v[2 * ct + 1], 0x3F803F80u, 0.f));
      st[2 * ct + 1] = dot2(v[2 * ct], v[2 * ct], dot2(v[2 * ct + 1], v[2 * ct + 1], 0.f));
    }
    swap32f(st[0], st[2]); swap32f(st[1], st[3]);
    float sA = st[0] + st[2], sB = st[1] + st[3];
    swap16f(sA, sB);
    *(LAS float*)(sts + c.w * 4) = sA + sB;
    u32 t0 = dpp_u<0xB1>(selu(M_B0, v[1], v[0])), t1 = dpp_u<0xB1>(selu(M_B0, v[3], v[2]));
    const u32 a0 = selu(M_B0, v[0], t0), a1 = selu(M_B0, t0, v[1]), a2 = selu(M_B0, v[2], t1), a3 = selu(M_B0, t1, v[3]);
    t0 = dpp_u<0x4E>(selu(M_B1, a2, a0)); t1 = dpp_u<0x4E>(selu(M_B1, a3, a1));
    SYNC_LDS();
    const f32x4 s4 = *(LAS f32x4*)sts;
    st16(c.rs, vo, so, u32x4{selu(M_B1, a0, t0), selu(M_B1, a1, t1), selu(M_B1, t0, a2), selu(M_B1, t1, a3)});
    const double sv = (double)((s4.x + s4.y) + (s4.z + s4.w));
    asm volatile("buffer_atomic_add_f64 %0, %1, %2, %3 offen" ::"v"(sv), "v"(va), "s"(c.rs), "s"(sa) : "memory");
    if constexpr (HALF == 0) poll_edge<true, 2>(c, 1);
  } else {
    swap16(v[0], v[2]); swap16(v[1], v[3]);
    st16<0>(mk_rsrc(p.out, SEQ * DM * 2), vo, so, u32x4{v[0], v[1], v[2], v[3]});
    if constexpr (HALF == 0) poll_edge<true, 1>(c, 3);
  }
  if constexpr (HALF == 0) { BARRIER(); FENCE(); }
  STAMP(17 + 2 * HALF + layer * 20);
}

__global__ void __launch_bounds__(256, 1) __attribute__((amdgpu_num_vgpr(140))) mega_kernel(Params p) {
  asm volatile("" ::: "v255", "a255");
  __shared__ __attribute__((aligned(16))) char smem[LDS_BYTES];
  const int w0 = __builtin_amdgcn_readfirstlane(threadIdx.x >> 6);
  Ctx c;
  c.ws = p.ws; c.lds = (LAS char*)smem; c.lds0 = (u32)(size_t)c.lds;
  c.rs = mk_rsrc(p.ws, WS_BYTES);
  refresh(c, w0);
  STAMP(0);
  u32 dsk;
  ConvIn inA, inB;
  ConvF cf;
  u32 pf_sink;
  {
    u64 pc; u32 len;
    asm volatile("s_getpc_b64 %0\n1:\n\ts_mov_b32 %1, .Lmega_tail-1b" : "=s"(pc), "=s"(len));
    const u32 off = ((c.b >> 3) * 16 + (c.tid & 15)) * 128;
    pf_sink = 0;
    if (c.tid < 16 && off + 4 <= len) pf_sink = *(const GAS u32*)(pc + off);
  }

  {
    const int b = c.b, tid = c.tid, lane = c.lane, w = c.w, q = c.q, l15 = c.l15;
    LAS char* lds = c.lds;
    const int kch = w >> 1, khalf = w & 1;
    const rsrc_t rx = mk_rsrc(p.x0, SEQ * DM * 2), rg = mk_rsrc(p.wg, DM * DF * 2), ru = mk_rsrc(p.wu, DM * DF * 2), rd = mk_rsrc(p.wd, DM * DF * 2);
    const u32x4 r0 = ld16p(rx, lane * 16, (8 * b + 2 * w) * 1024), r1 = ld16p(rx, lane * 16 + 1024, (8 * b + 2 * w) * 1024);
    u32x4 xt[2];
#pragma unroll
    for (int i = 0; i < 2; ++i) xt[i] = ld16p(rx, (tid >> 3) * 1024 + (tid & 7) * 16 + i * 128, (b >> 2) * 32768 + (b & 3) * 256);
    const int dch = 2 * c.cp + kch, pn = dch * NS + 64 * khalf + lane;
    const u16 pA = p.logA[pn], pI = p.Aim[pn], pCr = p.Cre[pn], pCi = p.Cim[pn], pdt = p.logdt[dch];
    const u16 pCr2 = p.Cre[pn ^ 64], pCi2 = p.Cim[pn ^ 64];
    dsk = *(const u32*)(p.Dskip + 2 * c.cp);
    const u32 ep = __hip_atomic_load((GAS u32*)(p.ws + OFF_EPOCH), RLX_AGENT);
    const int cbR = b >> 4, sR = b & 15;
    u32x4 wg[2], wu[2];
    int wrow[2], wtau[2];
#pragma unroll
    for (int i = 0; i < 2; ++i) {
      const int id = tid + 256 * i;
      wrow[i] = (id * 5958) >> 16; wtau[i] = id - wrow[i] * 11;
      const u32 vo = wrow[i] * (DF * 2) + wtau[i] * 16 + (id < 352 ? 0u : 0x40000000u) + sR * (32 * DF * 2) + cbR * 172;
      wg[i] = ld16p(rg, vo, 0); wu[i] = ld16p(ru, vo, 0);
    }
    u32x4 dv[2];
    const int s2 = b >> 2, dq = b & 3;
#pragma unroll
    for (int i = 0; i < 2; ++i) {
      const int krow = (tid >> 4) + 16 * i, U = 4 * s2 + (krow >> 3), cbU = (U * 5958) >> 16, fl = 8 * (U - cbU * 11) + (krow & 7);
      dv[i] = ld16p(rd, (86 * cbU + fl) * 1024 + (tid & 15) * 16 + dq * 256 + (fl < 86 ? 0u : 0x40000000u), 0);
    }
    LAS char* wxt = lds + L_XT + (tid >> 3) * 272 + (tid & 7) * 16;
    LAS char* rxt = lds + L_XT + (tid & 7) * (4 * 272) + (tid >> 3) * 4;
    u32 vst = l15 == 0 ? OFF_ST1 + (b >> 7) * 8192 + (w & 1) * 4096 + ((2 * b + (w >> 1)) & 255) * 16 + q * 4
                       : (l15 < 3 ? OFF_ST3 + b * 128 + w * 32 + (l15 - 1) * 16 + q * 4 : 0x7f000000u);
    u32 vxp = (tid >> 3) * 8192 + (tid & 7) * 16;
    u32 sxp = OFF_XP + (b & 3) * (64 * 8192) + (b >> 2) * 128;
    LAS char* wi1[2] = {lds + L_IMG1 + wtau[0] * 1056 + wrow[0] * 32, tid < 96 ? lds + L_IMG1 + wtau[1] * 1056 + wrow[1] * 32 : lds + L_DUMMY + tid * 16};
    LAS char* wi2 = lds + L_IMG2 + ((tid & 15) >> 1) * 1056 + (4 * (tid >> 7) + ((tid >> 4) & 3) + ((tid & 64) ? 16 : 0)) * 32 + (tid & 1) * 16;
    LAS char* ta0 = lds + L_KT + kch * KT_CH + (khalf ? 512 + 64 * q + l15 : 128 * (q >> 1) + 4 * (q & 1) + 8 * l15) * 4;
    LAS char* ta1 = lds + L_KT + kch * KT_CH + (q < 2 ? 256 + 64 * q + 4 * l15 : 384 + 64 * (q - 2) + 2 * l15) * 4;
    LAS char* ta1b = ta1 + (q < 2 ? 2 : 32) * 4;
    asm volatile("" : "+v"(wxt), "+v"(rxt), "+v"(vst), "+v"(vxp), "+v"(wi1[0]), "+v"(wi1[1]), "+v"(wi2), "+v"(ta0), "+v"(ta1), "+v"(ta1b));
    asm volatile("" : "+s"(sxp));
    LAS char* ci[11];
#pragma unroll
    for (int i = 0; i < 11; i += 2) {
      const int l0 = 2047 - 8 * tid - i, l1 = l0 - 1;
      const u32 pb = pk_bf16((float)(i >= 8 ? max(l0, 0) : l0), (float)(i >= 8 ? max(l1, 0) : l1));
      const int b0 = (int)(pb & 0xffffu) - (134 << 7) + 128, b1 = (int)(pb >> 16) - (134 << 7) + 128;
      ci[i] = lds + L_KT + 4 * max(b0, min(l0, 128));
      asm volatile("" : "+v"(ci[i]));
      if (i < 10) { ci[i + 1] = lds + L_KT + 4 * max(b1, min(l1, 128)); asm volatile("" : "+v"(ci[i + 1])); }
    }
    {
      const u32x4 z = {0u, 0u, 0u, 0u};
      *(LAS u32x4*)(tid < 16 ? lds + L_KC + (tid >> 1) * KC_COPY + 4096 + (tid & 1) * 16 : (tid < 18 ? lds + L_KT - 16 + (tid & 1) * KT_CH : lds + L_DUMMY + tid * 16)) = z;
    }
    conv_setup(c, 0, cf);
    PIN();
    STAMP(42);
    asm volatile("" :: "v"(pf_sink));
#pragma unroll
    for (int i = 0; i < 2; ++i) *(LAS u32x4*)(wxt + i * 128) = xt[i];
    {
      float s0 = 0.f, q0 = 0.f, s1 = 0.f, q1 = 0.f;
#pragma unroll
      for (int i = 0; i < 4; ++i) {
        s0 = dot2(r0[i], 0x3F803F80u, s0); q0 = dot2(r0[i], r0[i], q0);
        s1 = dot2(r1[i], 0x3F803F80u, s1); q1 = dot2(r1[i], r1[i], q1);
      }
      swap32f(s0, s1); swap32f(q0, q1);
      float sA = s0 + s1, sB = q0 + q1;
      swap16f(sA, sB);
      float sv = sA + sB;
      sv += dpp_f<0xB1>(sv); sv += dpp_f<0x4E>(sv); sv += dpp_f<0x141>(sv); sv += dpp_f<0x140>(sv);
      __builtin_amdgcn_raw_buffer_store_b32(l15 == 0 ? __builtin_bit_cast(u32, sv) : 0u, c.rs, vst, 0, 16);
    }
    SYNC_LDS();
    {
      u32x4 o[2];
#pragma unroll
      for (int i = 0; i < 2; ++i) o[i] = u32x4{*(LAS u32*)(rxt + i * 128), *(LAS u32*)(rxt + i * 128 + 272), *(LAS u32*)(rxt + i * 128 + 544), *(LAS u32*)(rxt + i * 128 + 816)};
      PIN();
      st16(c.rs, vxp, sxp, o[0]); st16(c.rs, vxp, sxp + 32 * 8192, o[1]);
    }
    STAMP(43);
    c.epoch = ep + 1u;

    const float dt = rnd_bf16(expf(bf2f(pdt)));
    const float a2 = -rnd_bf16(expf(bf2f(pA))) * dt * 1.44269504f;
    const float ai = bf2f(pI) * dt;
    const float i2p_hi = 0.15915494f, i2p_lo = (float)(0.15915494309189535 - (double)0.15915494f);
    const float fhi = ai * i2p_hi, flo = __builtin_fmaf(ai, i2p_hi, -fhi) + ai * i2p_lo;
    f32x2 wp[3];
#pragma unroll
    for (int i = 0; i < 3; ++i) {
      const float ps = i == 0 ? 1.f : (i == 1 ? 8.f : 128.f);
      const float mag = __builtin_amdgcn_exp2f(a2 * ps);
      const float rev = __builtin_fmaf(flo, ps, __builtin_amdgcn_fractf(fhi * ps));
      wp[i] = f32x2{mag * __builtin_amdgcn_cosf(rev), mag * __builtin_amdgcn_sinf(rev)};
    }
    const float cr = bf2f(pCr), cim = bf2f(pCi);
    int ce = 14 - __builtin_amdgcn_frexp_expf(wave_max(fmaxf(fmaxf(fabsf(cr), fabsf(cim)), fmaxf(fabsf(bf2f(pCr2)), fabsf(bf2f(pCi2))))));
    ce = ce > 100 ? 100 : (ce < -100 ? -100 : ce);
    LAS char* tb = lds + L_TB + w * TB_WAVE + lane * 4;
    auto put2 = [&](int ra, f32x2 va, int rb2, f32x2 vb) {
      *(LAS u32*)(tb + ra * TB_ROW) = __builtin_bit_cast(u32, __builtin_convertvector(va, f16x2));
      *(LAS u32*)(tb + rb2 * TB_ROW) = __builtin_bit_cast(u32, __builtin_convertvector(vb, f16x2));
    };
    {
      const f32x2 w8c = {wp[1].x, -wp[1].y}, w16c = cmul(w8c, w8c);
      f32x2 y0 = {ldexpf(cr, ce), -ldexpf(cim, ce)}, y1 = cmul(y0, w8c);
#pragma unroll
      for (int i = 0; i < 8; ++i) {
        put2(41 + 2 * i, y0, 42 + 2 * i, y1);
        if (i < 7) { y0 = cmul(y0, w16c); y1 = cmul(y1, w16c); }
      }
    }
    FENCE();
#pragma unroll
    for (int i = 0; i < 2; ++i) {
      const bool last = wtau[i] == 10;
      *(LAS u32x4*)wi1[i] = u32x4{wg[i].x, wu[i].x, wg[i].y, wu[i].y};
      *(LAS u32x4*)(wi1[i] + 16) = u32x4{wg[i].z, wu[i].z, last ? 0x00003F80u : wg[i].w, last ? 0u : wu[i].w};
      *(LAS u32x4*)(wi2 + i * 256) = dv[i];
    }
    vm_wait<0>();
    SYNC_LDS();
    flag_all(c, E_P0);
    STAMP(1);
    {
      LAS char* ri1 = lds + L_IMG1 + 3 * w * 1056 + lane * 8;
      LAS char* ri2 = lds + L_IMG2 + 2 * w * 1056 + lane * 8;
      const u32 sgu = OFF_WGU + ((cbR * 11 + 3 * w) * 16 + sR) * 1024, sgu2 = w == 3 ? 0x7f000000u : sgu + 32768;
      const u32 swd = b < 176 ? OFF_WD + ((dq * 8 + 2 * w) * KS2 + s2) * 1024 : 0x7f000000u;
      u32x4 o[5];
#pragma unroll
      for (int i = 0; i < 3; ++i) o[i] = tr_frag(ri1 + i * 1056);
#pragma unroll
      for (int i = 0; i < 2; ++i) o[3 + i] = tr_frag(ri2 + i * 1056);
      PIN();
      st16(c.rs, lane * 16, sgu, o[0]); st16(c.rs, lane * 16, sgu + 16384, o[1]); st16(c.rs, lane * 16, sgu2, o[2]);
      st16(c.rs, lane * 16, swd, o[3]); st16(c.rs, lane * 16, swd + KS2 * 1024, o[4]);
    }
    STAMP(2);
    {
      f32x2 e[8];
      e[1] = wp[0]; e[2] = cmul(e[1], e[1]); e[3] = cmul(e[2], e[1]); e[4] = cmul(e[2], e[2]);
      e[5] = cmul(e[4], e[1]); e[6] = cmul(e[4], e[2]); e[7] = cmul(e[4], e[3]);
      const f32x2 w256 = cmul(wp[2], wp[2]);
      f32x2 ta = {16384.f, 0.f}, tc = wp[2] * 16384.f;
      put2(0, ta, 8, tc);
#pragma unroll
      for (int i = 1; i < 8; ++i) put2(i, e[i] * 16384.f, 8 + i, cmul(tc, e[i]));
      ta = w256 * 16384.f; tc = cmul(tc, w256);
      put2(16, ta, 20, tc);
#pragma unroll
      for (int i = 1; i < 4; ++i) put2(16 + i, cmul(ta, e[2 * i]), 20 + i, cmul(tc, e[2 * i]));
#pragma unroll
      for (int i = 0; i < 2; ++i) {
        ta = cmul(ta, w256); tc = cmul(tc, w256);
        put2(24 + 4 * i, ta, 26 + 4 * i, tc);
        put2(25 + 4 * i, cmul(ta, e[4]), 27 + 4 * i, cmul(tc, e[4]));
      }
#pragma unroll
      for (int i = 0; i < 4; ++i) {
        ta = cmul(ta, w256); tc = cmul(tc, w256);
        put2(32 + 2 * i, ta, 33 + 2 * i, tc);
      }
      ta = cmul(ta, w256);
      put2(40, ta, 40, ta);
    }
    vm_wait<0>();
    SYNC_LDS();
    peek<false>(c, E_P0);
    flag_all(c, E_W);
    {
      LAS char* tr = lds + L_TB + 2 * kch * TB_WAVE + l15 * TB_ROW + q * 16;
      LAS char* tr0 = tr + khalf * 32 * TB_ROW;
      f32x4 kk[2];
      sfor<2>([&](auto H) {
        u32x4 fa[4][2], fb[4];
#pragma unroll
        for (int G = 0; G < 4; ++G) {
          fb[G] = lds16(tr + H * TB_WAVE + 41 * TB_ROW + G * 64);
          fa[G][0] = lds16(tr0 + H * TB_WAVE + G * 64);
          fa[G][1] = lds16(tr + H * TB_WAVE + 16 * TB_ROW + G * 64);
        }
        sfor<8>([&](auto N) { mm_f16<(H == 0 && N < 2)>(kk[N & 1], fa[N >> 1][N & 1], fb[N >> 1]); });
      });
      const float ks = ldexpf(2.f * dt, -14 - ce);
      asm volatile("s_nop 7" : "+v"(kk[0]), "+v"(kk[1]));
      kk[0] *= ks; kk[1] *= ks;
      STAMP(40);
      if (khalf) {
#pragma unroll
        for (int j = 0; j < 4; ++j) *(LAS float*)(ta0 + j * 64) = kk[0][j];
      } else {
        *(LAS f32x4*)ta0 = kk[0];
        *(LAS f32x2*)ta1 = kk[1].xy; *(LAS f32x2*)ta1b = kk[1].zw;
      }
    }
    poll_edge<false, 1>(c, E_P0);
    SYNC_LDS();
    STAMP(41);
    conv_load<0>(c, 0, inA); conv_load<0>(c, 1, inB);
    peek<false>(c, E_W);
    PIN();
    {
      f32x2 V[11];
#pragma unroll
      for (int i = 0; i < 11; ++i) V[i] = f32x2{*(LAS float*)ci[i], *(LAS float*)(ci[i] + KT_CH)};
      PIN();
#pragma unroll
      for (int ch = 0; ch < 2; ++ch)
#pragma unroll
        for (int s = 0; s < 4; ++s) {
          const u32x4 o = {pk_bf16(V[3 - s][ch], V[4 - s][ch]), pk_bf16(V[5 - s][ch], V[6 - s][ch]), pk_bf16(V[7 - s][ch], V[8 - s][ch]), pk_bf16(V[9 - s][ch], V[10 - s][ch])};
          *(LAS u32x4*)(lds + L_KC + ch * KC_CH + s * KC_COPY + tid * 16) = o;
        }
    }
  }
  STAMP(3);

  conv_phase<0>(c, cf, dsk, inA, inB);

#pragma unroll 1
  for (int layer = 0; layer < 2; ++layer) {
    refresh(c, w0);
    STAMP(10 + layer * 20);
    G1 g;
    g.layer = layer;
    g.xa = xa_ptr(c, layer);
    g.res = res_ptr(c, layer);
    g.ring[0] = c.lds + L_RING + c.lane * 8;
    g.ring[1] = g.ring[0] + 65536;
    asm volatile("" : "+v"(g.ring[0]), "+v"(g.ring[1]));
    g.ldsw = c.lds0 + L_RING + c.w * 4096;
    g.v16 = c.lane * 16;
    g.wd0 = wd_ptr(c, 0);
    g.wd1 = wd_ptr(c, 1);
    g.wd0s = g.wd0 - (c.w >> 1) * 2048; g.wd1s = g.wd1 - (c.w >> 1) * 2048;
    asm volatile("" : "+s"(g.wd0s), "+s"(g.wd1s));
    g.vst = c.q * (KS2 * 1024) + c.l15 * 16;
    g.sst = OFF_H + layer * H_BYTES + c.rb * 4 * (KS2 * 1024) + (c.cb * 11 + 3 * c.w) * 256;
    g.hsrc = OFF_H + layer * H_BYTES + (c.rt * 2 + (c.w & 1)) * (KS2 * 1024) + (c.w >> 1) * 4096;
    g.hsrc5 = g.hsrc + 5 * 8192 - (c.w >> 1) * 2048;
    g.ldsr = c.lds0 + L_RES + c.w * 1024;
    g.pk = c.lds + L_ST + c.l15 * 16;
    g.pws = c.lds + L_WS + c.w * 192 + c.q * 16;
    asm volatile("" : "+s"(g.wd0), "+s"(g.wd1), "+s"(g.xa), "+s"(g.res), "+s"(g.ldsw), "+s"(g.sst), "+s"(g.hsrc), "+s"(g.hsrc5), "+s"(g.ldsr));
    asm volatile("" : "+v"(g.vst), "+v"(g.pk), "+v"(g.v16), "+v"(g.pws));
    u32x4 Bc[4];
    f32x4 acc[3][4];
    if (layer == 0) vm_wait<11>(); else vm_wait<5>();
    BARRIER(); FENCE();
#pragma unroll
    for (int ct = 0; ct < 4; ++ct) Bc[ct] = tr_frag(g.ring[0] + ct * 4096);
    STAMP(11 + layer * 20);
    g1_main<0>(c, g, acc, Bc);
    if (layer == 0) wsum_part<11, 16>(g.pws);
    g1_epilogue<0>(c, g, acc);
    vm_wait<15>();
    BARRIER(); FENCE();
#pragma unroll
    for (int ct = 0; ct < 4; ++ct) Bc[ct] = tr_frag(g.ring[1] + ct * 4096);
    STAMP(13 + layer * 20);
    g1_main<1>(c, g, acc, Bc);
    g1_epilogue<1>(c, g, acc);
    STAMP(15 + layer * 20);
    refresh(c, w0);
    ConvIn nA, nB;
    g2_unit<0>(c, g, p, layer, nA);
    g2_unit<1>(c, g, p, layer, nA);
    if (layer == 0) { ConvF c2; refresh(c, w0); conv_setup(c, 1, c2); conv_phase<1>(c, c2, dsk, nA, nB); }
  }
  if (blockIdx.x == 0 && threadIdx.x == 0) __hip_atomic_store((GAS u32*)(c.ws + OFF_EPOCH), c.epoch, RLX_AGENT);
  asm volatile(".Lmega_tail:");
}

}

extern "C" void kernel_launch(void* const* d_in, const int* in_sizes, int n_in, void* d_out, int out_size, void* d_ws,
                              size_t ws_size, hipStream_t stream) {
  Params p;
  p.x0 = (const u16*)d_in[0];
  p.logA = (const u16*)d_in[1];
  p.Aim = (const u16*)d_in[2];
  p.Cre = (const u16*)d_in[3];
  p.Cim = (const u16*)d_in[4];
  p.logdt = (const u16*)d_in[5];
  p.Dskip = (const u16*)d_in[6];
  p.wg = (const u16*)d_in[8];
  p.wu = (const u16*)d_in[9];
  p.wd = (const u16*)d_in[10];
  p.out = (u16*)d_out;
  p.ws = (char*)d_ws;
  mega_kernel<<<NBLK, 256, 0, stream>>>(p);
}
```

```cpp
#include <hip/hip_runtime.h>
#include <hip/hip_bf16.h>
#include <utility>

typedef unsigned int u32;
typedef unsigned short u16;
typedef unsigned long long u64;
typedef u32 u32x4 __attribute__((ext_vector_type(4)));
typedef u32 u32x2 __attribute__((ext_vector_type(2)));
typedef float f32x4 __attribute__((ext_vector_type(4)));
typedef float f32x2 __attribute__((ext_vector_type(2)));
typedef short s16x4 __attribute__((ext_vector_type(4)));
typedef __bf16 bf16x8 __attribute__((ext_vector_type(8)));
typedef __bf16 bf16x2 __attribute__((ext_vector_type(2)));
typedef _Float16 f16x8 __attribute__((ext_vector_type(8)));
typedef _Float16 f16x2 __attribute__((ext_vector_type(2)));
#define LAS __attribute__((address_space(3)))
#define GAS __attribute__((address_space(1)))
#define RLX_AGENT __ATOMIC_RELAXED, __HIP_MEMORY_SCOPE_AGENT
#define DEV __device__ __forceinline__

#define CODE_PREFETCH 1
#define MAP_ROWS 0
#define H_REFETCH 0
#define PK_B 28
#define PK_A 4
#define FL_G 2
#define PK_G 84
#define CK_B 64
#define SP_H 48
#define SP_D 4
#define FL_A 2
#define PK_D 89
#define CK_D 1
#define FL_B 8
#define STAMP_WAVE 0

namespace {

constexpr int SEQ = 2048, DM = 512, NS = 128, DF = 1376;
constexpr int NBLK = 256;
constexpr int KS2 = 44;

constexpr u32 OFF_EPOCH = 0;
constexpr u32 OFF_FLAG = 4096;
constexpr u32 OFF_GFLAG = OFF_FLAG + 8 * 8192;
constexpr u32 OFF_ST1 = OFF_GFLAG + 8192;
constexpr u32 OFF_ST3 = OFF_ST1 + 16384;
constexpr u32 OFF_WGU = OFF_ST3 + 32768;
constexpr u32 OFF_WD = OFF_WGU + 16 * 11 * 16 * 1024;
constexpr u32 OFF_XC = OFF_WD + 32 * KS2 * 1024;
constexpr u32 OFF_XP = OFF_XC + 2 * 2097152;
constexpr u32 OFF_H = OFF_XP + 2 * 2097152;
constexpr u32 H_BYTES = 128 * KS2 * 1024;
constexpr u32 OFF_DBG = OFF_H + 2 * H_BYTES;
constexpr u32 WS_BYTES = OFF_DBG + 256 * 64 * 16;
constexpr int E_P0 = 0, E_W = 7, E_CONV = 1  , E_DOWN = 3  ;

constexpr int KC_COPY = 4160, KC_CH = 4 * KC_COPY;
constexpr int L_KC = 0;
constexpr int L_RING = 2 * KC_CH;
constexpr int L_RES = L_RING + 7 * 16384;
constexpr int L_ST = L_RES + 8192;
constexpr int L_WS = L_ST + 2048;
constexpr int L_VD = L_WS + 768;
constexpr int LDS_BYTES = 163840;
constexpr int L_U = L_RING + 5 * 16384;
constexpr int U_CH = 4608;
constexpr int L_P = L_RING + 6 * 16384;
constexpr int TB_ROW = 272, TB_WAVE = 57 * TB_ROW;
constexpr int L_TB = L_RING;
constexpr int L_IMG1 = L_TB + 4 * TB_WAVE;
constexpr int L_KT = L_IMG1 + 11 * 1056 + 16;
constexpr int KT_CH = 772 * 4;
constexpr int L_IMG2 = L_U + 2 * U_CH;
constexpr int L_XT = L_IMG2 + 8 * 1056;
constexpr int L_DUMMY = L_XT + 32 * 272;
static_assert(L_KT + 2 * KT_CH <= L_U && L_DUMMY + 4128 <= L_RES && L_VD + 16 <= LDS_BYTES, "lds");

struct Params {
  const u16 *x0, *logA, *Aim, *Cre, *Cim, *logdt, *Dskip, *wg, *wu, *wd;
  u16* out;
  char* ws;
};

DEV float bf_lo(u32 v) { return __builtin_bit_cast(float, v << 16); }
DEV float bf_hi(u32 v) { return __builtin_bit_cast(float, v & 0xffff0000u); }
DEV float bf2f(u16 v) { return __builtin_bit_cast(float, (u32)v << 16); }
DEV u32 pk_bf16(float lo, float hi) {
  f32x2 f = {lo, hi};
  return __builtin_bit_cast(u32, __builtin_convertvector(f, bf16x2));
}
DEV float rnd_bf16(float v) { return bf_lo(pk_bf16(v, 0.f)); }
DEV u32x2 tr_read(LAS char* p) {
  return __builtin_bit_cast(u32x2, __builtin_amdgcn_ds_read_tr16_b64_v4i16((LAS s16x4*)p));
}
DEV u32x4 tr_frag(LAS char* p) {
  u32x2 a = tr_read(p), b = tr_read(p + 512);
  u32x4 r = {a.x, a.y, b.x, b.y};
  return r;
}
DEV u32x4 lds16(LAS char* p) { return *(LAS u32x4*)p; }
template <int CTRL> DEV float dpp_f(float v) {
  return __builtin_bit_cast(float, __builtin_amdgcn_mov_dpp(__builtin_bit_cast(int, v), CTRL, 0xf, 0xf, true));
}
template <int CTRL> DEV u32 dpp_u(u32 v) { return (u32)__builtin_amdgcn_mov_dpp((int)v, CTRL, 0xf, 0xf, true); }
DEV void swap16(u32& a, u32& b) { auto r = __builtin_amdgcn_permlane16_swap(a, b, false, false); a = r[0]; b = r[1]; }
DEV void swap32(u32& a, u32& b) { auto r = __builtin_amdgcn_permlane32_swap(a, b, false, false); a = r[0]; b = r[1]; }
DEV void swap16f(float& a, float& b) { u32 x = __builtin_bit_cast(u32, a), y = __builtin_bit_cast(u32, b); swap16(x, y); a = __builtin_bit_cast(float, x); b = __builtin_bit_cast(float, y); }
DEV void swap32f(float& a, float& b) { u32 x = __builtin_bit_cast(u32, a), y = __builtin_bit_cast(u32, b); swap32(x, y); a = __builtin_bit_cast(float, x); b = __builtin_bit_cast(float, y); }
DEV float wave_sum(float v) {
  v += dpp_f<0xB1>(v);
  v += dpp_f<0x4E>(v);
  v += dpp_f<0x141>(v);
  v += dpp_f<0x140>(v);
  float a = v, b = v;
  swap16f(a, b); v = a + b;
  a = v; b = v;
  swap32f(a, b); v = a + b;
  return v;
}
DEV float wave_max(float v) {
  v = fmaxf(v, dpp_f<0xB1>(v)); v = fmaxf(v, dpp_f<0x4E>(v)); v = fmaxf(v, dpp_f<0x141>(v)); v = fmaxf(v, dpp_f<0x140>(v));
  float a = v, b = v;
  swap16f(a, b); v = fmaxf(a, b);
  a = v; b = v;
  swap32f(a, b); v = fmaxf(a, b);
  return v;
}
DEV float sel(u64 m, float a, float b) { float r; asm("v_cndmask_b32_e64 %0, %1, %2, %3" : "=v"(r) : "v"(a), "v"(b), "s"(m)); return r; }
DEV u32 selu(u64 m, u32 a, u32 b) { u32 r; asm("v_cndmask_b32_e64 %0, %1, %2, %3" : "=v"(r) : "v"(a), "v"(b), "s"(m)); return r; }
constexpr u64 M_B0 = 0xAAAAAAAAAAAAAAAAull, M_B1 = 0xCCCCCCCCCCCCCCCCull, M_B2 = 0xF0F0F0F0F0F0F0F0ull, M_B3 = 0xFF00FF00FF00FF00ull;
DEV float dot2(u32 a, u32 b, float acc) {
  return __builtin_amdgcn_fdot2_f32_bf16(__builtin_bit_cast(bf16x2, a), __builtin_bit_cast(bf16x2, b), acc, false);
}
DEV f32x2 cmul(f32x2 a, f32x2 b) {
  const f32x2 t = a.yy * f32x2{-b.y, b.x};
  return __builtin_elementwise_fma(a.xx, b, t);
}
#define ACLOB "a0","a1","a2","a3","a4","a5","a6","a7","a8","a9","a10","a11","a12","a13","a14","a15","a16","a17","a18","a19","a20","a21","a22","a23","a24","a25","a26","a27","a28","a29","a30","a31","a32","a33","a34","a35","a36","a37","a38","a39","a40","a41","a42","a43","a44","a45","a46","a47","a48","a49","a50","a51","a52","a53","a54","a55","a56","a57","a58","a59","a60","a61","a62","a63","a64","a65","a66","a67","a68","a69","a70","a71","a72","a73","a74","a75","a76","a77","a78","a79","a80","a81","a82","a83","a84","a85","a86","a87","a88","a89","a90","a91","a92","a93","a94","a95","a96","a97","a98","a99","a100","a101","a102","a103","a104","a105","a106","a107","a108","a109","a110","a111","a112","a113","a114","a115","a116","a117","a118","a119","a120","a121","a122","a123","a124","a125","a126","a127","a128","a129","a130","a131","a132","a133","a134","a135","a136","a137","a138","a139","a140","a141","a142","a143","a144","a145","a146","a147","a148","a149","a150","a151","a152","a153","a154","a155","a156","a157","a158","a159","a160","a161","a162","a163","a164","a165","a166","a167","a168","a169","a170","a171","a172","a173","a174","a175","a176","a177","a178","a179","a180","a181","a182","a183","a184","a185","a186","a187","a188","a189","a190","a191","a192","a193","a194","a195","a196","a197","a198","a199","a200","a201","a202","a203","a204","a205","a206","a207","a208","a209","a210","a211","a212","a213","a214","a215","a216","a217","a218","a219","a220","a221","a222","a223","a224","a225","a226","a227","a228","a229","a230","a231","a232","a233","a234","a235","a236","a237","a238","a239","a240","a241","a242","a243","a244","a245","a246","a247","a248","a249","a250","a251","a252","a253","a254","a255"
#define BARRIER() __builtin_amdgcn_s_barrier()
#define FENCE() asm volatile("" ::: "memory")
#define PIN() __builtin_amdgcn_sched_barrier(0)
#define LGKM0() asm volatile("s_waitcnt lgkmcnt(0)" ::: "memory", ACLOB)
#define SYNC_LDS() do { LGKM0(); BARRIER(); FENCE(); } while (0)
template <int N> DEV void vm_wait() { asm volatile("s_waitcnt vmcnt(%0)" ::"i"(N) : "memory", ACLOB); }

typedef __amdgpu_buffer_rsrc_t rsrc_t;
DEV rsrc_t mk_rsrc(const void* p, u32 bytes) { return __builtin_amdgcn_make_buffer_rsrc((void*)p, 0, bytes, 0x00020000); }
DEV u32x4 ld16(rsrc_t r, u32 voff, u32 soff) { return __builtin_amdgcn_raw_buffer_load_b128(r, voff, soff, 16); }
DEV u32x4 ld16p(rsrc_t r, u32 voff, u32 soff) { return __builtin_amdgcn_raw_buffer_load_b128(r, voff, soff, 0); }
template <int AUX = 16> DEV void st16(rsrc_t r, u32 voff, u32 soff, u32x4 v) {
  __builtin_amdgcn_raw_buffer_store_b128(v, r, voff, soff, AUX);
  asm volatile("s_nop 1" ::"v"(v) : "memory");
}

template <int... Is, class F> DEV void sfor_impl(std::integer_sequence<int, Is...>, F&& f) { (f(std::integral_constant<int, Is>{}), ...); }
template <int N, class F> DEV void sfor(F&& f) { sfor_impl(std::make_integer_sequence<int, N>{}, f); }
template <bool FIRST> DEV void mm_vv(f32x4& acc, const u32x4& a, const u32x4& b) {
  if constexpr (FIRST) asm volatile("v_mfma_f32_16x16x32_bf16 %0, %1, %2, 0" : "=&v"(acc) : "v"(a), "v"(b));
  else asm volatile("v_mfma_f32_16x16x32_bf16 %0, %1, %2, %0" : "+v"(acc) : "v"(a), "v"(b));
}
template <bool FIRST> DEV void mm_f16(f32x4& acc, const u32x4& a, const u32x4& b) {
  if constexpr (FIRST) asm volatile("v_mfma_f32_16x16x32_f16 %0, %1, %2, 0" : "=&v"(acc) : "v"(a), "v"(b));
  else asm volatile("v_mfma_f32_16x16x32_f16 %0, %1, %2, %0" : "+v"(acc) : "v"(a), "v"(b));
}
template <bool FIRST, int A> DEV void mm_av(f32x4& acc, const u32x4& b) {
  if constexpr (FIRST) asm volatile("v_mfma_f32_16x16x32_bf16 %0, a[%c2:%c3], %1, 0" : "=&v"(acc) : "v"(b), "i"(A), "i"(A + 3));
  else asm volatile("v_mfma_f32_16x16x32_bf16 %0, a[%c2:%c3], %1, %0" : "+v"(acc) : "v"(b), "i"(A), "i"(A + 3));
}
template <int A, int OFF> DEV void ld16_a(rsrc_t r, u32 voff, u32 soff) {
  asm volatile("buffer_load_dwordx4 a[%c3:%c4], %0, %1, %2 offen offset:%c5 sc1" ::"v"(voff), "s"(r), "s"(soff), "i"(A), "i"(A + 3), "i"(OFF) : "memory");
}
#define MM_DONE4(a) asm volatile("s_nop 7" : "+v"((a)[0]), "+v"((a)[1]), "+v"((a)[2]), "+v"((a)[3]))

template <int G, int L> DEV void dma1(rsrc_t r, u32 voff, u32 soff, u32 lds) {
  u32 t;
  asm volatile("s_add_u32 m0, %4, %6\n\ts_add_u32 %0, %3, %5\n\tbuffer_load_dwordx4 %1, %2, %0 offen sc1 lds"
               : "=&s"(t) : "v"(voff), "s"(r), "s"(soff), "s"(lds), "i"(G), "i"(L) : "memory", "scc");
}
template <int G, int L, int P> DEV void dmap(rsrc_t r, u32 voff, u32 soff, u32 lds, u32& t) {
  if constexpr (P == 0)
    asm volatile("s_add_u32 m0, %4, %6\n\ts_add_u32 %0, %3, %5\n\tbuffer_load_dwordx4 %1, %2, %0 offen sc1 lds"
                 : "=&s"(t) : "v"(voff), "s"(r), "s"(soff), "s"(lds), "i"(G), "i"(L) : "memory", "scc");
  else
    asm volatile("buffer_load_dwordx4 %0, %1, %2 offen offset:%c3 sc1 lds" ::"v"(voff), "s"(r), "s"(t), "i"(P * 1024) : "memory");
}
template <int G, int L> DEV void dma4(rsrc_t r, u32 voff, u32 soff, u32 lds) {
  u32 t;
  asm volatile("s_add_u32 m0, %4, %6\n\ts_add_u32 %0, %3, %5\n\tbuffer_load_dwordx4 %1, %2, %0 offen sc1 lds\n\t"
               "buffer_load_dwordx4 %1, %2, %0 offen offset:1024 sc1 lds\n\tbuffer_load_dwordx4 %1, %2, %0 offen offset:2048 sc1 lds\n\t"
               "buffer_load_dwordx4 %1, %2, %0 offen offset:3072 sc1 lds"
               : "=&s"(t) : "v"(voff), "s"(r), "s"(soff), "s"(lds), "i"(G), "i"(L) : "memory", "scc");
}

constexpr u32 SPIN_CAP = 1u << 16;
constexpr int SYNC_WAVE = 2;

struct Ctx {
  char* ws;
  rsrc_t rs;
  LAS char* lds;
  u32 lds0, epoch;
  int b, tid, lane, w, q, l15, cp, cb, rb, db, rt;
  mutable u32 junk;
  u32 vflag, vpeek, sflag, sflagg, speek, speekg;
};

DEV void flag_setup(Ctx& c) {
  c.vflag = c.tid == 192 ? 0u : 0x7f000000u;
  c.vpeek = c.lane * 64;
  c.sflag = OFF_FLAG + (c.b >> 2) * 64 + (c.b & 3) * 4;
  c.sflagg = OFF_FLAG + 8 * 4096 + c.rb * 256 + (c.cb >> 2) * 64 + (c.cb & 3) * 4;
  c.speek = c.w == SYNC_WAVE ? OFF_FLAG : 0x7f000000u;
  c.speekg = c.w == SYNC_WAVE ? OFF_FLAG + 8 * 4096 + (c.rt >> 1) * 256 : 0x7f000000u;
  asm volatile("" : "+v"(c.vflag), "+v"(c.vpeek));
  asm volatile("" : "+s"(c.sflag), "+s"(c.sflagg), "+s"(c.speek), "+s"(c.speekg));
}
template <int E> DEV void flag_st(const Ctx& c, u32 base) {
  u32 t;
  asm volatile("s_add_u32 %0, %4, %5\n\tbuffer_store_dword %1, %2, %3, %0 offen sc1" : "=&s"(t) : "v"(c.epoch), "v"(c.vflag), "s"(c.rs), "s"(base), "i"(E * 4096) : "memory", "scc");
}
template <bool GRP, int E> DEV void peek(const Ctx& c, u32 base) {
  u32 t;
  if constexpr (GRP)
    asm volatile("s_mov_b64 exec, 15\n\ts_add_u32 %0, %3, %4\n\tbuffer_load_dwordx4 a[252:255], %1, %2, %0 offen sc1\n\ts_mov_b64 exec, -1"
                 : "=&s"(t) : "v"(c.vpeek), "s"(c.rs), "s"(base), "i"(E * 4096) : "memory", "scc");
  else
    asm volatile("s_add_u32 %0, %3, %4\n\tbuffer_load_dwordx4 a[252:255], %1, %2, %0 offen sc1"
                 : "=&s"(t) : "v"(c.vpeek), "s"(c.rs), "s"(base), "i"(E * 4096) : "memory", "scc");
}
template <bool GRP, int N> DEV u64 flags_miss(const Ctx& c) {
  u32 f0, f1, f2, f3;
  asm volatile("s_waitcnt vmcnt(%4)\n\tv_accvgpr_read_b32 %0, a252\n\tv_accvgpr_read_b32 %1, a253\n\tv_accvgpr_read_b32 %2, a254\n\tv_accvgpr_read_b32 %3, a255"
               : "=v"(f0), "=v"(f1), "=v"(f2), "=v"(f3) : "i"(N) : "memory");
  const u32 d = ((f0 ^ c.epoch) | (f1 ^ c.epoch)) | ((f2 ^ c.epoch) | (f3 ^ c.epoch));
  return __builtin_amdgcn_ballot_w64(d != 0u) & (GRP ? 15ull : ~0ull);
}
template <bool GRP, int N, int E> DEV void poll_edge(const Ctx& c, u32 base) {
  if (c.w == SYNC_WAVE) {
    if (__builtin_expect(flags_miss<GRP, N>(c) != 0ull, 0)) {
#pragma unroll 1
      for (u32 sp = 0; sp < SPIN_CAP; ++sp) {
        peek<GRP, E>(c, base);
        if (flags_miss<GRP, 0>(c) == 0ull) break;
        __builtin_amdgcn_s_sleep(4);
      }
    }
  }
  FENCE();
}

#define STAMP(i) do { } while (0)

DEV void refresh(Ctx& c, int w0) {
  int tid = threadIdx.x, b = blockIdx.x, w = w0;
  asm volatile("" : "+v"(tid)); asm volatile("" : "+s"(b)); asm volatile("" : "+s"(w));
  c.tid = tid; c.b = b; c.w = w; c.lane = tid & 63; c.q = c.lane >> 4; c.l15 = tid & 15;
  const int xcd = b & 7, rr = b >> 3;
  c.cp = xcd * 32 + rr;
  c.cb = (xcd & 3) * 4 + (rr & 3); c.rb = (xcd >> 2) * 8 + (rr >> 2);
  c.db = (xcd & 1) * 4 + (rr & 3); c.rt = (xcd >> 1) * 8 + (rr >> 2);
}

constexpr int AW(int tile, int s) { return (tile * 16 + s) * 4; }
template <int N> DEV void ld_wgu(const Ctx& c) {
  constexpr int tl = N % 3, s = N / 3;
  const u32 v = c.lane * 16 + ((tl == 2 && c.w == 3) ? 0x40000000u : 0u);
  ld16_a<AW(tl, s), (s & 3) * 1024>(c.rs, v, OFF_WGU + (c.cb * 11 + 3 * c.w + tl) * 16384 + (s >> 2) * 4096);
}
constexpr int HB_OFF = 64 * KS2 * 1024;
constexpr int NWDA = 15, VWD = 140;
DEV u32 wd_ptr(const Ctx& c, int other) { return OFF_WD + (c.db * 4 + 2 * (c.w & 1) + ((c.w >> 1) ^ other)) * (KS2 * 1024) + (c.w >> 1) * 4096; }
template <int N> DEV void ld_wd(const Ctx& c, u32 v16, u32 wd0, u32 wd1, u64 lanes) {
  constexpr int OFF = ((N >> 1) & 3) * 1024;
  if constexpr (N < NWDA)
    asm volatile("s_mov_b64 exec, %3\n\tbuffer_load_dwordx4 a[%c5:%c6], %0, %1, %2 offen offset:%c4 sc1\n\ts_mov_b64 exec, -1"
                 :: "v"(v16), "s"(c.rs), "s"((N & 1) ? wd1 : wd0), "s"(lanes), "i"(OFF), "i"(192 + 4 * N), "i"(195 + 4 * N) : "memory");
  else
    asm volatile("s_mov_b64 exec, %3\n\tbuffer_load_dwordx4 v[%c5:%c6], %0, %1, %2 offen offset:%c4 sc1\n\ts_mov_b64 exec, -1"
                 :: "v"(v16), "s"(c.rs), "s"((N & 1) ? wd1 : wd0), "s"(lanes), "i"(OFF), "i"(VWD + 4 * (N - NWDA)), "i"(VWD + 3 + 4 * (N - NWDA)) : "memory");
}
template <int V> DEV void mm_wv(f32x4& acc, const u32x4& b) {
  asm volatile("v_mfma_f32_16x16x32_bf16 %0, v[%c2:%c3], %1, %0" : "+v"(acc) : "v"(b), "i"(V), "i"(V + 3));
}

struct ConvIn { u32x4 x; u32x4 st[4]; };
template <int LAYER> DEV void conv_load(const Ctx& c, int half, ConvIn& in, u32 skip = 0) {
  const u32 v = c.tid * 16;
  if constexpr (LAYER == 0) {
    asm volatile("buffer_load_dwordx4 %0, %3, %4, %5 offen sc1\n\tbuffer_load_dwordx4 %1, %3, %4, %6 offen sc1\n\tbuffer_load_dwordx4 %2, %3, %4, %7 offen sc1"
                 : "=&v"(in.x), "=&v"(in.st[0]), "=&v"(in.st[1])
                 : "v"(v), "s"(c.rs), "s"(OFF_XP + c.cp * 8192 + half * 4096), "s"(OFF_ST1 + half * 8192), "s"(OFF_ST1 + half * 8192 + 4096)
                 : "memory");
  } else {
    const u32 so = OFF_ST3 + half * 16384;
    asm volatile("s_cmp_lg_u32 %12, 0\n\ts_cbranch_scc1 1f\n\t"
                 "buffer_load_dwordx4 %1, %5, %6, %8 offen sc1\n\tbuffer_load_dwordx4 %2, %5, %6, %9 offen sc1\n\t"
                 "buffer_load_dwordx4 %3, %5, %6, %10 offen sc1\n\tbuffer_load_dwordx4 %4, %5, %6, %11 offen sc1\n\tbuffer_load_dwordx4 %0, %5, %6, %7 offen sc1\n1:"
                 : "=&v"(in.x), "=&v"(in.st[0]), "=&v"(in.st[1]), "=&v"(in.st[2]), "=&v"(in.st[3])
                 : "v"(v), "s"(c.rs), "s"(OFF_XP + 2097152 + c.cp * 8192 + half * 4096), "s"(so), "s"(so + 4096), "s"(so + 8192), "s"(so + 12288), "s"(skip)
                 : "memory", "scc");
  }
}
template <int LAYER, int N> DEV void conv_wait(ConvIn& in) {
  if constexpr (LAYER == 0) asm volatile("s_waitcnt vmcnt(%3)" : "+v"(in.x), "+v"(in.st[0]), "+v"(in.st[1]) : "i"(N) : "memory");
  else asm volatile("s_waitcnt vmcnt(%5)" : "+v"(in.x), "+v"(in.st[0]), "+v"(in.st[1]), "+v"(in.st[2]), "+v"(in.st[3]) : "i"(N) : "memory");
}
template <int LAYER> DEV void conv_norm(LAS char* pu, const ConvIn& in, u32 dsk, f32x4 (&base)[2]) {
  f32x2 st[4];
  if (LAYER == 0) {
#pragma unroll
    for (int i = 0; i < 2; ++i) { const f32x4 v = __builtin_bit_cast(f32x4, in.st[i]); st[2 * i] = v.xy; st[2 * i + 1] = v.zw; }
  } else {
#pragma unroll
    for (int i = 0; i < 4; ++i) {
      const u32x2 lo = {in.st[i].x, in.st[i].y}, hi = {in.st[i].z, in.st[i].w};
      st[i] = f32x2{(float)__builtin_bit_cast(double, lo), (float)__builtin_bit_cast(double, hi)};
    }
  }
  const f32x2 dd = {bf_lo(dsk), bf_hi(dsk)};
  f32x2 u[4], x[4];
#pragma unroll
  for (int i = 0; i < 4; ++i) {
    const f32x2 me = __builtin_elementwise_fma(st[i], f32x2{-1.f / DM, 1.f / DM}, f32x2{0.f, 1e-5f});
    const float r = __builtin_amdgcn_rsqf(fmaxf(__builtin_fmaf(-me.x, me.x, me.y), 1e-5f));
    x[i] = f32x2{bf_lo(in.x[i]), bf_hi(in.x[i])};
    u[i] = (x[i] + me.x) * r;
  }
  *(LAS u32x2*)pu = u32x2{pk_bf16(u[0].x, u[1].x), pk_bf16(u[2].x, u[3].x)};
  *(LAS u32x2*)(pu + U_CH) = u32x2{pk_bf16(u[0].y, u[1].y), pk_bf16(u[2].y, u[3].y)};
#pragma unroll
  for (int i = 0; i < 4; ++i) { const f32x2 bs = __builtin_elementwise_fma(u[i], dd, x[i]); base[0][i] = bs.x; base[1][i] = bs.y; }
  asm volatile("" : "+v"(base[0]), "+v"(base[1]));
}
template <int H> DEV void lds8h(u32x4& d, LAS char* p) {
  const u32x2 r = *(LAS u32x2*)(p + 8 * H);
  if constexpr (H == 0) { d.x = r.x; d.y = r.y; } else { d.z = r.x; d.w = r.y; }
}
DEV void ldsA(u32x4& d, LAS char* p) { lds8h<0>(d, p); PIN(); lds8h<1>(d, p); PIN(); }
constexpr int tri_j(int m) { return m < 4 ? 0 : (m < 7 ? 1 : (m < 9 ? 2 : 3)); }
constexpr int tri_i(int m) { return m < 4 ? m : (m < 7 ? m - 3 : (m < 9 ? m - 5 : 3)); }
template <int B0, bool ZERO, class F>
DEV void conv_tri(LAS char* pa, LAS char* pb, u32x4 (&Ac)[4], f32x4 (&acc)[4], F&& side) {
  u32x4 Bc[4];
#pragma unroll
  for (int j = 0; j < 4; ++j) Bc[j] = lds16(pb + (B0 + j) * 512);
  PIN();
  sfor<4>([&](auto IT) {
    constexpr int it = IT;
    u32x4 An[4], Bn[4];
    sfor<10>([&](auto M) {
      constexpr int m = M, i = tri_i(m), j = tri_j(m);
      if constexpr (it < 3) {
        if constexpr (m < 8) lds8h<m & 1>(An[m >> 1], pa - 64 * (it + 1) + (7 - (m >> 1)) * 512);
        if constexpr (m == 0 || m == 5) { PIN(); Bn[m / 5] = lds16(pb - 64 * (it + 1) + (B0 + m / 5) * 512); }
        if constexpr (m >= 8) Bn[m - 6] = lds16(pb - 64 * (it + 1) + (B0 + m - 6) * 512);
      }
      side(std::integral_constant<int, it * 10 + m>{});
      mm_vv<ZERO && it == 0 && j == 0>(acc[i], Ac[i - j], Bc[j]);
      PIN();
    });
    if constexpr (it < 3) {
#pragma unroll
      for (int j = 0; j < 4; ++j) { Ac[j] = An[j]; Bc[j] = Bn[j]; }
    }
  });
}
template <int R> DEV void cross_first(LAS char* pa, LAS char* pb, u32x4 (&Ac)[7], u32x4 (&Bc)[4]) {
  if constexpr (R == 0) Bc[0] = lds16(pb);
  else if constexpr (R < 9) lds8h<(R - 1) & 1>(Ac[3 + (R - 1) / 2], pa + (3 - (R - 1) / 2) * 512);
  else if constexpr ((R - 9) % 3 == 0) Bc[1 + (R - 9) / 3] = lds16(pb + (1 + (R - 9) / 3) * 512);
  else lds8h<(R - 9) % 3 - 1>(Ac[2 - (R - 9) / 3], pa + (4 + (R - 9) / 3) * 512);
}
template <class F>
DEV void conv_cross(LAS char* pa, LAS char* pb, f32x4 (&acc)[4], u32x4 (&Ac)[7], u32x4 (&Bc)[4], u32x4 (&At)[4], F&& side) {
  sfor<4>([&](auto IT) {
    constexpr int it = IT;
    u32x4 An[7], Bn[4];
    sfor<16>([&](auto M) {
      constexpr int m = M, j = m >> 2, i = m & 3;
      if constexpr (it < 3) {
        if constexpr (m < 14) lds8h<m & 1>(An[6 - (m >> 1)], pa - 64 * (it + 1) + (m >> 1) * 512);
        if constexpr (m == 0 || m == 8) { PIN(); Bn[m / 8] = lds16(pb - 64 * (it + 1) + (m / 8) * 512); }
        if constexpr (m >= 14) Bn[m - 12] = lds16(pb - 64 * (it + 1) + (m - 12) * 512);
      } else if constexpr (m < 8) {
        lds8h<m & 1>(At[m >> 1], pa + (7 - (m >> 1)) * 512);
      }
      side(std::integral_constant<int, it * 16 + m>{});
      mm_vv<it == 0 && j == 0>(acc[i], Ac[3 + i - j], Bc[j]);
      PIN();
    });
    if constexpr (it < 3) {
#pragma unroll
      for (int k = 0; k < 7; ++k) Ac[k] = An[k];
#pragma unroll
      for (int k = 0; k < 4; ++k) Bc[k] = Bn[k];
    }
  });
}
struct ConvF { LAS char *pa, *pb, *pu, *pw, *pr; u32 vo, so, xa, ldsw, v16; f32x4 a[2], b[2]; };
DEV u32 xa_ptr(const Ctx& c, int layer) { return OFF_XC + layer * 2097152 + (c.rb * 4 + c.w) * 16384; }
DEV void conv_setup(const Ctx& c, int layer, ConvF& r) {
  const int qq = c.tid & 3, ii = (c.tid >> 2) & 15, kch = c.w >> 1, khalf = c.w & 1;
  r.pa = c.lds + L_KC + kch * KC_CH + (c.l15 & 3) * KC_COPY + 2 * (2044 - 16 * (8 * khalf + 1) - (c.l15 & 12) + 8 * c.q) - 7 * 512 - 192;
  r.pb = c.lds + L_U + kch * U_CH + 2 * (256 + 16 * (c.l15 - 8 * khalf - 1) + 8 * c.q) - 192;
  r.pu = c.lds + L_U + 512 + c.tid * 8;
  r.pw = c.lds + L_P + c.w * 4096 + (c.q * 16 + (c.l15 ^ (c.q * 4))) * 16;
  r.pr = c.lds + L_P + c.w * 1024 + (qq * 16 + (ii ^ (qq * 4))) * 16;
  r.vo = (c.tid >> 2) * 16384 + (c.tid & 1) * 32 + (c.tid & 2) * 8;
  r.so = OFF_XC + layer * 2097152 + c.cp * 64;
  r.xa = xa_ptr(c, layer); r.ldsw = c.lds0 + L_RING + c.w * 4096; r.v16 = c.lane * 16;
  asm volatile("" : "+v"(r.pa), "+v"(r.pb), "+v"(r.pu), "+v"(r.pw), "+v"(r.pr), "+v"(r.vo), "+v"(r.v16));
  asm volatile("" : "+s"(r.so), "+s"(r.xa), "+s"(r.ldsw));
}
DEV void conv_pad(const Ctx& c) {
  u32x2 z = {0u, 0u};
  asm volatile("" : "+v"(z));
  *(LAS u32x2*)(c.lds + L_U + c.lane * 8) = z; *(LAS u32x2*)(c.lds + L_U + U_CH + c.lane * 8) = z;
}
DEV void conv_finish2(ConvF& r) {
#pragma unroll
  for (int ch = 0; ch < 2; ++ch) { r.a[ch] = *(LAS f32x4*)(r.pr + ch * 8192); r.b[ch] = *(LAS f32x4*)(r.pr + ch * 8192 + 4096); }
}
DEV void conv_finish3(const Ctx& c, int half, const ConvF& r, const f32x4 (&base)[2]) {
  u32x2 o[2];
#pragma unroll
  for (int ch = 0; ch < 2; ++ch) {
    const f32x4 t = (r.a[ch] + base[ch]) + r.b[ch];
    o[ch].x = pk_bf16(t.x, t.y); o[ch].y = pk_bf16(t.z, t.w);
  }
  const u32 r0 = dpp_u<0xB1>(selu(M_B0, o[1].x, o[0].x)), r1 = dpp_u<0xB1>(selu(M_B0, o[1].y, o[0].y));
  const u32x4 v = {selu(M_B0, o[0].x, r0), selu(M_B0, o[0].y, r1), selu(M_B0, r0, o[1].x), selu(M_B0, r1, o[1].y)};
  st16(c.rs, r.vo, r.so + half * 1048576, v);
}
DEV u32 res_ptr(const Ctx& c, int layer) { return OFF_XC + layer * 2097152 + (c.rt * 2 + (c.w >> 1)) * 16384 + c.db * 2048 + (c.w & 1) * 1024; }

struct G1 {
  u32 xa;
  u32 res, ldsr;
  LAS char* ring[2];
  u32 ldsw;
  u32 v16;
  u64 lanes;
  u32 wd[6][2];
  u32 vst, sst;
  u32 hsrc, hsrc5;
  LAS char* pk;
  LAS char* pws;
  LAS char *rd[4], *prs, *prs1, *exw, *exr, *sts, *stw;
  u32 vo, so, soh, va, sa, eoff, fA, fG, pA, pG, sst2, so1;
  int layer;
};
DEV void ffn_scalars(const Ctx& c, G1& g) {
  const int dfin = 2 * (c.w & 1) + (c.w >> 1);
  g.xa = xa_ptr(c, 0);
  g.res = res_ptr(c, 0);
  g.ldsw = c.lds0 + L_RING + c.w * 4096;
  g.lanes = ~0ull;
#pragma unroll
  for (int i = 0; i < 6; ++i) {
    g.wd[i][0] = wd_ptr(c, 0) + i * 8192 - (i == 5 ? (c.w >> 1) * 2048 : 0); g.wd[i][1] = wd_ptr(c, 1) + i * 8192 - (i == 5 ? (c.w >> 1) * 2048 : 0);
    asm volatile("" : "+s"(g.wd[i][0]), "+s"(g.wd[i][1]));
  }
  g.sst = OFF_H + c.rb * 4 * (KS2 * 1024) + (c.cb * 11 + 3 * c.w) * 256;
  g.sst2 = c.w == 3 ? 0x7f000000u : g.sst;
  g.hsrc = OFF_H + (c.rt * 2 + (c.w & 1)) * (KS2 * 1024) + (c.w >> 1) * 4096;
  g.hsrc5 = g.hsrc + 5 * 8192 - (c.w >> 1) * 2048;
  g.ldsr = c.lds0 + L_RES + c.w * 1024;
  g.so = OFF_XP + 2097152 + (c.db * 32 + dfin * 8) * 8192 + c.rt * 128;
  g.soh = g.so + 4096;
  g.so1 = c.rt * 32768 + c.db * 128 + dfin * 32;
  g.sa = c.w == 0 ? OFF_ST3 + c.rt * 128 : 0x7f000000u;
  g.eoff = c.sflag; g.fA = c.sflag; g.fG = c.sflagg; g.pA = c.speek; g.pG = c.speekg;
  asm volatile("" : "+s"(g.ldsw), "+s"(g.ldsr), "+s"(g.so1), "+s"(g.sa));
}
DEV void ffn_next(G1& g) {
  g.xa += 2097152; g.res += 2097152; g.lanes = 1ull;
  g.sst += H_BYTES; g.sst2 += H_BYTES; g.hsrc += H_BYTES; g.hsrc5 += H_BYTES;
  g.so = g.so1; g.soh = g.so1 + 1048576; g.eoff = 0x7f000000u;
  g.fA += 16384; g.fG += 8192; g.pA += 16384; g.pG += 8192;
}
template <int S0, int S1, class F> DEV void wsum_part(LAS char* pws, F&& side) {
  f32x4 ws[3];
  u32x4 ones = {0x3F803F80u, 0x3F803F80u, 0x3F803F80u, 0x3F803F80u};
  if constexpr (S0 > 0) {
#pragma unroll
    for (int i = 0; i < 3; ++i) ws[i] = *(LAS f32x4*)(pws + i * 64);
  }
  asm volatile("s_nop 1" : "+v"(ones));
  sfor<3 * (S1 - S0)>([&](auto N) { mm_av<(S0 == 0 && N < 3), AW(N % 3, S0 + N / 3)>(ws[N % 3], ones); side(N); });
  asm volatile("s_nop 7" : "+v"(ws[0]), "+v"(ws[1]), "+v"(ws[2]));
#pragma unroll
  for (int i = 0; i < 3; ++i) *(LAS f32x4*)(pws + i * 64) = ws[i];
}
template <int LAYER> DEV void conv_phase(Ctx& c, ConvF& cf, G1& g, u32 dsk, ConvIn& inA, ConvIn& inB) {
  LAS char* const pa = cf.pa + 192;
  LAS char* const pb = cf.pb + 192;
  f32x4 baseA[2], baseB[2];
  u32x4 Ac[4];
  if constexpr (LAYER == 1) {
#pragma unroll
    for (int j = 0; j < 4; ++j) ldsA(Ac[j], pa + (7 - j) * 512);
  }
  conv_wait<LAYER, LAYER == 0 ? 4 : 0>(inA);
  STAMP(4 + LAYER * 20);
  conv_norm<LAYER>(cf.pu, inA, dsk, baseA);
  SYNC_LDS();
  if constexpr (LAYER == 0) {
#pragma unroll
    for (int j = 0; j < 4; ++j) ldsA(Ac[j], pa + (7 - j) * 512);
  } else {
    flag_st<E_DOWN + 1>(c, c.sflag);
  }
  STAMP(5 + LAYER * 20);
  f32x4 accA[4], acc[4];
  u32x4 Ax[7], Bx[4];
  conv_tri<0, true>(pa, pb, Ac, accA, [&](auto K) {
    if constexpr (LAYER == 0 && K == 16) peek<false, E_W>(c, c.speek);
    if constexpr (K >= 30 && K < 39) { cross_first<2 * (K - 30)>(pa, pb, Ax, Bx); PIN(); cross_first<2 * (K - 30) + 1>(pa, pb, Ax, Bx); }
  });
  STAMP(6 + LAYER * 20);
  conv_cross(pa, pb, acc, Ax, Bx, Ac, [&](auto K) {
    if constexpr (K == 3) asm volatile("s_nop 3" : "+v"(accA[0]), "+v"(accA[1]), "+v"(accA[2]), "+v"(accA[3]));
    if constexpr (K < 3) *(LAS f32x4*)(cf.pw + K * 1024) = accA[K];
    if constexpr (K == 3) *(LAS f32x4*)(cf.pw + K * 1024) = accA[K];
    if constexpr (K == 9) { if constexpr (LAYER == 0) poll_edge<false, 0, E_W>(c, c.speek); SYNC_LDS(); conv_finish2(cf); }
    if constexpr (K == 14) conv_finish3(c, 0, cf, baseA);
    if constexpr (LAYER == 0) {
      if constexpr (K >= 16 && K % 4 == 0) ld_wgu<K / 4 - 4>(c);
    } else {
      if constexpr (K == PK_B) peek<false, E_DOWN + 1>(c, c.speek);
      if constexpr (K == CK_B) { poll_edge<false, (PK_B < 14), E_DOWN + 1>(c, c.speek); BARRIER(); FENCE(); conv_load<1>(c, 1, inB); }
    }
  });
  if constexpr (LAYER == 1 && CK_B >= 64) poll_edge<false, 0, E_DOWN + 1>(c, c.speek);
  vm_wait<LAYER == 0 ? 12 : (CK_B >= 64 ? 0 : 5)>();
  BARRIER(); FENCE();
  if constexpr (LAYER == 1 && CK_B >= 64) conv_load<1>(c, 1, inB);
  flag_st<E_CONV + 4 * LAYER>(c, c.sflag);
  STAMP(7 + LAYER * 20);
  if constexpr (LAYER == 0) sfor<4>([&](auto N) { ld_wgu<12 + N>(c); });
  conv_wait<LAYER, LAYER == 0 ? 17 : 1>(inB);
  conv_norm<LAYER>(cf.pu + 2048, inB, dsk, baseB);
  SYNC_LDS();
  STAMP(8 + LAYER * 20);
  conv_tri<4, false>(pa, pb, Ac, acc, [&](auto K) {
    if constexpr (LAYER == 0) { if constexpr (K % 4 == 0) ld_wgu<16 + K / 4>(c); }
    else if constexpr (K == PK_A) peek<false, E_CONV + 4>(c, c.speek);
  });
  MM_DONE4(acc);
#pragma unroll
  for (int i = 0; i < 4; ++i) *(LAS f32x4*)(cf.pw + i * 1024) = acc[i];
  if constexpr (LAYER == 0) { peek<false, E_CONV>(c, c.speek); sfor<4>([&](auto N) { ld_wgu<26 + N>(c); }); }
  else poll_edge<false, 0, E_CONV + 4>(c, c.speek);
  SYNC_LDS();
  conv_finish2(cf);
  if constexpr (LAYER == 1) dma4<0, 0>(c.rs, cf.v16, cf.xa, cf.ldsw);
  conv_finish3(c, 1, cf, baseB);
  STAMP(9 + LAYER * 20);
  if constexpr (LAYER == 0) {
    sfor<3>([&](auto N) { ld_wgu<30 + N>(c); });
    ffn_scalars(c, g);
    asm volatile("" : "+s"(g.xa), "+s"(g.res), "+s"(g.sst), "+s"(g.sst2), "+s"(g.hsrc), "+s"(g.hsrc5), "+s"(g.so), "+s"(g.soh));
    poll_edge<false, 8, E_CONV>(c, c.speek);
    BARRIER(); FENCE();
    dma4<0, 0>(c.rs, cf.v16, cf.xa, cf.ldsw); dma4<4096, 16384>(c.rs, cf.v16, cf.xa, cf.ldsw);
    vm_wait<17>();
    wsum_part<0, 8>(c.lds + L_WS + c.w * 192 + c.q * 16, [&](auto N) { if constexpr (N < 15) ld_wgu<33 + N>(c); });
  } else {
    dma4<4096, 16384>(c.rs, cf.v16, cf.xa, cf.ldsw);
  }
}

DEV void h_refetch(const Ctx& c, u32 vst, u32 sst) {
}
template <int N> DEV void ld_wd(const Ctx& c, const G1& g) { ld_wd<N>(c, g.v16, g.wd[N >> 3][0], g.wd[N >> 3][1], g.lanes); }
template <int HALF, bool STATS, int S>
DEV void g1_step(const Ctx& c, const G1& g, f32x4 (&acc)[3][4], u32x4 (&Bc)[4], u32& td) {
  if constexpr (S == 3) {
    vm_wait<4>(); BARRIER(); FENCE();
    if constexpr (STATS && HALF == 0) flag_st<E_CONV + 1>(c, g.fA);
    if constexpr (STATS && HALF == 1) flag_st<0>(c, g.fG);
    if constexpr (HALF == 1) h_refetch(c, g.vst, g.sst);
  }
  if constexpr (S == 7) { vm_wait<HALF ? 8 : 9>(); BARRIER(); FENCE(); }
  if constexpr (HALF == 0 && S == 11) { vm_wait<16>(); BARRIER(); FENCE(); }
  if constexpr (HALF == 1 && S == 11) { if constexpr (!STATS) poll_edge<true, 6, 0>(c, g.pG); vm_wait<11>(); BARRIER(); FENCE(); }
  u32x4 Bn[4];
  sfor<12>([&](auto M) {
    constexpr int m = M, ct = m / 3, k = m % 3;
    if constexpr (S < 15 && k < 2) {
      constexpr int slot = (HALF * 4 + (S + 1) / 4) % 7;
      const u32x2 r = tr_read(g.ring[slot >> 2] + (slot & 3) * 16384 + ct * 4096 + ((S + 1) & 3) * 1024 + k * 512);
      if constexpr (k == 0) { Bn[ct].x = r.x; Bn[ct].y = r.y; } else { Bn[ct].z = r.x; Bn[ct].w = r.y; }
    }
    if constexpr (k == 2 && (ct & 1) == 0) {
      constexpr int n = 2 * (S - 1) + (ct >> 1);
      if constexpr (S >= 1 && S <= 4) dmap<HALF * 1048576 + 8192 + (n >> 2) * 4096, ((HALF * 4 + 2 + (n >> 2)) % 7) * 16384, n & 3>(c.rs, g.v16, g.xa, g.ldsw, td);
      if constexpr (S == 5 && ct == 0) dma1<HALF * 1048576, HALF * 4096>(c.rs, g.v16, g.res, g.ldsr);
      if constexpr (HALF == 1 && S >= 11) dmap<((n - 20) >> 2) * 8192, (((n - 20) >> 2) + 1) * 16384, (n - 20) & 3>(c.rs, g.v16, g.hsrc, g.ldsw, td);
      if constexpr (HALF == 0 && n >= 9) ld_wd<n - 9>(c, g);
      if constexpr (HALF == 1 && n >= 9 && n <= 19) ld_wd<24 + n>(c, g);
    }
    if constexpr (HALF == 0 && S >= 13 && m == 5) {
      u32 t;
      asm volatile("s_add_u32 %0, %4, %5\n\tbuffer_store_dwordx4 %1, %2, %3, %0 offen offset:%c6 sc1\n\ts_nop 1"
                   : "=&s"(t) : "v"(u32x4{~0u, ~0u, ~0u, ~0u}), "v"(g.vst), "s"(c.rs), "s"(S == 15 ? g.sst2 : g.sst), "i"(HB_OFF), "i"((S - 13) * 256) : "memory", "scc");
    }
    if constexpr (HALF == 0 && m == 5 && S >= 6 && S <= 12) ld_wd<15 + S>(c, g);
    if constexpr (!STATS && HALF == 0 && S == 12 && m == 11) peek<false, E_CONV + 1>(c, g.pA);
    if constexpr (!STATS && HALF == 1 && S == 7 && m == 11) peek<true, 0>(c, g.pG);
    if constexpr (STATS && k == 2) mm_vv<S == 0>(acc[k][ct], Bc[ct], Bc[ct]);
    else if constexpr (!(STATS && k == 0 && S >= 13)) mm_av<S == 0, AW(k, S)>(acc[k][ct], Bc[ct]);
    PIN();
  });
  if constexpr (STATS && S == 13) {
    u32x4 B14[4], B15[4];
#pragma unroll
    for (int ct = 0; ct < 4; ++ct) B14[ct] = Bn[ct];
    g1_step<HALF, STATS, 14>(c, g, acc, Bn, td);
#pragma unroll
    for (int ct = 0; ct < 4; ++ct) B15[ct] = Bn[ct];
    auto held = [&](auto N) { mm_av<false, AW(0, 13 + N / 4)>(acc[0][N & 3], N < 4 ? Bc[N & 3] : (N < 8 ? B14[N & 3] : B15[N & 3])); PIN(); };
#define HELD(n) held(std::integral_constant<int, n>{})
    HELD(0); HELD(1); HELD(2);
    asm volatile("s_nop 4" : "+v"(acc[2][0]), "+v"(acc[2][1]), "+v"(acc[2][2]), "+v"(acc[2][3]), "+v"(acc[1][0]), "+v"(acc[1][1]), "+v"(acc[1][2]), "+v"(acc[1][3]));
    float sm[4], sq[4];
    sfor<4>([&](auto CT) {
      sm[CT] = acc[1][CT][0];
      sq[CT] = sel(M_B1, sel(M_B0, acc[2][CT][0], acc[2][CT][1]), sel(M_B0, acc[2][CT][2], acc[2][CT][3]));
      asm volatile("" : "+v"(sm[CT]), "+v"(sq[CT]));
      held(std::integral_constant<int, 3 + CT>{});
    });
    swap32f(sm[0], sm[2]); swap32f(sm[1], sm[3]); PIN(); HELD(7);
    swap32f(sq[0], sq[2]); swap32f(sq[1], sq[3]); PIN(); HELD(8);
    swap16f(sm[2], sm[3]); swap16f(sq[0], sq[1]); PIN(); HELD(9);
    swap16f(sq[2], sq[3]);
    const float mu = sm[3] * (-1.f / DM);
    float e2 = sel(M_B3, sel(M_B2, sq[0], sq[1]), sel(M_B2, sq[2], sq[3]));
    asm volatile("" : "+v"(e2)); HELD(10);
    const float vr = fmaxf(__builtin_fmaf(-mu, mu, __builtin_fmaf(e2, 1.f / DM, 1e-5f)), 1e-5f);
    float r = __builtin_amdgcn_rsqf(vr);
    asm volatile("" : "+v"(r)); HELD(11);
    const float c1 = (-2.f * 0.7978845608f * 1.44269504f) * r;
    float r2 = r * r;
    asm volatile("" : "+v"(r2));
    *(LAS f32x4*)(c.lds + L_ST + g.v16) = f32x4{mu, vr, c1, (c1 * 0.044715f) * r2};
    asm volatile("s_nop 3" : "+v"(acc[0][0]), "+v"(acc[0][1]), "+v"(acc[0][2]), "+v"(acc[0][3]));
  } else if constexpr (S < 15) {
#pragma unroll
    for (int ct = 0; ct < 4; ++ct) Bc[ct] = Bn[ct];
    g1_step<HALF, STATS, S + 1>(c, g, acc, Bc, td);
  } else if constexpr (!STATS) {
    MM_DONE4(acc[2]); asm volatile("" : "+v"(acc[0][3]), "+v"(acc[1][3]), "+v"(acc[0][2]), "+v"(acc[1][2]), "+v"(acc[0][1]), "+v"(acc[1][1]));
  }
}
template <int HALF>
DEV void g1_main(const Ctx& c, const G1& g, f32x4 (&acc)[3][4], u32x4 (&Bc)[4]) {
  u32 td;
  if (c.w == 3) {
    g1_step<HALF, true, 0>(c, g, acc, Bc, td);
  } else {
    g1_step<HALF, false, 0>(c, g, acc, Bc, td);
    if constexpr (HALF == 0) poll_edge<false, 9, E_CONV + 1>(c, g.pA);
  }
}
template <int HALF, class F>
DEV void g1_epilogue(const Ctx& c, const G1& g, const f32x4 (&acc)[3][4], F&& next) {
  f32x4 k4[4], wsum[3];
#pragma unroll
  for (int i = 0; i < 3; ++i) wsum[i] = *(LAS f32x4*)(g.pws + i * 64);
  const u32 vst = g.vst, sst = g.sst + HALF * 64 * (KS2 * 1024);
  const u32 sst2 = g.sst2 + HALF * 64 * (KS2 * 1024);
  SYNC_LDS();
  STAMP(12 + 2 * HALF + g.layer * 20);
#pragma unroll
  for (int ct = 0; ct < 4; ++ct) k4[ct] = *(LAS f32x4*)(g.pk + ct * 256);
  u32 td;
  auto stage = [&](auto N) {
    if constexpr (HALF == 0) {
      if constexpr (N < 8) dmap<1048576 + (N >> 2) * 4096, (4 + (N >> 2)) * 16384, N & 3>(c.rs, g.v16, g.xa, g.ldsw, td);
      else if constexpr (N >= 9 && N < 14) ld_wd<19 + N>(c, g);
    } else if constexpr (N < 12 && N % 2 == 0) {
      constexpr int j = 10 + N / 2;
      if constexpr (j < 12) dmap<2 * 8192 + 2048, 3 * 16384 + 2048, j - 10>(c.rs, g.v16, g.hsrc, g.ldsw, td);
      else dmap<3 * 8192, 4 * 16384, j - 12>(c.rs, g.v16, g.hsrc, g.ldsw, td);
    }
    PIN();
  };
  sfor<3>([&](auto I) {
    constexpr int i = I;
    u32 hv[4];
    sfor<2>([&](auto P) {
      constexpr int pi = P;
      constexpr int n0 = 6 * i + 3 * pi;
      f32x2 gg[2], gu[2], ex[2];
      if constexpr (n0 == 15) { next(); PIN(); }
      stage(std::integral_constant<int, n0>{});
#pragma unroll
      for (int e = 0; e < 2; ++e) {
        const f32x4 k = k4[2 * pi + e];
        gg[e] = __builtin_elementwise_fma(k.xx, wsum[i].xy, acc[i][2 * pi + e].xy);
        const f32x2 ar = gg[e] * __builtin_elementwise_fma(gg[e] * gg[e], k.ww, k.zz);
        ex[e] = f32x2{__builtin_amdgcn_exp2f(ar.x), __builtin_amdgcn_exp2f(ar.y)};
      }
      PIN();
      stage(std::integral_constant<int, n0 + 1>{});
#pragma unroll
      for (int e = 0; e < 2; ++e) {
        const f32x4 k = k4[2 * pi + e];
        gu[e] = gg[e] * __builtin_elementwise_fma(k.xx, wsum[i].zw, acc[i][2 * pi + e].zw);
        const f32x2 dn = __builtin_elementwise_fma(ex[e], k.yy, k.yy);
        const f32x2 h = gu[e] * f32x2{__builtin_amdgcn_rcpf(dn.x), __builtin_amdgcn_rcpf(dn.y)};
        hv[2 * pi + e] = pk_bf16(h.x, h.y);
      }
      PIN();
      stage(std::integral_constant<int, n0 + 2>{});
    });
    swap32(hv[0], hv[2]); swap32(hv[1], hv[3]);
    swap16(hv[0], hv[1]); swap16(hv[2], hv[3]);
    st16(c.rs, vst + i * 256, i == 2 ? sst2 : sst, u32x4{hv[0], hv[1], hv[2], hv[3]});
    PIN();
  });
}

DEV u32 umax3(u32 a, u32 b, u32 d) { return max(max(a, b), d); }
template <int K> DEV LAS char* g2_frag(const G1& g, int other, int ks) { return g.rd[other * 2 + ((K + 1) >> 2)] + ((K + 1) & 3) * 16384 + ks * 1024; }
template <int K> DEV void g2_verify(const Ctx& c, const G1& g, u32x4 (&B)[8], u32 mx) {
  constexpr int NK = K < 5 ? 4 : 2;
  if (__builtin_expect(__builtin_amdgcn_ballot_w64(mx == 0xffffffffu) != 0ull, 0)) {
#pragma unroll 1
    for (u32 sp = 0; sp < SPIN_CAP; ++sp) {
      LGKM0();
      if constexpr (K < 5) dma4<HB_OFF + K * 8192, (K + 1) * 16384>(c.rs, g.v16, g.hsrc, g.ldsw);
      else { dma1<HB_OFF, 6 * 16384>(c.rs, g.v16, g.hsrc5, g.ldsw); dma1<HB_OFF + 1024, 6 * 16384 + 1024>(c.rs, g.v16, g.hsrc5, g.ldsw); }
      vm_wait<0>();
      u32 m = 0;
#pragma unroll
      for (int ks = 0; ks < NK; ++ks) { B[2 * ks] = lds16(g2_frag<K>(g, 0, ks)); m = umax3(umax3(m, B[2 * ks].x, B[2 * ks].y), B[2 * ks].z, B[2 * ks].w); }
      if (!__builtin_amdgcn_ballot_w64(m == 0xffffffffu)) break;
      __builtin_amdgcn_s_sleep(2);
    }
  }
  FENCE();
}
template <int HALF, int C>
DEV void g2_chunk(const Ctx& c, const G1& g, int layer, f32x4 (&acc)[4], u32x4 (&Bc)[8], u32& td) {
  if constexpr (HALF == 0 && C == 2) { vm_wait<8>(); BARRIER(); FENCE(); }
  static_assert(SP_H >= 32 && SP_H + 8 * SP_D >= 48, "ring slots");
  if constexpr (HALF == 0 && C == 3) { vm_wait<(SP_H < 48 ? (47 - SP_H) / SP_D + 1 : 0)>(); BARRIER(); FENCE(); }
  constexpr int NK = C < 5 ? 4 : 2, NKN = C < 4 ? 4 : 2;
  u32x4 Bn[8];
  u32 mx = 0;
  sfor<NK * 4>([&](auto M) {
    constexpr int m = M, ks = (m >> 3) * 2 + ((m >> 1) & 1), ct = (m >> 2) & 1, dt = m & 1, n = (C * 4 + ks) * 2 + dt;
    if constexpr (HALF == 0) {
      if constexpr (C < 5 && m < NKN * 2) Bn[m] = lds16(g2_frag<C + 1>(g, m & 1, m >> 1));
      if constexpr (C == 0 && m % 3 == 0) {
        if constexpr (m < 12) dmap<4 * 8192, 5 * 16384, m / 3>(c.rs, g.v16, g.hsrc, g.ldsw, td);
        else dmap<0, 6 * 16384, m / 3 - 4>(c.rs, g.v16, g.hsrc5, g.ldsw, td);
      }
      constexpr int x = C * 16 + m - SP_H;
      if constexpr (x >= 0 && x % SP_D == 0 && x / SP_D < 12) dmap<HB_OFF + (x / SP_D / 4) * 8192, (x / SP_D / 4 + 1) * 16384, (x / SP_D) & 3>(c.rs, g.v16, g.hsrc, g.ldsw, td);
    } else if constexpr (C < 5) {
      if constexpr (C == 0 && (m == 4 || m >= 11)) {
        constexpr int j = m == 4 ? 0 : m - 10;
        if constexpr (j < 4) dma1<HB_OFF + 4 * 8192 + j * 1024, 5 * 16384 + j * 1024>(c.rs, g.v16, g.hsrc, g.ldsw);
        else dma1<HB_OFF + (j - 4) * 1024, 6 * 16384 + (j - 4) * 1024>(c.rs, g.v16, g.hsrc5, g.ldsw);
      }
      if constexpr (m == 0) vm_wait<C == 0 ? 9 : (C == 1 ? 11 : (C == 2 ? (FL_A == 2 ? 6 : 7) : (C == 3 ? (FL_A == 2 ? 3 : 2) : 1)))>();
      if constexpr (m < NKN) Bn[2 * m] = lds16(g2_frag<C + 1>(g, 0, m));
      if constexpr (m >= 5 && m < 5 + NKN) { mx = umax3(umax3(mx, Bn[2 * (m - 5)].x, Bn[2 * (m - 5)].y), Bn[2 * (m - 5)].z, Bn[2 * (m - 5)].w); }
      if constexpr (m == 9) {
        g2_verify<C + 1>(c, g, Bn, mx);
        BARRIER(); FENCE();
        if constexpr (C == FL_A) flag_st<E_DOWN>(c, g.eoff);
      }
      if constexpr (m >= 9 && m < 9 + NKN) Bn[2 * (m - 9) + 1] = lds16(g2_frag<C + 1>(g, 1, m - 9));
    }
    if constexpr (HALF == 1 && C * 16 + m == PK_D) peek<false, E_DOWN>(c, c.speek);
    if constexpr (n < NWDA) mm_av<C == 0 && ks == 0 && dt == 1, 192 + 4 * n>(acc[dt * 2 + ct], Bc[ks * 2 + ct]);
    else mm_wv<VWD + 4 * (n - NWDA)>(acc[dt * 2 + ct], Bc[ks * 2 + ct]);
    PIN();
  });
  if constexpr (C < 5) {
#pragma unroll
    for (int m = 0; m < NKN * 2; ++m) Bc[m] = Bn[m];
    g2_chunk<HALF, C + 1>(c, g, layer, acc, Bc, td);
  }
}
template <int HALF> DEV void g2_res(const G1& g, f32x4 (&acc)[4]) {
#pragma unroll
  for (int ct = 0; ct < 2; ++ct) {
    const u32x2 r = tr_read((ct ? g.prs1 : g.prs) + HALF * 4096);
    acc[ct] = f32x4{bf_lo(r.x), bf_hi(r.x), bf_lo(r.y), bf_hi(r.y)};
  }
  asm volatile("" : "+v"(acc[0]), "+v"(acc[1]));
}
DEV void g2_first(const G1& g, u32x4 (&Bc)[8]) {
#pragma unroll
  for (int m = 0; m < 8; ++m) Bc[m] = lds16(g2_frag<0>(g, m & 1, m >> 1));
}
template <int HALF>
DEV void g2_unit(const Ctx& c, const G1& g, const Params& p, int layer, f32x4 (&acc)[4], u32x4 (&Bc)[8]) {
  u32 td;
  g2_chunk<HALF, 0>(c, g, layer, acc, Bc, td);
  constexpr int NSP = (87 - SP_H) / SP_D + 1 < 12 ? (87 - SP_H) / SP_D + 1 : 12;
  if constexpr (HALF == 1 && PK_D == 88) peek<false, E_DOWN>(c, c.speek);
  MM_DONE4(acc);
  STAMP(16 + 2 * HALF + layer * 20);
#pragma unroll
  for (int ct = 0; ct < 2; ++ct) *(LAS f32x4*)(g.exw + HALF * 8192 + ct * 1024) = acc[2 + ct];
  if constexpr (HALF == 0) sfor<12 - NSP>([&](auto X) { dmap<HB_OFF + ((NSP + X) / 4) * 8192, ((NSP + X) / 4 + 1) * 16384, (NSP + X) & 3>(c.rs, g.v16, g.hsrc, g.ldsw, td); });
  SYNC_LDS();
  f32x4 ex[2];
#pragma unroll
  for (int ct = 0; ct < 2; ++ct) ex[ct] = *(LAS f32x4*)(g.exr + HALF * 8192 + ct * 1024);
  if constexpr (HALF == 0) dma4<HB_OFF + 3 * 8192, 4 * 16384>(c.rs, g.v16, g.hsrc, g.ldsw);
  if constexpr (HALF == 1 && PK_D == 89) peek<false, E_DOWN>(c, c.speek);
  u32 v[4];
#pragma unroll
  for (int ct = 0; ct < 2; ++ct) {
    const f32x4 o = acc[ct] + ex[ct];
    v[2 * ct] = pk_bf16(o[0], o[1]); v[2 * ct + 1] = pk_bf16(o[2], o[3]);
  }
  if (layer == 0) {
    float st[4];
#pragma unroll
    for (int ct = 0; ct < 2; ++ct) {
      st[2 * ct] = dot2(v[2 * ct], 0x3F803F80u, dot2(v[2 * ct + 1], 0x3F803F80u, 0.f));
      st[2 * ct + 1] = dot2(v[2 * ct], v[2 * ct], dot2(v[2 * ct + 1], v[2 * ct + 1], 0.f));
    }
    swap32f(st[0], st[2]); swap32f(st[1], st[3]);
    float sA = st[0] + st[2], sB = st[1] + st[3];
    swap16f(sA, sB);
    *(LAS float*)g.stw = sA + sB;
    u32 t0 = dpp_u<0xB1>(selu(M_B0, v[1], v[0])), t1 = dpp_u<0xB1>(selu(M_B0, v[3], v[2]));
    const u32 a0 = selu(M_B0, v[0], t0), a1 = selu(M_B0, t0, v[1]), a2 = selu(M_B0, v[2], t1), a3 = selu(M_B0, t1, v[3]);
    t0 = dpp_u<0x4E>(selu(M_B1, a2, a0)); t1 = dpp_u<0x4E>(selu(M_B1, a3, a1));
    if constexpr (HALF == 1 && CK_D == 0) poll_edge<false, 0, E_DOWN>(c, c.speek);
    SYNC_LDS();
    const f32x4 s4 = *(LAS f32x4*)g.sts;
    st16(c.rs, g.vo, HALF ? g.soh : g.so, u32x4{selu(M_B1, a0, t0), selu(M_B1, a1, t1), selu(M_B1, t0, a2), selu(M_B1, t1, a3)});
    const double sv = (double)((s4.x + s4.y) + (s4.z + s4.w));
    asm volatile("buffer_atomic_add_f64 %0, %1, %2, %3 offen" ::"v"(sv), "v"(g.va), "s"(c.rs), "s"(g.sa + HALF * 16384) : "memory");
  } else {
    swap16(v[0], v[2]); swap16(v[1], v[3]);
    st16<0>(mk_rsrc(p.out, SEQ * DM * 2), g.vo, HALF ? g.soh : g.so, u32x4{v[0], v[1], v[2], v[3]});
  }
  STAMP(17 + 2 * HALF + layer * 20);
}

__global__ void __launch_bounds__(256, 1) __attribute__((amdgpu_num_vgpr(140))) mega_kernel(Params p) {
  asm volatile("" ::: "v255", "a255");
  __shared__ __attribute__((aligned(16))) char smem[LDS_BYTES];
  const int w0 = __builtin_amdgcn_readfirstlane(threadIdx.x >> 6);
  Ctx c;
  c.ws = p.ws; c.lds = (LAS char*)smem; c.lds0 = (u32)(size_t)c.lds;
  c.rs = mk_rsrc(p.ws, WS_BYTES);
  refresh(c, w0);
  STAMP(0);
  u32 dsk;
  ConvIn inA, inB;
  ConvF cf;
  u32 pf_sink;
  {
    u64 pc; u32 len;
    asm volatile("s_getpc_b64 %0\n1:\n\ts_mov_b32 %1, .Lmega_tail-1b" : "=s"(pc), "=s"(len));
    const u32 off = ((c.b >> 3) * 16 + (c.tid & 15)) * 128;
    pf_sink = 0;
    if (c.tid < 16 && off + 4 <= len) pf_sink = *(const GAS u32*)(pc + off);
  }

  {
    const int b = c.b, tid = c.tid, lane = c.lane, w = c.w, q = c.q, l15 = c.l15;
    LAS char* lds = c.lds;
    const int kch = w >> 1, khalf = w & 1;
    const rsrc_t rx = mk_rsrc(p.x0, SEQ * DM * 2), rg = mk_rsrc(p.wg, DM * DF * 2), ru = mk_rsrc(p.wu, DM * DF * 2), rd = mk_rsrc(p.wd, DM * DF * 2);
    const u32x4 r0 = ld16p(rx, lane * 16, (8 * b + 2 * w) * 1024), r1 = ld16p(rx, lane * 16 + 1024, (8 * b + 2 * w) * 1024);
    u32x4 xt[2];
#pragma unroll
    for (int i = 0; i < 2; ++i) xt[i] = ld16p(rx, (tid >> 3) * 1024 + (tid & 7) * 16 + i * 128, (b >> 2) * 32768 + (b & 3) * 256);
    const int dch = 2 * c.cp + kch, pn = dch * NS + 64 * khalf + lane;
    const u16 pA = p.logA[pn], pI = p.Aim[pn], pCr = p.Cre[pn], pCi = p.Cim[pn], pdt = p.logdt[dch];
    const u16 pCr2 = p.Cre[pn ^ 64], pCi2 = p.Cim[pn ^ 64];
    dsk = *(const u32*)(p.Dskip + 2 * c.cp);
    const u32 ep = __hip_atomic_load((GAS u32*)(p.ws + OFF_EPOCH), RLX_AGENT);
    const int cbR = b >> 4, sR = b & 15;
    u32x4 wg[2], wu[2];
    int wrow[2], wtau[2];
#pragma unroll
    for (int i = 0; i < 2; ++i) {
      const int id = tid + 256 * i;
      wrow[i] = (id * 5958) >> 16; wtau[i] = id - wrow[i] * 11;
      const u32 vo = wrow[i] * (DF * 2) + wtau[i] * 16 + (id < 352 ? 0u : 0x40000000u) + sR * (32 * DF * 2) + cbR * 172;
      wg[i] = ld16p(rg, vo, 0); wu[i] = ld16p(ru, vo, 0);
    }
    u32x4 dv[2];
    const int s2 = b >> 2, dq = b & 3;
#pragma unroll
    for (int i = 0; i < 2; ++i) {
      const int krow = (tid >> 4) + 16 * i, U = 4 * s2 + (krow >> 3), cbU = (U * 5958) >> 16, fl = 8 * (U - cbU * 11) + (krow & 7);
      dv[i] = ld16p(rd, (86 * cbU + fl) * 1024 + (tid & 15) * 16 + dq * 256 + (fl < 86 ? 0u : 0x40000000u), 0);
    }
    LAS char* wxt = lds + L_XT + (tid >> 3) * 272 + (tid & 7) * 16;
    LAS char* rxt = lds + L_XT + (tid & 7) * (4 * 272) + (tid >> 3) * 4;
    u32 vst = selu(0x0001000100010001ull, selu(0x0006000600060006ull, 0x7f000000u, OFF_ST3 + b * 128 + w * 32 - 16 + l15 * 16 + q * 4),
                   OFF_ST1 + (b >> 7) * 8192 + (w & 1) * 4096 + ((2 * b + (w >> 1)) & 255) * 16 + q * 4);
    u32 vxp = (tid >> 3) * 8192 + (tid & 7) * 16;
    u32 sxp = OFF_XP + (b & 3) * (64 * 8192) + (b >> 2) * 128;
    LAS char* wi1[2] = {lds + L_IMG1 + wtau[0] * 1056 + wrow[0] * 32, lds + selu(__builtin_amdgcn_ballot_w64(tid < 96), L_DUMMY + tid * 16, L_IMG1 + wtau[1] * 1056 + wrow[1] * 32)};
    LAS char* wi2 = lds + L_IMG2 + ((tid & 15) >> 1) * 1056 + (4 * (tid >> 7) + ((tid >> 4) & 3) + ((tid & 64) ? 16 : 0)) * 32 + (tid & 1) * 16;
    asm volatile("" : "+v"(wxt), "+v"(rxt), "+v"(vst), "+v"(vxp), "+v"(wi1[0]), "+v"(wi1[1]), "+v"(wi2));
    asm volatile("" : "+s"(sxp));
    {
      const u32x4 z = {0u, 0u, 0u, 0u};
      *(LAS u32x4*)(lds + L_KC + (l15 >> 1) * KC_COPY + 4096 + (l15 & 1) * 16) = z;
      *(LAS u32x4*)(lds + L_KT - 16 + (l15 & 1) * KT_CH) = z;
    }
    conv_pad(c);
    flag_setup(c);
    LAS char* ci[11];
#pragma unroll
    for (int i = 0; i < 11; i += 2) {
      const int l0 = 2047 - 8 * tid - i, l1 = l0 - 1;
      const u32 pb = pk_bf16((float)(i >= 8 ? max(l0, 0) : l0), (float)(i >= 8 ? max(l1, 0) : l1));
      const int b0 = (int)(pb & 0xffffu) - (134 << 7) + 128, b1 = (int)(pb >> 16) - (134 << 7) + 128;
      ci[i] = lds + L_KT + 4 * max(b0, min(l0, 128));
      asm volatile("" : "+v"(ci[i]));
      if (i < 10) { ci[i + 1] = lds + L_KT + 4 * max(b1, min(l1, 128)); asm volatile("" : "+v"(ci[i + 1])); }
    }
    PIN();
    STAMP(42);
    asm volatile("" :: "v"(pf_sink));
#pragma unroll
    for (int i = 0; i < 2; ++i) *(LAS u32x4*)(wxt + i * 128) = xt[i];
    {
      float s0 = 0.f, q0 = 0.f, s1 = 0.f, q1 = 0.f;
#pragma unroll
      for (int i = 0; i < 4; ++i) {
        s0 = dot2(r0[i], 0x3F803F80u, s0); q0 = dot2(r0[i], r0[i], q0);
        s1 = dot2(r1[i], 0x3F803F80u, s1); q1 = dot2(r1[i], r1[i], q1);
      }
      swap32f(s0, s1); swap32f(q0, q1);
      float sA = s0 + s1, sB = q0 + q1;
      swap16f(sA, sB);
      float sv = sA + sB;
      sv += dpp_f<0xB1>(sv); sv += dpp_f<0x4E>(sv); sv += dpp_f<0x141>(sv); sv += dpp_f<0x140>(sv);
      __builtin_amdgcn_raw_buffer_store_b32(selu(0x0001000100010001ull, 0u, __builtin_bit_cast(u32, sv)), c.rs, vst, 0, 16);
    }
    SYNC_LDS();
    {
      u32x4 o[2];
#pragma unroll
      for (int i = 0; i < 2; ++i) o[i] = u32x4{*(LAS u32*)(rxt + i * 128), *(LAS u32*)(rxt + i * 128 + 272), *(LAS u32*)(rxt + i * 128 + 544), *(LAS u32*)(rxt + i * 128 + 816)};
      PIN();
      st16(c.rs, vxp, sxp, o[0]); st16(c.rs, vxp, sxp + 32 * 8192, o[1]);
    }
    STAMP(43);
    c.epoch = ep + 1u;

    const float dt = rnd_bf16(expf(bf2f(pdt)));
    const float a2 = -rnd_bf16(expf(bf2f(pA))) * dt * 1.44269504f;
    const float ai = bf2f(pI) * dt;
    const float i2p_hi = 0.15915494f, i2p_lo = (float)(0.15915494309189535 - (double)0.15915494f);
    const float fhi = ai * i2p_hi, flo = __builtin_fmaf(ai, i2p_hi, -fhi) + ai * i2p_lo;
    f32x2 wp[3];
#pragma unroll
    for (int i = 0; i < 3; ++i) {
      const float ps = i == 0 ? 1.f : (i == 1 ? 8.f : 128.f);
      const float mag = __builtin_amdgcn_exp2f(a2 * ps);
      const float rev = __builtin_fmaf(flo, ps, __builtin_amdgcn_fractf(fhi * ps));
      wp[i] = f32x2{mag * __builtin_amdgcn_cosf(rev), mag * __builtin_amdgcn_sinf(rev)};
    }
    const float cr = bf2f(pCr), cim = bf2f(pCi);
    int ce = 14 - __builtin_amdgcn_frexp_expf(wave_max(fmaxf(fmaxf(fabsf(cr), fabsf(cim)), fmaxf(fabsf(bf2f(pCr2)), fabsf(bf2f(pCi2))))));
    ce = ce > 100 ? 100 : (ce < -100 ? -100 : ce);
    LAS char* tb = lds + L_TB + w * TB_WAVE + lane * 4;
    auto put2 = [&](int ra, f32x2 va, int rb2, f32x2 vb) {
      *(LAS u32*)(tb + ra * TB_ROW) = __builtin_bit_cast(u32, __builtin_convertvector(va, f16x2));
      *(LAS u32*)(tb + rb2 * TB_ROW) = __builtin_bit_cast(u32, __builtin_convertvector(vb, f16x2));
    };
    {
      const f32x2 w8c = {wp[1].x, -wp[1].y}, w16c = cmul(w8c, w8c);
      f32x2 y0 = {ldexpf(cr, ce), -ldexpf(cim, ce)}, y1 = cmul(y0, w8c);
#pragma unroll
      for (int i = 0; i < 8; ++i) {
        put2(41 + 2 * i, y0, 42 + 2 * i, y1);
        if (i < 7) { y0 = cmul(y0, w16c); y1 = cmul(y1, w16c); }
      }
    }
    FENCE();
#pragma unroll
    for (int i = 0; i < 2; ++i) {
      const bool last = wtau[i] == 10;
      *(LAS u32x4*)wi1[i] = u32x4{wg[i].x, wu[i].x, wg[i].y, wu[i].y};
      *(LAS u32x4*)(wi1[i] + 16) = u32x4{wg[i].z, wu[i].z, last ? 0x00003F80u : wg[i].w, last ? 0u : wu[i].w};
      *(LAS u32x4*)(wi2 + i * 256) = dv[i];
    }
    vm_wait<0>();
    SYNC_LDS();
    flag_st<E_P0>(c, c.sflag);
    STAMP(1);
    {
      LAS char* ri1 = lds + L_IMG1 + 3 * w * 1056 + lane * 8;
      LAS char* ri2 = lds + L_IMG2 + 2 * w * 1056 + lane * 8;
      const u32 sgu = OFF_WGU + ((cbR * 11 + 3 * w) * 16 + sR) * 1024, sgu2 = w == 3 ? 0x7f000000u : sgu + 32768;
      const u32 swd = b < 176 ? OFF_WD + ((dq * 8 + 2 * w) * KS2 + s2) * 1024 : 0x7f000000u;
      u32x4 o[5];
#pragma unroll
      for (int i = 0; i < 3; ++i) o[i] = tr_frag(ri1 + i * 1056);
#pragma unroll
      for (int i = 0; i < 2; ++i) o[3 + i] = tr_frag(ri2 + i * 1056);
      PIN();
      st16(c.rs, lane * 16, sgu, o[0]);
      STAMP(2);
      f32x2 e[8];
      e[1] = wp[0]; e[2] = cmul(e[1], e[1]); e[3] = cmul(e[2], e[1]); e[4] = cmul(e[2], e[2]);
      e[5] = cmul(e[4], e[1]); e[6] = cmul(e[4], e[2]); e[7] = cmul(e[4], e[3]);
      const f32x2 w256 = cmul(wp[2], wp[2]);
      f32x2 ta = {16384.f, 0.f}, tc = wp[2] * 16384.f;
      st16(c.rs, lane * 16, sgu + 16384, o[1]);
      put2(0, ta, 8, tc);
#pragma unroll
      for (int i = 1; i < 8; ++i) { put2(i, e[i] * 16384.f, 8 + i, cmul(e[i], tc)); if (i == 4) st16(c.rs, lane * 16, sgu2, o[2]); }
      st16(c.rs, lane * 16, swd, o[3]);
      ta = w256 * 16384.f; tc = cmul(tc, w256);
      put2(16, ta, 20, tc);
#pragma unroll
      for (int i = 1; i < 4; ++i) put2(16 + i, cmul(ta, e[2 * i]), 20 + i, cmul(tc, e[2 * i]));
      st16(c.rs, lane * 16, swd + KS2 * 1024, o[4]);
#pragma unroll
      for (int i = 0; i < 2; ++i) {
        ta = cmul(ta, w256); tc = cmul(tc, w256);
        put2(24 + 4 * i, ta, 26 + 4 * i, tc);
        put2(25 + 4 * i, cmul(ta, e[4]), 27 + 4 * i, cmul(tc, e[4]));
      }
#pragma unroll
      for (int i = 0; i < 4; ++i) {
        ta = cmul(ta, w256); tc = cmul(tc, w256);
        put2(32 + 2 * i, ta, 33 + 2 * i, tc);
      }
      ta = cmul(ta, w256);
      put2(40, ta, 40, ta);
    }
    peek<false, E_P0>(c, c.speek);
    SYNC_LDS();
    LAS char* ta0 = lds + L_KT + kch * KT_CH + selu((u64)(long long)-khalf, 128 * (q >> 1) + 4 * (q & 1) + 8 * l15, 512 + 64 * q + l15) * 4;
    LAS char* ta1 = lds + L_KT + kch * KT_CH + (q < 2 ? 256 + 64 * q + 4 * l15 : 384 + 64 * (q - 2) + 2 * l15) * 4;
    LAS char* ta1b = ta1 + (q < 2 ? 2 : 32) * 4;
    {
      LAS char* tr = lds + L_TB + 2 * kch * TB_WAVE + l15 * TB_ROW + q * 16;
      LAS char* tr0 = tr + khalf * 32 * TB_ROW;
      f32x4 kk[2];
      sfor<2>([&](auto H) {
        u32x4 fa[4][2], fb[4];
#pragma unroll
        for (int G = 0; G < 4; ++G) {
          fb[G] = lds16(tr + H * TB_WAVE + 41 * TB_ROW + G * 64);
          fa[G][0] = lds16(tr0 + H * TB_WAVE + G * 64);
          fa[G][1] = lds16(tr + H * TB_WAVE + 16 * TB_ROW + G * 64);
        }
        sfor<8>([&](auto N) { mm_f16<(H == 0 && N < 2)>(kk[N & 1], fa[N >> 1][N & 1], fb[N >> 1]); });
      });
      const float ks = ldexpf(2.f * dt, -14 - ce);
      asm volatile("s_nop 7" : "+v"(kk[0]), "+v"(kk[1]));
      kk[0] *= ks; kk[1] *= ks;
      STAMP(40);
      if (khalf) {
#pragma unroll
        for (int j = 0; j < 4; ++j) *(LAS float*)(ta0 + j * 64) = kk[0][j];
      } else {
        *(LAS f32x4*)ta0 = kk[0];
        *(LAS f32x2*)ta1 = kk[1].xy; *(LAS f32x2*)ta1b = kk[1].zw;
      }
    }
    conv_setup(c, 0, cf);
    poll_edge<false, 0, E_P0>(c, c.speek);
    vm_wait<0>();
    SYNC_LDS();
    STAMP(41);
    conv_load<0>(c, 0, inA); conv_load<0>(c, 1, inB);
    flag_st<E_W>(c, c.sflag);
    PIN();
    {
      f32x2 V[11];
#pragma unroll
      for (int i = 0; i < 11; ++i) V[i] = f32x2{*(LAS float*)ci[i], *(LAS float*)(ci[i] + KT_CH)};
      PIN();
#pragma unroll
      for (int ch = 0; ch < 2; ++ch)
#pragma unroll
        for (int s = 0; s < 4; ++s) {
          const u32x4 o = {pk_bf16(V[3 - s][ch], V[4 - s][ch]), pk_bf16(V[5 - s][ch], V[6 - s][ch]), pk_bf16(V[7 - s][ch], V[8 - s][ch]), pk_bf16(V[9 - s][ch], V[10 - s][ch])};
          *(LAS u32x4*)(lds + L_KC + ch * KC_CH + s * KC_COPY + tid * 16) = o;
        }
    }
  }
  STAMP(3);

  G1 g;
  conv_phase<0>(c, cf, g, dsk, inA, inB);

#pragma unroll 1
  for (int layer = 0; layer < 2; ++layer) {
    refresh(c, w0);
    STAMP(10 + layer * 20);
    g.layer = layer;
    asm volatile("" : "+s"(g.lanes), "+s"(g.xa), "+s"(g.res), "+s"(g.sst), "+s"(g.sst2), "+s"(g.hsrc), "+s"(g.hsrc5));
    asm volatile("" : "+s"(g.so), "+s"(g.soh), "+s"(g.eoff), "+s"(g.fA), "+s"(g.fG), "+s"(g.pA), "+s"(g.pG));
    g.ring[0] = c.lds + L_RING + c.lane * 8;
    g.ring[1] = g.ring[0] + 65536;
    g.v16 = c.lane * 16;
    g.vst = c.q * (KS2 * 1024) + c.l15 * 16;
    g.pk = c.lds + L_ST + c.l15 * 16;
    g.pws = c.lds + L_WS + c.w * 192 + c.q * 16;
    g.rd[0] = c.lds + L_RING + c.w * 4096 + c.lane * 16;
    g.rd[1] = g.rd[0] + 65536;
    g.rd[2] = c.lds + L_RING + (c.w ^ 1) * 4096 + c.lane * 16;
    g.rd[3] = g.rd[2] + 65536;
    g.prs = c.lds + L_RES + (2 * (c.w & 1) + (c.w >> 1)) * 512 + (c.w & 1) * 2048 + c.lane * 8;
    g.prs1 = c.lds + L_RES + (2 * (c.w & 1) + (c.w >> 1)) * 512 + ((c.w & 1) ^ 1) * 2048 + c.lane * 8;
    g.exw = c.lds + L_RING + c.w * 2048 + c.lane * 16;
    g.exr = c.lds + L_RING + (c.w ^ 2) * 2048 + c.lane * 16;
    g.sts = c.lds + L_ST + 1024 + c.lane * 16;
    g.stw = c.lds + L_ST + 1024 + (c.lane ^ ((c.w & 1) * 32)) * 16 + c.w * 4;
    g.vo = selu((u64)((long long)g.lanes >> 1), (((c.q ^ c.w) & 1) * 16 + c.l15) * 1024 + (c.q >> 1) * 16, (2 * c.q + (c.lane & 1)) * 8192 + ((c.lane ^ (2 * c.w)) & 2) * 32 + (c.l15 >> 2) * 16);
    g.va = (c.l15 & 3) * 4096 + (c.l15 >> 2) * 16 + (c.q >> 1) * 64 + (c.q & 1) * 8;
    asm volatile("" : "+v"(g.ring[0]), "+v"(g.ring[1]), "+v"(g.vst), "+v"(g.pk), "+v"(g.v16), "+v"(g.pws), "+v"(g.rd[0]), "+v"(g.rd[1]), "+v"(g.rd[2]), "+v"(g.rd[3]));
    asm volatile("" : "+v"(g.prs), "+v"(g.prs1), "+v"(g.exw), "+v"(g.exr), "+v"(g.sts), "+v"(g.stw), "+v"(g.vo), "+v"(g.va));
    u32x4 Bc[4];
    f32x4 acc[3][4];
    if (layer == 0) vm_wait<19>(); else vm_wait<5>();
    BARRIER(); FENCE();
#pragma unroll
    for (int ct = 0; ct < 4; ++ct) Bc[ct] = tr_frag(g.ring[0] + ct * 4096);
    STAMP(11 + layer * 20);
    g1_main<0>(c, g, acc, Bc);
    if (layer == 0) wsum_part<8, 16>(g.pws, [](auto) {});
    g1_epilogue<0>(c, g, acc, [&]() {
      vm_wait<11>();
      BARRIER(); FENCE();
#pragma unroll
      for (int ct = 0; ct < 4; ++ct) Bc[ct] = tr_frag(g.ring[1] + ct * 4096);
    });
    STAMP(13 + layer * 20);
    g1_main<1>(c, g, acc, Bc);
    f32x4 acc2[4];
    u32x4 Bd[8];
    g1_epilogue<1>(c, g, acc, [&]() {
      vm_wait<6>();
      BARRIER(); FENCE();
      g2_first(g, Bd);
      g2_res<0>(g, acc2);
    });
    STAMP(15 + layer * 20);
    ConvIn nA, nB;
    ConvF c2;
    g2_unit<0>(c, g, p, layer, acc2, Bd);
    vm_wait<13>();
    {
      u32 mx = 0;
#pragma unroll
      for (int ks = 0; ks < 4; ++ks) Bd[2 * ks] = lds16(g2_frag<0>(g, 0, ks));
      g2_res<1>(g, acc2);
#pragma unroll
      for (int ks = 0; ks < 4; ++ks) mx = umax3(umax3(mx, Bd[2 * ks].x, Bd[2 * ks].y), Bd[2 * ks].z, Bd[2 * ks].w);
      g2_verify<0>(c, g, Bd, mx);
      BARRIER(); FENCE();
#pragma unroll
      for (int ks = 0; ks < 4; ++ks) Bd[2 * ks + 1] = lds16(g2_frag<0>(g, 1, ks));
    }
    g2_unit<1>(c, g, p, layer, acc2, Bd);
    if (layer == 0) {
      refresh(c, w0);
      if (CK_D == 0) conv_load<1>(c, 0, nA);
      conv_setup(c, 1, c2);
      conv_pad(c);
      ffn_next(g);
      if (CK_D == 1) { poll_edge<false, 2, E_DOWN>(c, c.speek); BARRIER(); FENCE(); conv_load<1>(c, 0, nA); }
      conv_phase<1>(c, c2, g, dsk, nA, nB);
    }
  }
  if (blockIdx.x == 0 && threadIdx.x == 0) __hip_atomic_store((GAS u32*)(c.ws + OFF_EPOCH), c.epoch, RLX_AGENT);
  asm volatile(".Lmega_tail:");
}

}

extern "C" void kernel_launch(void* const* d_in, const int* in_sizes, int n_in, void* d_out, int out_size, void* d_ws,
                              size_t ws_size, hipStream_t stream) {
  Params p;
  p.x0 = (const u16*)d_in[0];
  p.logA = (const u16*)d_in[1];
  p.Aim = (const u16*)d_in[2];
  p.Cre = (const u16*)d_in[3];
  p.Cim = (const u16*)d_in[4];
  p.logdt = (const u16*)d_in[5];
  p.Dskip = (const u16*)d_in[6];
  p.wg = (const u16*)d_in[8];
  p.wu = (const u16*)d_in[9];
  p.wd = (const u16*)d_in[10];
  p.out = (u16*)d_out;
  p.ws = (char*)d_ws;
  mega_kernel<<<NBLK, 256, 0, stream>>>(p);
}
```

```cpp
#define SP_H 40
#include <hip/hip_runtime.h>
#include <hip/hip_bf16.h>
#include <utility>

typedef unsigned int u32;
typedef unsigned short u16;
typedef unsigned long long u64;
typedef u32 u32x4 __attribute__((ext_vector_type(4)));
typedef u32 u32x2 __attribute__((ext_vector_type(2)));
typedef float f32x4 __attribute__((ext_vector_type(4)));
typedef float f32x2 __attribute__((ext_vector_type(2)));
typedef short s16x4 __attribute__((ext_vector_type(4)));
typedef __bf16 bf16x8 __attribute__((ext_vector_type(8)));
typedef __bf16 bf16x2 __attribute__((ext_vector_type(2)));
typedef _Float16 f16x8 __attribute__((ext_vector_type(8)));
typedef _Float16 f16x2 __attribute__((ext_vector_type(2)));
#define LAS __attribute__((address_space(3)))
#define GAS __attribute__((address_space(1)))
#define RLX_AGENT __ATOMIC_RELAXED, __HIP_MEMORY_SCOPE_AGENT
#define DEV __device__ __forceinline__

#define CODE_PREFETCH 1
#define MAP_ROWS 0
#define H_REFETCH 0
#define PK_B -10
#define PK_A 4
#define FL_G 2
#define PK_G 84
#define CK_B 24
#define SP_D 4
#define FL_A 2
#define PK_D 89
#define CK_D 1
#define SP_X 4
#define LD_A 80
#define LD_B 20
#define FL_B 8
#define STAMP_WAVE 0

namespace {

constexpr int SEQ = 2048, DM = 512, NS = 128, DF = 1376;
constexpr int NBLK = 256;
constexpr int KS2 = 44;

constexpr u32 OFF_EPOCH = 0;
constexpr u32 OFF_FLAG = 4096;
constexpr u32 OFF_GFLAG = OFF_FLAG + 8 * 8192;
constexpr u32 OFF_ST1 = OFF_GFLAG + 8192;
constexpr u32 OFF_ST3 = OFF_ST1 + 16384;
constexpr u32 OFF_WGU = OFF_ST3 + 32768;
constexpr u32 OFF_WD = OFF_WGU + 16 * 11 * 16 * 1024;
constexpr u32 OFF_XC = OFF_WD + 32 * KS2 * 1024;
constexpr u32 OFF_XP = OFF_XC + 2 * 2097152;
constexpr u32 OFF_H = OFF_XP + 2 * 2097152;
constexpr u32 H_BYTES = 128 * KS2 * 1024;
constexpr u32 OFF_DBG = OFF_H + 2 * H_BYTES;
constexpr u32 WS_BYTES = OFF_DBG + 256 * 64 * 16;
constexpr int E_P0 = 0, E_W = 7, E_W2 = 1, E_CONV = 1  , E_DOWN = 3  ;

constexpr int KC_COPY = 4160, KC_CH = 4 * KC_COPY;
constexpr int L_KC = 0;
constexpr int L_RING = 2 * KC_CH;
constexpr int L_RES = L_RING + 7 * 16384;
constexpr int L_ST = L_RES + 8192;
constexpr int L_WS = L_ST + 2048;
constexpr int L_CA = L_WS + 768;
constexpr int LDS_BYTES = 163840;
constexpr int L_U = L_RING + 5 * 16384;
constexpr int U_CH = 4608;
constexpr int L_P = L_RING + 6 * 16384;
constexpr int TB_ROW = 272, TB_WAVE = 57 * TB_ROW;
constexpr int L_TB = L_RING;
constexpr int L_IMG1 = L_TB + 4 * TB_WAVE;
constexpr int L_KT = L_IMG1 + 11 * 1056 + 16;
constexpr int KT_CH = 772 * 4;
constexpr int L_IMG2 = L_U + 2 * U_CH;
constexpr int L_XT = L_IMG2 + 8 * 1056;
constexpr int L_DUMMY = L_XT + 32 * 272;
static_assert(L_KT + 2 * KT_CH <= L_U && L_DUMMY + 4128 <= L_RES && L_CA + 4096 <= LDS_BYTES, "lds");

struct Params {
  const u16 *x0, *logA, *Aim, *Cre, *Cim, *logdt, *Dskip, *wg, *wu, *wd;
  u16* out;
  char* ws;
};

DEV float bf_lo(u32 v) { return __builtin_bit_cast(float, v << 16); }
DEV float bf_hi(u32 v) { return __builtin_bit_cast(float, v & 0xffff0000u); }
DEV float bf2f(u16 v) { return __builtin_bit_cast(float, (u32)v << 16); }
DEV u32 pk_bf16(float lo, float hi) {
  f32x2 f = {lo, hi};
  return __builtin_bit_cast(u32, __builtin_convertvector(f, bf16x2));
}
DEV float rnd_bf16(float v) { return bf_lo(pk_bf16(v, 0.f)); }
DEV u32x2 tr_read(LAS char* p) {
  return __builtin_bit_cast(u32x2, __builtin_amdgcn_ds_read_tr16_b64_v4i16((LAS s16x4*)p));
}
DEV u32x4 tr_frag(LAS char* p) {
  u32x2 a = tr_read(p), b = tr_read(p + 512);
  u32x4 r = {a.x, a.y, b.x, b.y};
  return r;
}
DEV u32x4 lds16(LAS char* p) { return *(LAS u32x4*)p; }
template <int CTRL> DEV float dpp_f(float v) {
  return __builtin_bit_cast(float, __builtin_amdgcn_mov_dpp(__builtin_bit_cast(int, v), CTRL, 0xf, 0xf, true));
}
template <int CTRL> DEV u32 dpp_u(u32 v) { return (u32)__builtin_amdgcn_mov_dpp((int)v, CTRL, 0xf, 0xf, true); }
DEV void swap16(u32& a, u32& b) { auto r = __builtin_amdgcn_permlane16_swap(a, b, false, false); a = r[0]; b = r[1]; }
DEV void swap32(u32& a, u32& b) { auto r = __builtin_amdgcn_permlane32_swap(a, b, false, false); a = r[0]; b = r[1]; }
DEV void swap16f(float& a, float& b) { u32 x = __builtin_bit_cast(u32, a), y = __builtin_bit_cast(u32, b); swap16(x, y); a = __builtin_bit_cast(float, x); b = __builtin_bit_cast(float, y); }
DEV void swap32f(float& a, float& b) { u32 x = __builtin_bit_cast(u32, a), y = __builtin_bit_cast(u32, b); swap32(x, y); a = __builtin_bit_cast(float, x); b = __builtin_bit_cast(float, y); }
DEV float wave_sum(float v) {
  v += dpp_f<0xB1>(v);
  v += dpp_f<0x4E>(v);
  v += dpp_f<0x141>(v);
  v += dpp_f<0x140>(v);
  float a = v, b = v;
  swap16f(a, b); v = a + b;
  a = v; b = v;
  swap32f(a, b); v = a + b;
  return v;
}
DEV float wave_max(float v) {
  v = fmaxf(v, dpp_f<0xB1>(v)); v = fmaxf(v, dpp_f<0x4E>(v)); v = fmaxf(v, dpp_f<0x141>(v)); v = fmaxf(v, dpp_f<0x140>(v));
  float a = v, b = v;
  swap16f(a, b); v = fmaxf(a, b);
  a = v; b = v;
  swap32f(a, b); v = fmaxf(a, b);
  return v;
}
DEV float sel(u64 m, float a, float b) { float r; asm("v_cndmask_b32_e64 %0, %1, %2, %3" : "=v"(r) : "v"(a), "v"(b), "s"(m)); return r; }
DEV u32 selu(u64 m, u32 a, u32 b) { u32 r; asm("v_cndmask_b32_e64 %0, %1, %2, %3" : "=v"(r) : "v"(a), "v"(b), "s"(m)); return r; }
constexpr u64 M_B0 = 0xAAAAAAAAAAAAAAAAull, M_B1 = 0xCCCCCCCCCCCCCCCCull, M_B2 = 0xF0F0F0F0F0F0F0F0ull, M_B3 = 0xFF00FF00FF00FF00ull;
DEV float dot2(u32 a, u32 b, float acc) {
  return __builtin_amdgcn_fdot2_f32_bf16(__builtin_bit_cast(bf16x2, a), __builtin_bit_cast(bf16x2, b), acc, false);
}
DEV f32x2 cmul(f32x2 a, f32x2 b) {
  const f32x2 t = a.yy * f32x2{-b.y, b.x};
  return __builtin_elementwise_fma(a.xx, b, t);
}
#define ACLOB "a0","a1","a2","a3","a4","a5","a6","a7","a8","a9","a10","a11","a12","a13","a14","a15","a16","a17","a18","a19","a20","a21","a22","a23","a24","a25","a26","a27","a28","a29","a30","a31","a32","a33","a34","a35","a36","a37","a38","a39","a40","a41","a42","a43","a44","a45","a46","a47","a48","a49","a50","a51","a52","a53","a54","a55","a56","a57","a58","a59","a60","a61","a62","a63","a64","a65","a66","a67","a68","a69","a70","a71","a72","a73","a74","a75","a76","a77","a78","a79","a80","a81","a82","a83","a84","a85","a86","a87","a88","a89","a90","a91","a92","a93","a94","a95","a96","a97","a98","a99","a100","a101","a102","a103","a104","a105","a106","a107","a108","a109","a110","a111","a112","a113","a114","a115","a116","a117","a118","a119","a120","a121","a122","a123","a124","a125","a126","a127","a128","a129","a130","a131","a132","a133","a134","a135","a136","a137","a138","a139","a140","a141","a142","a143","a144","a145","a146","a147","a148","a149","a150","a151","a152","a153","a154","a155","a156","a157","a158","a159","a160","a161","a162","a163","a164","a165","a166","a167","a168","a169","a170","a171","a172","a173","a174","a175","a176","a177","a178","a179","a180","a181","a182","a183","a184","a185","a186","a187","a188","a189","a190","a191","a192","a193","a194","a195","a196","a197","a198","a199","a200","a201","a202","a203","a204","a205","a206","a207","a208","a209","a210","a211","a212","a213","a214","a215","a216","a217","a218","a219","a220","a221","a222","a223","a224","a225","a226","a227","a228","a229","a230","a231","a232","a233","a234","a235","a236","a237","a238","a239","a240","a241","a242","a243","a244","a245","a246","a247","a248","a249","a250","a251","a252","a253","a254","a255"
#define BARRIER() __builtin_amdgcn_s_barrier()
#define FENCE() asm volatile("" ::: "memory")
#define PIN() __builtin_amdgcn_sched_barrier(0)
#define LGKM0() asm volatile("s_waitcnt lgkmcnt(0)" ::: "memory", ACLOB)
#define SYNC_LDS() do { LGKM0(); BARRIER(); FENCE(); } while (0)
template <int N> DEV void vm_wait() { asm volatile("s_waitcnt vmcnt(%0)" ::"i"(N) : "memory", ACLOB); }

typedef __amdgpu_buffer_rsrc_t rsrc_t;
DEV rsrc_t mk_rsrc(const void* p, u32 bytes) { return __builtin_amdgcn_make_buffer_rsrc((void*)p, 0, bytes, 0x00020000); }
DEV u32x4 ld16(rsrc_t r, u32 voff, u32 soff) { return __builtin_amdgcn_raw_buffer_load_b128(r, voff, soff, 16); }
DEV u32x4 ld16p(rsrc_t r, u32 voff, u32 soff) { return __builtin_amdgcn_raw_buffer_load_b128(r, voff, soff, 0); }
DEV u32x4 all_ones() { u32x4 v = {~0u, ~0u, ~0u, ~0u}; asm volatile("" : "+v"(v)); return v; }
template <int AUX = 16> DEV void st16(rsrc_t r, u32 voff, u32 soff, u32x4 v) {
  __builtin_amdgcn_raw_buffer_store_b128(v, r, voff, soff, AUX);
  asm volatile("s_nop 1" ::"v"(v) : "memory");
}

template <int... Is, class F> DEV void sfor_impl(std::integer_sequence<int, Is...>, F&& f) { (f(std::integral_constant<int, Is>{}), ...); }
template <int N, class F> DEV void sfor(F&& f) { sfor_impl(std::make_integer_sequence<int, N>{}, f); }
template <bool FIRST> DEV void mm_vv(f32x4& acc, const u32x4& a, const u32x4& b) {
  if constexpr (FIRST) asm volatile("v_mfma_f32_16x16x32_bf16 %0, %1, %2, 0" : "=&v"(acc) : "v"(a), "v"(b));
  else asm volatile("v_mfma_f32_16x16x32_bf16 %0, %1, %2, %0" : "+v"(acc) : "v"(a), "v"(b));
}
template <bool FIRST> DEV void mm_f16(f32x4& acc, const u32x4& a, const u32x4& b) {
  if constexpr (FIRST) asm volatile("v_mfma_f32_16x16x32_f16 %0, %1, %2, 0" : "=&v"(acc) : "v"(a), "v"(b));
  else asm volatile("v_mfma_f32_16x16x32_f16 %0, %1, %2, %0" : "+v"(acc) : "v"(a), "v"(b));
}
template <bool FIRST, int A> DEV void mm_av(f32x4& acc, const u32x4& b) {
  if constexpr (FIRST) asm volatile("v_mfma_f32_16x16x32_bf16 %0, a[%c2:%c3], %1, 0" : "=&v"(acc) : "v"(b), "i"(A), "i"(A + 3));
  else asm volatile("v_mfma_f32_16x16x32_bf16 %0, a[%c2:%c3], %1, %0" : "+v"(acc) : "v"(b), "i"(A), "i"(A + 3));
}
template <int A, int OFF> DEV void ld16_a(rsrc_t r, u32 voff, u32 soff) {
  asm volatile("buffer_load_dwordx4 a[%c3:%c4], %0, %1, %2 offen offset:%c5 sc1" ::"v"(voff), "s"(r), "s"(soff), "i"(A), "i"(A + 3), "i"(OFF) : "memory");
}
#define MM_DONE4(a) asm volatile("s_nop 7" : "+v"((a)[0]), "+v"((a)[1]), "+v"((a)[2]), "+v"((a)[3]))

template <int G, int L> DEV void dma1(rsrc_t r, u32 voff, u32 soff, u32 lds) {
  u32 t;
  asm volatile("s_add_u32 m0, %4, %6\n\ts_add_u32 %0, %3, %5\n\tbuffer_load_dwordx4 %1, %2, %0 offen sc1 lds"
               : "=&s"(t) : "v"(voff), "s"(r), "s"(soff), "s"(lds), "i"(G), "i"(L) : "memory", "scc");
}
template <int G, int L, int P> DEV void dmap(rsrc_t r, u32 voff, u32 soff, u32 lds, u32& t) {
  if constexpr (P == 0)
    asm volatile("s_add_u32 m0, %4, %6\n\ts_add_u32 %0, %3, %5\n\tbuffer_load_dwordx4 %1, %2, %0 offen sc1 lds"
                 : "=&s"(t) : "v"(voff), "s"(r), "s"(soff), "s"(lds), "i"(G), "i"(L) : "memory", "scc");
  else
    asm volatile("buffer_load_dwordx4 %0, %1, %2 offen offset:%c3 sc1 lds" ::"v"(voff), "s"(r), "s"(t), "i"(P * 1024) : "memory");
}
template <int G, int L> DEV void dma4(rsrc_t r, u32 voff, u32 soff, u32 lds) {
  u32 t;
  asm volatile("s_add_u32 m0, %4, %6\n\ts_add_u32 %0, %3, %5\n\tbuffer_load_dwordx4 %1, %2, %0 offen sc1 lds\n\t"
               "buffer_load_dwordx4 %1, %2, %0 offen offset:1024 sc1 lds\n\tbuffer_load_dwordx4 %1, %2, %0 offen offset:2048 sc1 lds\n\t"
               "buffer_load_dwordx4 %1, %2, %0 offen offset:3072 sc1 lds"
               : "=&s"(t) : "v"(voff), "s"(r), "s"(soff), "s"(lds), "i"(G), "i"(L) : "memory", "scc");
}

constexpr u32 SPIN_CAP = 1u << 16;
constexpr int SYNC_WAVE = 2;

struct Ctx {
  char* ws;
  rsrc_t rs;
  LAS char* lds;
  u32 lds0, epoch;
  int b, tid, lane, w, q, l15, cp, cb, rb, db, rt;
  mutable u32 junk;
  u32 vt16, sxp, vflag, vpeek, sflag, sflagg, speek, speekg;
};

DEV void flag_setup(Ctx& c) {
  c.vflag = c.tid == 192 ? 0u : 0x7f000000u;
  c.vpeek = c.lane * 64;
  c.vt16 = c.tid * 16;
  c.sxp = OFF_XP + c.cp * 8192;
  c.sflag = OFF_FLAG + (c.b >> 2) * 64 + (c.b & 3) * 4;
  c.sflagg = OFF_FLAG + 8 * 4096 + c.rb * 256 + (c.cb >> 2) * 64 + (c.cb & 3) * 4;
  c.speek = c.w == SYNC_WAVE ? OFF_FLAG : 0x7f000000u;
  c.speekg = c.w == SYNC_WAVE ? OFF_FLAG + 8 * 4096 + (c.rt >> 1) * 256 : 0x7f000000u;
  asm volatile("" : "+v"(c.vflag), "+v"(c.vpeek), "+v"(c.vt16));
  asm volatile("" : "+s"(c.sflag), "+s"(c.sflagg), "+s"(c.speek), "+s"(c.speekg), "+s"(c.sxp));
}
template <int E> DEV void flag_st(const Ctx& c, u32 base) {
  u32 t;
  asm volatile("s_add_u32 %0, %4, %5\n\tbuffer_store_dword %1, %2, %3, %0 offen sc1" : "=&s"(t) : "v"(c.epoch), "v"(c.vflag), "s"(c.rs), "s"(base), "i"(E * 4096) : "memory", "scc");
}
template <bool GRP, int E> DEV void peek(const Ctx& c, u32 base) {
  u32 t;
  if constexpr (GRP)
    asm volatile("s_mov_b64 exec, 15\n\ts_add_u32 %0, %3, %4\n\tbuffer_load_dwordx4 a[252:255], %1, %2, %0 offen sc1\n\ts_mov_b64 exec, -1"
                 : "=&s"(t) : "v"(c.vpeek), "s"(c.rs), "s"(base), "i"(E * 4096) : "memory", "scc");
  else
    asm volatile("s_add_u32 %0, %3, %4\n\tbuffer_load_dwordx4 a[252:255], %1, %2, %0 offen sc1"
                 : "=&s"(t) : "v"(c.vpeek), "s"(c.rs), "s"(base), "i"(E * 4096) : "memory", "scc");
}
template <bool GRP, int N> DEV u64 flags_miss(const Ctx& c) {
  u32 f0, f1, f2, f3;
  asm volatile("s_waitcnt vmcnt(%4)\n\tv_accvgpr_read_b32 %0, a252\n\tv_accvgpr_read_b32 %1, a253\n\tv_accvgpr_read_b32 %2, a254\n\tv_accvgpr_read_b32 %3, a255"
               : "=v"(f0), "=v"(f1), "=v"(f2), "=v"(f3) : "i"(N) : "memory");
  const u32 d = ((f0 ^ c.epoch) | (f1 ^ c.epoch)) | ((f2 ^ c.epoch) | (f3 ^ c.epoch));
  return __builtin_amdgcn_ballot_w64(d != 0u) & (GRP ? 15ull : ~0ull);
}
template <bool GRP, int N, int E, int WV = SYNC_WAVE> DEV void poll_edge(const Ctx& c, u32 base) {
  if (c.w == WV) {
    if (__builtin_expect(flags_miss<GRP, N>(c) != 0ull, 0)) {
#pragma unroll 1
      for (u32 sp = 0; sp < SPIN_CAP; ++sp) {
        peek<GRP, E>(c, base);
        if (flags_miss<GRP, 0>(c) == 0ull) break;
        __builtin_amdgcn_s_sleep(4);
      }
    }
  }
  FENCE();
}

#define STAMP(i) do { } while (0)

DEV void refresh(Ctx& c, int w0) {
  int tid = threadIdx.x, b = blockIdx.x, w = w0;
  asm volatile("" : "+v"(tid)); asm volatile("" : "+s"(b)); asm volatile("" : "+s"(w));
  c.tid = tid; c.b = b; c.w = w; c.lane = tid & 63; c.q = c.lane >> 4; c.l15 = tid & 15;
  const int xcd = b & 7, rr = b >> 3;
  c.cp = xcd * 32 + rr;
  c.cb = (xcd & 3) * 4 + (rr & 3); c.rb = (xcd >> 2) * 8 + (rr >> 2);
  c.db = (xcd & 1) * 4 + (rr & 3); c.rt = (xcd >> 1) * 8 + (rr >> 2);
}

constexpr int AW(int tile, int s) { return (tile * 16 + s) * 4; }
template <int N> DEV void ld_wgu(const Ctx& c) {
  constexpr int tl = N % 3, s = N / 3;
  const u32 v = c.lane * 16 + ((tl == 2 && c.w == 3) ? 0x40000000u : 0u);
  ld16_a<AW(tl, s), (s & 3) * 1024>(c.rs, v, OFF_WGU + (c.cb * 11 + 3 * c.w + tl) * 16384 + (s >> 2) * 4096);
}
constexpr int HB_OFF = 64 * KS2 * 1024;
constexpr int NWDA = 15, VWD = 140;
DEV u32 wd_ptr(const Ctx& c, int other) { return OFF_WD + (c.db * 4 + 2 * (c.w & 1) + ((c.w >> 1) ^ other)) * (KS2 * 1024) + (c.w >> 1) * 4096; }
template <int N> DEV void ld_wd(const Ctx& c, u32 v16, u32 wd0, u32 wd1, u64 lanes) {
  constexpr int OFF = ((N >> 1) & 3) * 1024;
  if constexpr (N < NWDA)
    asm volatile("s_mov_b64 exec, %3\n\tbuffer_load_dwordx4 a[%c5:%c6], %0, %1, %2 offen offset:%c4 sc1\n\ts_mov_b64 exec, -1"
                 :: "v"(v16), "s"(c.rs), "s"((N & 1) ? wd1 : wd0), "s"(lanes), "i"(OFF), "i"(192 + 4 * N), "i"(195 + 4 * N) : "memory");
  else
    asm volatile("s_mov_b64 exec, %3\n\tbuffer_load_dwordx4 v[%c5:%c6], %0, %1, %2 offen offset:%c4 sc1\n\ts_mov_b64 exec, -1"
                 :: "v"(v16), "s"(c.rs), "s"((N & 1) ? wd1 : wd0), "s"(lanes), "i"(OFF), "i"(VWD + 4 * (N - NWDA)), "i"(VWD + 3 + 4 * (N - NWDA)) : "memory");
}
constexpr int WS0 = 9;
template <int V> DEV void mm_wv(f32x4& acc, const u32x4& b) {
  asm volatile("v_mfma_f32_16x16x32_bf16 %0, v[%c2:%c3], %1, %0" : "+v"(acc) : "v"(b), "i"(V), "i"(V + 3));
}

DEV u32 umax3(u32 a, u32 b, u32 d) { return max(max(a, b), d); }
DEV u32 umax5(u32 m, const u32x4& v) { return umax3(umax3(m, v.x, v.y), v.z, v.w); }
struct ConvIn { u32x4 x; u32x4 st[4]; };
template <int LAYER, int HALF> DEV void conv_load(const Ctx& c, ConvIn& in, u32 skip = 0) {
  u32 t;
  if constexpr (LAYER == 0) {
    asm volatile("s_add_u32 %3, %6, %7\n\tbuffer_load_dwordx4 %0, %4, %5, %3 offen sc1\n\t"
                 "s_mov_b32 %3, %8\n\tbuffer_load_dwordx4 %1, %4, %5, %3 offen sc1\n\ts_mov_b32 %3, %9\n\tbuffer_load_dwordx4 %2, %4, %5, %3 offen sc1"
                 : "=&v"(in.x), "=&v"(in.st[0]), "=&v"(in.st[1]), "=&s"(t)
                 : "v"(c.vt16), "s"(c.rs), "s"(c.sxp), "i"(HALF * 4096), "i"(OFF_ST1 + HALF * 8192), "i"(OFF_ST1 + HALF * 8192 + 4096)
                 : "memory", "scc");
  } else {
    constexpr u32 so = OFF_ST3 + HALF * 16384;
    asm volatile("s_cmp_lg_u32 %9, 0\n\ts_cbranch_scc1 1f\n\t"
                 "s_mov_b32 %5, %11\n\tbuffer_load_dwordx4 %1, %6, %7, %5 offen sc1\n\ts_mov_b32 %5, %12\n\tbuffer_load_dwordx4 %2, %6, %7, %5 offen sc1\n\t"
                 "s_mov_b32 %5, %13\n\tbuffer_load_dwordx4 %3, %6, %7, %5 offen sc1\n\ts_mov_b32 %5, %14\n\tbuffer_load_dwordx4 %4, %6, %7, %5 offen sc1\n\t"
                 "s_add_u32 %5, %8, %10\n\tbuffer_load_dwordx4 %0, %6, %7, %5 offen sc1\n1:"
                 : "=&v"(in.x), "=&v"(in.st[0]), "=&v"(in.st[1]), "=&v"(in.st[2]), "=&v"(in.st[3]), "=&s"(t)
                 : "v"(c.vt16), "s"(c.rs), "s"(c.sxp), "s"(skip), "i"(2097152 + HALF * 4096), "i"(so), "i"(so + 4096), "i"(so + 8192), "i"(so + 12288)
                 : "memory", "scc");
  }
}
template <int LAYER, int N> DEV void conv_wait(ConvIn& in) {
  if constexpr (LAYER == 0) asm volatile("s_waitcnt vmcnt(%3)" : "+v"(in.x), "+v"(in.st[0]), "+v"(in.st[1]) : "i"(N) : "memory");
  else asm volatile("s_waitcnt vmcnt(%5)" : "+v"(in.x), "+v"(in.st[0]), "+v"(in.st[1]), "+v"(in.st[2]), "+v"(in.st[3]) : "i"(N) : "memory");
}
DEV u32 conv_pending(const ConvIn& in) {
  u32 t = 0;
#pragma unroll
  for (int i = 0; i < 4; ++i) t |= (in.st[i].x ^ 8u) | (in.st[i].z ^ 8u);
  return (t & 0xffu) | (umax5(0u, in.x) == 0xffffffffu ? 1u : 0u);
}
template <int HALF> DEV void conv_accept(const Ctx& c, ConvIn& in, f32x2 (&st)[4]) {
  u32 pend = conv_pending(in);
#pragma unroll
  for (int i = 0; i < 4; ++i)
    st[i] = __builtin_elementwise_fma(f32x2{(float)in.st[i].x, (float)in.st[i].z}, f32x2{0x1p-28f, 0x1p-28f}, f32x2{(float)(int)in.st[i].y, (float)(int)in.st[i].w} * 16.f);
  if (__builtin_expect(__builtin_amdgcn_ballot_w64(pend != 0u) != 0ull, 0)) {
#pragma unroll 1
    for (u32 sp = 0; sp < SPIN_CAP && __builtin_amdgcn_ballot_w64((pend & 15u) != 0u); ++sp) {
      __builtin_amdgcn_s_sleep(2);
      conv_load<1, HALF>(c, in);
      conv_wait<1, 0>(in);
      pend = conv_pending(in);
    }
#pragma unroll
    for (int i = 0; i < 4; ++i)
      st[i] = __builtin_elementwise_fma(f32x2{(float)in.st[i].x, (float)in.st[i].z}, f32x2{0x1p-28f, 0x1p-28f}, f32x2{(float)(int)in.st[i].y, (float)(int)in.st[i].w} * 16.f);
    if (__builtin_amdgcn_ballot_w64((pend & 0xf0u) != 0u)) {
#pragma unroll
      for (int i = 0; i < 4; ++i) st[i] = f32x2{0.f, 0.f};
      u32 budget = SPIN_CAP;
#pragma unroll 1
      for (int pr = 0; pr < 256;) {
        FENCE();
        const u32x4 v = ld16(c.rs, c.vt16, OFF_XP + 2097152 + pr * 8192 + HALF * 4096);
        if (__builtin_amdgcn_ballot_w64(umax5(0u, v) == 0xffffffffu) && budget) { --budget; continue; }
#pragma unroll
        for (int i = 0; i < 4; ++i) { const float a = bf_lo(v[i]), e = bf_hi(v[i]); st[i] += f32x2{a + e, a * a + e * e}; }
        ++pr;
      }
    }
  }
}
template <int LAYER, int HALF> DEV void conv_norm(const Ctx& c, LAS char* pu, ConvIn& in, u32 dsk, f32x4 (&base)[2]) {
  f32x2 st[4];
  if (LAYER == 0) {
#pragma unroll
    for (int i = 0; i < 2; ++i) { const f32x4 v = __builtin_bit_cast(f32x4, in.st[i]); st[2 * i] = v.xy; st[2 * i + 1] = v.zw; }
  } else {
    conv_accept<HALF>(c, in, st);
  }
  const f32x2 dd = {bf_lo(dsk), bf_hi(dsk)};
  f32x2 u[4], x[4];
#pragma unroll
  for (int i = 0; i < 4; ++i) {
    const f32x2 me = __builtin_elementwise_fma(st[i], f32x2{-1.f / DM, 1.f / DM}, f32x2{0.f, 1e-5f});
    const float r = __builtin_amdgcn_rsqf(fmaxf(__builtin_fmaf(-me.x, me.x, me.y), 1e-5f));
    x[i] = f32x2{bf_lo(in.x[i]), bf_hi(in.x[i])};
    u[i] = (x[i] + me.x) * r;
  }
  *(LAS u32x2*)pu = u32x2{pk_bf16(u[0].x, u[1].x), pk_bf16(u[2].x, u[3].x)};
  *(LAS u32x2*)(pu + U_CH) = u32x2{pk_bf16(u[0].y, u[1].y), pk_bf16(u[2].y, u[3].y)};
#pragma unroll
  for (int i = 0; i < 4; ++i) { const f32x2 bs = __builtin_elementwise_fma(u[i], dd, x[i]); base[0][i] = bs.x; base[1][i] = bs.y; }
  asm volatile("" : "+v"(base[0]), "+v"(base[1]));
}
template <int H> DEV void lds8h(u32x4& d, LAS char* p) {
  const u32x2 r = *(LAS u32x2*)(p + 8 * H);
  if constexpr (H == 0) { d.x = r.x; d.y = r.y; } else { d.z = r.x; d.w = r.y; }
}
DEV void ldsA(u32x4& d, LAS char* p) { lds8h<0>(d, p); PIN(); lds8h<1>(d, p); PIN(); }
constexpr int tri_j(int m) { return m < 4 ? 0 : (m < 7 ? 1 : (m < 9 ? 2 : 3)); }
constexpr int tri_i(int m) { return m < 4 ? m : (m < 7 ? m - 3 : (m < 9 ? m - 5 : 3)); }
template <int B0, bool ZERO, class F>
DEV void conv_tri(LAS char* pa, LAS char* pb, u32x4 (&Ac)[4], f32x4 (&acc)[4], F&& side) {
  u32x4 Bc[4];
#pragma unroll
  for (int j = 0; j < 4; ++j) Bc[j] = lds16(pb + (B0 + j) * 512);
  PIN();
  sfor<4>([&](auto IT) {
    constexpr int it = IT;
    u32x4 An[4], Bn[4];
    sfor<10>([&](auto M) {
      constexpr int m = M, i = tri_i(m), j = tri_j(m);
      if constexpr (it < 3) {
        if constexpr (m < 8) lds8h<m & 1>(An[m >> 1], pa - 64 * (it + 1) + (7 - (m >> 1)) * 512);
        if constexpr (m == 0 || m == 5) { PIN(); Bn[m / 5] = lds16(pb - 64 * (it + 1) + (B0 + m / 5) * 512); }
        if constexpr (m >= 8) Bn[m - 6] = lds16(pb - 64 * (it + 1) + (B0 + m - 6) * 512);
      }
      side(std::integral_constant<int, it * 10 + m>{});
      mm_vv<ZERO && it == 0 && j == 0>(acc[i], Ac[i - j], Bc[j]);
      PIN();
    });
    if constexpr (it < 3) {
#pragma unroll
      for (int j = 0; j < 4; ++j) { Ac[j] = An[j]; Bc[j] = Bn[j]; }
    }
  });
}
template <int R> DEV void cross_first(LAS char* pa, LAS char* pb, u32x4 (&Ac)[7], u32x4 (&Bc)[4]) {
  if constexpr (R == 0) Bc[0] = lds16(pb);
  else if constexpr (R < 9) lds8h<(R - 1) & 1>(Ac[3 + (R - 1) / 2], pa + (3 - (R - 1) / 2) * 512);
  else if constexpr ((R - 9) % 3 == 0) Bc[1 + (R - 9) / 3] = lds16(pb + (1 + (R - 9) / 3) * 512);
  else lds8h<(R - 9) % 3 - 1>(Ac[2 - (R - 9) / 3], pa + (4 + (R - 9) / 3) * 512);
}
template <class F>
DEV void conv_cross(LAS char* pa, LAS char* pb, f32x4 (&acc)[4], u32x4 (&Ac)[7], u32x4 (&Bc)[4], u32x4 (&At)[4], F&& side) {
  sfor<4>([&](auto IT) {
    constexpr int it = IT;
    u32x4 An[7], Bn[4];
    sfor<16>([&](auto M) {
      constexpr int m = M, j = m >> 2, i = m & 3;
      if constexpr (it < 3) {
        if constexpr (m < 14) lds8h<m & 1>(An[6 - (m >> 1)], pa - 64 * (it + 1) + (m >> 1) * 512);
        if constexpr (m == 0 || m == 8) { PIN(); Bn[m / 8] = lds16(pb - 64 * (it + 1) + (m / 8) * 512); }
        if constexpr (m >= 14) Bn[m - 12] = lds16(pb - 64 * (it + 1) + (m - 12) * 512);
      } else if constexpr (m < 8) {
        lds8h<m & 1>(At[m >> 1], pa + (7 - (m >> 1)) * 512);
      }
      side(std::integral_constant<int, it * 16 + m>{});
      mm_vv<it == 0 && j == 0>(acc[i], Ac[3 + i - j], Bc[j]);
      PIN();
    });
    if constexpr (it < 3) {
#pragma unroll
      for (int k = 0; k < 7; ++k) Ac[k] = An[k];
#pragma unroll
      for (int k = 0; k < 4; ++k) Bc[k] = Bn[k];
    }
  });
}
struct ConvF { LAS char *pa, *pb, *pu, *pw, *pr; u32 vo, so, xa, ldsw, v16; f32x4 a[2], b[2]; };
DEV u32 xa_ptr(const Ctx& c, int layer) { return OFF_XC + layer * 2097152 + (c.rb * 4 + c.w) * 16384; }
DEV void conv_setup(const Ctx& c, int layer, ConvF& r) {
  const int qq = c.tid & 3, ii = (c.tid >> 2) & 15, kch = c.w >> 1, khalf = c.w & 1;
  r.pa = c.lds + L_KC + kch * KC_CH + (c.l15 & 3) * KC_COPY + 2 * (2044 - 16 * (8 * khalf + 1) - (c.l15 & 12) + 8 * c.q) - 7 * 512 - 192;
  r.pb = c.lds + L_U + kch * U_CH + 2 * (256 + 16 * (c.l15 - 8 * khalf - 1) + 8 * c.q) - 192;
  r.pu = c.lds + L_U + 512 + c.tid * 8;
  r.pw = c.lds + L_P + c.w * 4096 + (c.q * 16 + (c.l15 ^ (c.q * 4))) * 16;
  r.pr = c.lds + L_P + c.w * 1024 + (qq * 16 + (ii ^ (qq * 4))) * 16;
  r.vo = (c.tid >> 2) * 16384 + (c.tid & 1) * 32 + (c.tid & 2) * 8;
  r.so = OFF_XC + layer * 2097152 + c.cp * 64;
  r.xa = xa_ptr(c, layer); r.ldsw = c.lds0 + L_RING + c.w * 4096; r.v16 = c.lane * 16;
  asm volatile("" : "+v"(r.pa), "+v"(r.pb), "+v"(r.pu), "+v"(r.pw), "+v"(r.pr), "+v"(r.vo), "+v"(r.v16));
  asm volatile("" : "+s"(r.so), "+s"(r.xa), "+s"(r.ldsw));
}
DEV void conv_park(const Ctx& c, const ConvF& r) {
  *(LAS u32x4*)(c.lds + L_CA + c.vt16) = u32x4{(u32)(size_t)r.pa | ((u32)(size_t)r.pb - L_U) << 16, (u32)(size_t)r.pw, (u32)(size_t)r.pr, r.vo};
}
DEV void conv_again(const Ctx& c, const ConvF& o, u32 v16, ConvF& r) {
  const u32x4 a = *(LAS u32x4*)(c.lds + L_CA + c.vt16);
  r.pa = (LAS char*)(size_t)(a.x & 0xffffu); r.pb = (LAS char*)(size_t)((a.x >> 16) + L_U); r.pw = (LAS char*)(size_t)a.y; r.pr = (LAS char*)(size_t)a.z; r.vo = a.w;
  r.pu = c.lds + L_U + 512 + (c.vt16 >> 1);
  r.so = o.so + 2097152; r.xa = o.xa + 2097152; r.ldsw = o.ldsw; r.v16 = v16;
  asm volatile("" : "+v"(r.pa), "+v"(r.pb), "+v"(r.pu), "+v"(r.pw), "+v"(r.pr), "+v"(r.vo));
}
DEV void conv_pad(const Ctx& c) {
  u32x2 z = {0u, 0u};
  asm volatile("" : "+v"(z));
  LAS char* d = c.lds + L_U + (c.vpeek >> 3);
  *(LAS u32x2*)d = z; *(LAS u32x2*)(d + U_CH) = z;
}
DEV void conv_finish2(ConvF& r) {
#pragma unroll
  for (int ch = 0; ch < 2; ++ch) { r.a[ch] = *(LAS f32x4*)(r.pr + ch * 8192); r.b[ch] = *(LAS f32x4*)(r.pr + ch * 8192 + 4096); }
}
DEV void conv_finish3(const Ctx& c, int half, const ConvF& r, const f32x4 (&base)[2]) {
  u32x2 o[2];
#pragma unroll
  for (int ch = 0; ch < 2; ++ch) {
    const f32x4 t = (r.a[ch] + base[ch]) + r.b[ch];
    o[ch].x = pk_bf16(t.x, t.y); o[ch].y = pk_bf16(t.z, t.w);
  }
  const u32 r0 = dpp_u<0xB1>(selu(M_B0, o[1].x, o[0].x)), r1 = dpp_u<0xB1>(selu(M_B0, o[1].y, o[0].y));
  const u32x4 v = {selu(M_B0, o[0].x, r0), selu(M_B0, o[0].y, r1), selu(M_B0, r0, o[1].x), selu(M_B0, r1, o[1].y)};
  st16(c.rs, r.vo, r.so + half * 1048576, v);
}
DEV u32 res_ptr(const Ctx& c, int layer) { return OFF_XC + layer * 2097152 + (c.rt * 2 + (c.w >> 1)) * 16384 + c.db * 2048 + (c.w & 1) * 1024; }

struct G1 {
  u32 xa;
  u32 res, ldsr;
  LAS char* ring[2];
  u32 ldsw;
  u32 v16;
  u64 lanes;
  u32 wd[6][2];
  u32 vst, sst;
  u32 hsrc, hsrc5;
  LAS char* pk;
  LAS char* pws;
  LAS char *rd[4], *prs, *prs1, *exw, *exr, *sts, *stw;
  u32 vo, so, soh, va, sa, eoff, fA, fG, pA, pG, sst2, so1;
  int layer;
};
DEV void ffn_scalars(const Ctx& c, G1& g) {
  const int dfin = 2 * (c.w & 1) + (c.w >> 1);
  g.xa = xa_ptr(c, 0);
  g.res = res_ptr(c, 0);
  g.ldsw = c.lds0 + L_RING + c.w * 4096;
  g.lanes = ~0ull;
#pragma unroll
  for (int i = 0; i < 6; ++i) {
    g.wd[i][0] = wd_ptr(c, 0) + i * 8192 - (i == 5 ? (c.w >> 1) * 2048 : 0); g.wd[i][1] = wd_ptr(c, 1) + i * 8192 - (i == 5 ? (c.w >> 1) * 2048 : 0);
    asm volatile("" : "+s"(g.wd[i][0]), "+s"(g.wd[i][1]));
  }
  g.sst = OFF_H + c.rb * 4 * (KS2 * 1024) + (c.cb * 11 + 3 * c.w) * 256;
  g.sst2 = c.w == 3 ? 0x7f000000u : g.sst;
  g.hsrc = OFF_H + (c.rt * 2 + (c.w & 1)) * (KS2 * 1024) + (c.w >> 1) * 4096;
  g.hsrc5 = g.hsrc + 5 * 8192 - (c.w >> 1) * 2048;
  g.ldsr = c.lds0 + L_RES + c.w * 1024;
  g.so = OFF_XP + 2097152 + (c.db * 32 + dfin * 8) * 8192 + c.rt * 128;
  g.soh = g.so + 4096;
  g.so1 = c.rt * 32768 + c.db * 128 + dfin * 32;
  g.sa = c.w == 0 ? OFF_ST3 + c.rt * 128 : 0x7f000000u;
  g.eoff = c.sflag; g.fA = c.sflag; g.fG = c.sflagg; g.pA = c.speek; g.pG = c.speekg;
  asm volatile("" : "+s"(g.ldsw), "+s"(g.ldsr), "+s"(g.so1), "+s"(g.sa));
}
DEV void ffn_next(G1& g) {
  g.xa += 2097152; g.res += 2097152; g.lanes = 1ull;
  g.sst += H_BYTES; g.sst2 += H_BYTES; g.hsrc += H_BYTES; g.hsrc5 += H_BYTES;
  g.so = g.so1; g.soh = g.so1 + 1048576; g.eoff = 0x7f000000u;
  g.fA += 16384; g.fG += 8192; g.pA += 16384; g.pG += 8192;
}
template <int S0, int S1, class F> DEV void wsum_part(LAS char* pws, F&& side) {
  f32x4 ws[3];
  u32x4 ones = {0x3F803F80u, 0x3F803F80u, 0x3F803F80u, 0x3F803F80u};
  if constexpr (S0 > 0) {
#pragma unroll
    for (int i = 0; i < 3; ++i) ws[i] = *(LAS f32x4*)(pws + i * 64);
  }
  asm volatile("s_nop 1" : "+v"(ones));
  sfor<3 * (S1 - S0)>([&](auto N) { mm_av<(S0 == 0 && N < 3), AW(N % 3, S0 + N / 3)>(ws[N % 3], ones); side(N); });
  asm volatile("s_nop 7" : "+v"(ws[0]), "+v"(ws[1]), "+v"(ws[2]));
#pragma unroll
  for (int i = 0; i < 3; ++i) *(LAS f32x4*)(pws + i * 64) = ws[i];
}
template <int LAYER> DEV void conv_phase(Ctx& c, ConvF& cf, G1& g, u32 dsk, ConvIn& inA, ConvIn& inB) {
  LAS char* const pa = cf.pa + 192;
  LAS char* const pb = cf.pb + 192;
  f32x4 baseA[2], baseB[2];
  u32x4 Ac[4];
  if constexpr (LAYER == 1) {
#pragma unroll
    for (int j = 0; j < 4; ++j) ldsA(Ac[j], pa + (7 - j) * 512);
  }
  conv_wait<LAYER, LAYER == 0 ? 4 : (LD_A < 90 ? 2 : 0)>(inA);
  STAMP(4 + LAYER * 20);
  conv_norm<LAYER, 0>(c, cf.pu, inA, dsk, baseA);
  if constexpr (LAYER == 0) poll_edge<false, 0, E_W>(c, c.speek);
  SYNC_LDS();
  if constexpr (LAYER == 0) {
    flag_st<E_W2>(c, c.sflag);
#pragma unroll
    for (int j = 0; j < 4; ++j) ldsA(Ac[j], pa + (7 - j) * 512);
    sfor<2>([&](auto N) { ld_wgu<N>(c); });
  }
  STAMP(5 + LAYER * 20);
  f32x4 accA[4], acc[4];
  u32x4 Ax[7], Bx[4];
  conv_tri<0, true>(pa, pb, Ac, accA, [&](auto K) {
    if constexpr (LAYER == 0 && K % 4 == 1) ld_wgu<2 + K / 4>(c);
    if constexpr (K >= 30 && K < 39) { cross_first<2 * (K - 30)>(pa, pb, Ax, Bx); PIN(); cross_first<2 * (K - 30) + 1>(pa, pb, Ax, Bx); }
  });
  STAMP(6 + LAYER * 20);
  conv_cross(pa, pb, acc, Ax, Bx, Ac, [&](auto K) {
    if constexpr (K == 3) asm volatile("s_nop 3" : "+v"(accA[0]), "+v"(accA[1]), "+v"(accA[2]), "+v"(accA[3]));
    if constexpr (K < 3) *(LAS f32x4*)(cf.pw + K * 1024) = accA[K];
    if constexpr (K == 3) *(LAS f32x4*)(cf.pw + K * 1024) = accA[K];
    if constexpr (K == 9) { SYNC_LDS(); conv_finish2(cf); }
    if constexpr (K == 14) conv_finish3(c, 0, cf, baseA);
    if constexpr (LAYER == 0) {
      if constexpr (K % 4 == 0) ld_wgu<12 + K / 4>(c);
      if constexpr (K == 30) peek<false, E_W2>(c, c.speek);
    } else {
      if constexpr (K == LD_B) conv_load<1, 1>(c, inB);
    }
  });
  STAMP(7 + LAYER * 20);
  if constexpr (LAYER == 0) sfor<4>([&](auto N) { ld_wgu<28 + N>(c); });
  conv_wait<LAYER, LAYER == 0 ? 30 : 0>(inB);
  conv_norm<LAYER, 1>(c, cf.pu + 2048, inB, dsk, baseB);
  f32x4 ws[3];
  u32x4 ones = {0x3F803F80u, 0x3F803F80u, 0x3F803F80u, 0x3F803F80u};
  if constexpr (LAYER == 0) {
    vm_wait<20>();
    asm volatile("s_nop 1" : "+v"(ones));
    sfor<6>([&](auto N) { mm_av<(N < 3), AW(N % 3, N / 3)>(ws[N % 3], ones); });
    poll_edge<false, 12, E_W2>(c, c.speek);
  }
  SYNC_LDS();
  STAMP(8 + LAYER * 20);
  u32 td;
  conv_tri<4, false>(pa, pb, Ac, acc, [&](auto K) {
    if constexpr (LAYER == 0 && K % 8 == 2) ld_wgu<32 + K / 8>(c);
    if constexpr (K >= SP_X && (K - SP_X) % 4 == 0 && (K - SP_X) / 4 < 8)
      dmap<((K - SP_X) / 16) * 4096, ((K - SP_X) / 16) * 16384, ((K - SP_X) / 4) & 3>(c.rs, cf.v16, cf.xa, cf.ldsw, td);
  });
  MM_DONE4(acc);
#pragma unroll
  for (int i = 0; i < 4; ++i) *(LAS f32x4*)(cf.pw + i * 1024) = acc[i];
  if constexpr (LAYER == 0) {
    vm_wait<17>();
    sfor<15>([&](auto N) { mm_av<false, AW(N % 3, 2 + N / 3)>(ws[N % 3], ones); if constexpr (N % 5 == 0) ld_wgu<37 + N / 5>(c); });
  }
  SYNC_LDS();
  conv_finish2(cf);
  if constexpr (LAYER == 0) { PIN(); sfor<3 * WS0 - 21>([&](auto N) { mm_av<false, AW(N % 3, 7 + N / 3)>(ws[N % 3], ones); }); PIN(); }
  conv_finish3(c, 1, cf, baseB);
  STAMP(9 + LAYER * 20);
  if constexpr (LAYER == 0) {
    st16(c.rs, cf.vo, cf.so + 2097152, all_ones());
    asm volatile("" : "+v"(ws[0]), "+v"(ws[1]), "+v"(ws[2]));
#pragma unroll
    for (int i = 0; i < 3; ++i) *(LAS f32x4*)(c.lds + L_WS + c.w * 192 + c.q * 16 + i * 64) = ws[i];
    sfor<4>([&](auto N) { ld_wgu<40 + N>(c); });
    ffn_scalars(c, g);
    asm volatile("" : "+s"(g.xa), "+s"(g.res), "+s"(g.sst), "+s"(g.sst2), "+s"(g.hsrc), "+s"(g.hsrc5), "+s"(g.so), "+s"(g.soh));
    sfor<4>([&](auto N) { ld_wgu<44 + N>(c); });
  }
}

DEV void h_refetch(const Ctx& c, u32 vst, u32 sst) {
}
template <int K> DEV void g1_verify(const Ctx& c, const G1& g, u32 mx) {
  if (__builtin_expect(__builtin_amdgcn_ballot_w64(mx == 0xffffffffu) != 0ull, 0)) {
#pragma unroll 1
    for (u32 sp = 0; sp < SPIN_CAP; ++sp) {
      dma4<K * 4096, K * 16384>(c.rs, g.v16, g.xa, g.ldsw);
      vm_wait<0>();
      u32 m = 0;
#pragma unroll
      for (int i = 0; i < 4; ++i) m = umax5(m, lds16(g.rd[0] + K * 16384 + i * 1024));
      if (!__builtin_amdgcn_ballot_w64(m == 0xffffffffu)) break;
      __builtin_amdgcn_s_sleep(2);
    }
  }
  FENCE();
}
template <int N> DEV void ld_wd(const Ctx& c, const G1& g) { ld_wd<N>(c, g.v16, g.wd[N >> 3][0], g.wd[N >> 3][1], g.lanes); }
template <int HALF, bool STATS, int S>
DEV void g1_step(const Ctx& c, const G1& g, f32x4 (&acc)[3][4], u32x4 (&Bc)[4], u32& td, u32& mx) {
  if constexpr (HALF == 0 && (S & 3) == 3 && S < 12) g1_verify<(S + 1) / 4>(c, g, mx);
  if constexpr (S == 3) {
    vm_wait<4>(); BARRIER(); FENCE();
    if constexpr (STATS && HALF == 0) flag_st<E_CONV + 1>(c, g.fA);
    if constexpr (STATS && HALF == 1) flag_st<0>(c, g.fG);
  }
  if constexpr (HALF == 0 && (S == 7 || S == 11)) { BARRIER(); FENCE(); }
  if constexpr (HALF == 1 && S == 7) { vm_wait<9>(); BARRIER(); FENCE(); }
  if constexpr (HALF == 1 && S == 11) { if constexpr (!STATS) poll_edge<true, 6, 0>(c, g.pG); vm_wait<11>(); BARRIER(); FENCE(); }
  u32x4 Bn[4], ck[4];
  sfor<12>([&](auto M) {
    constexpr int m = M, ct = m / 3, k = m % 3;
    if constexpr (HALF == 0 && (S & 3) == 2 && S < 12) {
      if constexpr (m == 0) { vm_wait<S == 10 ? 13 : 5>(); mx = 0; }
      if constexpr (k == 1) ck[ct] = lds16(g.rd[0] + ((S + 2) / 4) * 16384 + ct * 1024);
      if constexpr (m == 5 || m == 8) mx = umax5(mx, ck[ct - 1]);
      if constexpr (m == 11) mx = umax5(mx, ck[2]);
    }
    if constexpr (S < 15 && k < 2) {
      constexpr int slot = (HALF * 4 + (S + 1) / 4) % 7;
      const u32x2 r = tr_read(g.ring[slot >> 2] + (slot & 3) * 16384 + ct * 4096 + ((S + 1) & 3) * 1024 + k * 512);
      if constexpr (k == 0) { Bn[ct].x = r.x; Bn[ct].y = r.y; } else { Bn[ct].z = r.x; Bn[ct].w = r.y; }
    }
    if constexpr (k == 2 && (ct & 1) == 0) {
      constexpr int n = 2 * (S - 1) + (ct >> 1);
      constexpr int nx = (HALF == 0 && S < 2) ? n + 2 : n;
      if constexpr (HALF == 0 ? (S <= 4 && S != 2) : (S >= 1 && S <= 4)) dmap<HALF * 1048576 + 8192 + (nx >> 2) * 4096, ((HALF * 4 + 2 + (nx >> 2)) % 7) * 16384, nx & 3>(c.rs, g.v16, g.xa, g.ldsw, td);
      if constexpr (HALF == 1 && S == 5 && ct == 0) dma1<0, 0>(c.rs, g.v16, g.res, g.ldsr);
      if constexpr (HALF == 1 && S >= 11) dmap<((n - 20) >> 2) * 8192, (((n - 20) >> 2) + 1) * 16384, (n - 20) & 3>(c.rs, g.v16, g.hsrc, g.ldsw, td);
      if constexpr (HALF == 0 && n >= 9) ld_wd<n - 9>(c, g);
      if constexpr (HALF == 1 && n >= 9 && n <= 19) ld_wd<24 + n>(c, g);
    }
    if constexpr (HALF == 0 && S >= 13 && m == 5) {
      u32 t;
      asm volatile("s_add_u32 %0, %4, %5\n\tbuffer_store_dwordx4 %1, %2, %3, %0 offen offset:%c6 sc1\n\ts_nop 1"
                   : "=&s"(t) : "v"(u32x4{~0u, ~0u, ~0u, ~0u}), "v"(g.vst), "s"(c.rs), "s"(S == 15 ? g.sst2 : g.sst), "i"(HB_OFF), "i"((S - 13) * 256) : "memory", "scc");
    }
    if constexpr (HALF == 1 && S == 5 && m == 5) dma1<1048576, 4096>(c.rs, g.v16, g.res, g.ldsr);
    if constexpr (HALF == 0 && m == 5 && S >= 6 && S <= 12) ld_wd<15 + S>(c, g);
    if constexpr (!STATS && HALF == 0 && S == 12 && m == 11) peek<false, E_CONV + 1>(c, g.pA);
    if constexpr (!STATS && HALF == 1 && S == 7 && m == 11) peek<true, 0>(c, g.pG);
    if constexpr (STATS && k == 2) mm_vv<S == 0>(acc[k][ct], Bc[ct], Bc[ct]);
    else if constexpr (!(STATS && k == 0 && S >= 12)) mm_av<S == 0, AW(k, S)>(acc[k][ct], Bc[ct]);
    PIN();
  });
  if constexpr (HALF == 0 && (S & 3) == 2 && S < 12) mx = umax5(mx, ck[3]);
  if constexpr (S < 15) {
#pragma unroll
    for (int ct = 0; ct < 4; ++ct) Bc[ct] = Bn[ct];
    g1_step<HALF, STATS, S + 1>(c, g, acc, Bc, td, mx);
  } else {
    MM_DONE4(acc[2]); asm volatile("" : "+v"(acc[0][3]), "+v"(acc[1][3]), "+v"(acc[0][2]), "+v"(acc[1][2]), "+v"(acc[0][1]), "+v"(acc[1][1]), "+v"(acc[1][0]));
  }
}
template <int HALF, int N> DEV void g1_stage(const Ctx& c, const G1& g, u32& td) {
  if constexpr (HALF == 0) {
    if constexpr (N < 4) dmap<1048576, 4 * 16384, N>(c.rs, g.v16, g.xa, g.ldsw, td);
    else if constexpr (N >= 6 && N < 10) dmap<1048576 + 4096, 5 * 16384, N - 6>(c.rs, g.v16, g.xa, g.ldsw, td);
    else if constexpr (N >= 10 && N < 15) ld_wd<18 + N>(c, g);
  } else {
    if constexpr (N == 0 || N == 3) dmap<2 * 8192 + 2048, 3 * 16384 + 2048, N / 3>(c.rs, g.v16, g.hsrc, g.ldsw, td);
    else if constexpr (N >= 6 && N < 12 && N % 3 != 2) dmap<3 * 8192, 4 * 16384, ((N - 6) / 3) * 2 + N % 3>(c.rs, g.v16, g.hsrc, g.ldsw, td);
  }
  PIN();
}
template <int HALF, int I, int POS, class F>
DEV void g1_tile(const Ctx& c, const G1& g, const f32x4 (&acc)[4], const f32x4 (&k4)[4], const f32x4& ws, F&& next) {
  u32 hv[4], td;
  sfor<2>([&](auto P) {
    constexpr int pi = P, n0 = 6 * POS + 3 * pi;
    f32x2 gg[2], gu[2], ex[2];
    if constexpr (n0 == 15) { next(); PIN(); }
    g1_stage<HALF, n0>(c, g, td);
#pragma unroll
    for (int e = 0; e < 2; ++e) {
      const f32x4 k = k4[2 * pi + e];
      gg[e] = __builtin_elementwise_fma(k.xx, ws.xy, acc[2 * pi + e].xy);
      const f32x2 ar = gg[e] * __builtin_elementwise_fma(gg[e] * gg[e], k.ww, k.zz);
      ex[e] = f32x2{__builtin_amdgcn_exp2f(ar.x), __builtin_amdgcn_exp2f(ar.y)};
    }
    PIN();
    g1_stage<HALF, n0 + 1>(c, g, td);
#pragma unroll
    for (int e = 0; e < 2; ++e) {
      const f32x4 k = k4[2 * pi + e];
      gu[e] = gg[e] * __builtin_elementwise_fma(k.xx, ws.zw, acc[2 * pi + e].zw);
      ex[e] = __builtin_elementwise_fma(ex[e], k.yy, k.yy);
    }
#pragma unroll
    for (int e = 0; e < 2; ++e) ex[e] = f32x2{__builtin_amdgcn_rcpf(ex[e].x), __builtin_amdgcn_rcpf(ex[e].y)};
#pragma unroll
    for (int e = 0; e < 2; ++e) gu[e] *= ex[e];
#pragma unroll
    for (int e = 0; e < 2; ++e) hv[2 * pi + e] = pk_bf16(gu[e].x, gu[e].y);
    PIN();
    g1_stage<HALF, n0 + 2>(c, g, td);
  });
  swap32(hv[0], hv[2]); swap32(hv[1], hv[3]);
  swap16(hv[0], hv[1]); swap16(hv[2], hv[3]);
  u32 t;
  asm volatile("s_add_u32 %0, %4, %5\n\tbuffer_store_dwordx4 %1, %2, %3, %0 offen offset:%c6 sc1\n\ts_nop 1"
               : "=&s"(t) : "v"(u32x4{hv[0], hv[1], hv[2], hv[3]}), "v"(g.vst), "s"(c.rs), "s"(g.sst), "i"(HALF * HB_OFF), "i"(I * 256) : "memory", "scc");
  PIN();
}
template <int HALF, class F>
DEV void g1_unit(const Ctx& c, const G1& g, u32x4 (&Bc)[4], F&& next) {
  f32x4 acc[3][4], k4[4], ws[3];
  u32 td, mx;
  if (c.w == 3) {
    g1_step<HALF, true, 0>(c, g, acc, Bc, td, mx);
    float sm[4], sq[4], s0[4], s1[4];
#pragma unroll
    for (int ct = 0; ct < 4; ++ct) { sm[ct] = acc[1][ct][0]; s0[ct] = sel(M_B0, acc[2][ct][0], acc[2][ct][1]); s1[ct] = sel(M_B0, acc[2][ct][2], acc[2][ct][3]); }
#pragma unroll
    for (int ct = 0; ct < 4; ++ct) sq[ct] = sel(M_B1, s0[ct], s1[ct]);
    swap32f(sm[0], sm[2]); swap32f(sm[1], sm[3]); swap32f(sq[0], sq[2]); swap32f(sq[1], sq[3]);
    swap16f(sm[2], sm[3]); swap16f(sq[0], sq[1]); swap16f(sq[2], sq[3]);
    const float mu = sm[3] * (-1.f / DM);
    const float e2 = sel(M_B3, sel(M_B2, sq[0], sq[1]), sel(M_B2, sq[2], sq[3]));
    const float vr = fmaxf(__builtin_fmaf(-mu, mu, __builtin_fmaf(e2, 1.f / DM, 1e-5f)), 1e-5f);
    const float r = __builtin_amdgcn_rsqf(vr);
    const float c1 = (-2.f * 0.7978845608f * 1.44269504f) * r;
    *(LAS f32x4*)(g.sts - 1024) = f32x4{mu, vr, c1, (c1 * 0.044715f) * (r * r)};
    SYNC_LDS();
    STAMP(12 + 2 * HALF + g.layer * 20);
    LAS char* last = g.ring[0] + (HALF ? 0 : 3 * 16384);
    u32x4 B[2][4];
    sfor<16>([&](auto N) {
      constexpr int s = N >> 2, ct = N & 3;
      if constexpr (s < 3) B[s & 1][ct] = tr_frag(last + ct * 4096 + s * 1024);
      else k4[ct] = *(LAS f32x4*)(g.pk + ct * 256);
      if constexpr (N % 3 == 0) g1_stage<HALF, N / 3>(c, g, td);
      if constexpr (s == 0) mm_av<false, AW(0, 15)>(acc[0][ct], Bc[ct]);
      else mm_av<false, AW(0, 11 + s)>(acc[0][ct], B[(s - 1) & 1][ct]);
      PIN();
    });
    if constexpr (HALF == 0) { if (g.layer == 0) wsum_part<WS0, 16>(g.pws, [](auto) {}); }
#pragma unroll
    for (int i = 0; i < 2; ++i) ws[i] = *(LAS f32x4*)(g.pws + i * 64);
    flag_st<0>(c, 0x7f000000u);
    asm volatile("s_nop 3" : "+v"(acc[0][0]), "+v"(acc[0][1]), "+v"(acc[0][2]), "+v"(acc[0][3]));
  } else {
    g1_step<HALF, false, 0>(c, g, acc, Bc, td, mx);
    if constexpr (HALF == 0) { if (g.layer == 0) wsum_part<WS0, 16>(g.pws, [](auto) {}); poll_edge<false, 9, E_CONV + 1>(c, g.pA); }
#pragma unroll
    for (int i = 0; i < 3; ++i) ws[i] = *(LAS f32x4*)(g.pws + i * 64);
    BARRIER(); FENCE();
    STAMP(12 + 2 * HALF + g.layer * 20);
#pragma unroll
    for (int ct = 0; ct < 4; ++ct) k4[ct] = *(LAS f32x4*)(g.pk + ct * 256);
    g1_tile<HALF, 2, 0>(c, g, acc[2], k4, ws[2], next);
  }
  asm volatile("" : "+v"(k4[0]), "+v"(k4[1]), "+v"(k4[2]), "+v"(k4[3]));
  g1_tile<HALF, 1, 1>(c, g, acc[1], k4, ws[1], next);
  g1_tile<HALF, 0, 2>(c, g, acc[0], k4, ws[0], next);
}

template <int K> DEV LAS char* g2_frag(const G1& g, int other, int ks) { return g.rd[other * 2 + ((K + 1) >> 2)] + ((K + 1) & 3) * 16384 + ks * 1024; }
template <int K> DEV void g2_verify(const Ctx& c, const G1& g, u32x4 (&B)[8], u32 mx) {
  constexpr int NK = K < 5 ? 4 : 2;
  if (__builtin_expect(__builtin_amdgcn_ballot_w64(mx == 0xffffffffu) != 0ull, 0)) {
#pragma unroll 1
    for (u32 sp = 0; sp < SPIN_CAP; ++sp) {
      LGKM0();
      if constexpr (K < 5) dma4<HB_OFF + K * 8192, (K + 1) * 16384>(c.rs, g.v16, g.hsrc, g.ldsw);
      else { dma1<HB_OFF, 6 * 16384>(c.rs, g.v16, g.hsrc5, g.ldsw); dma1<HB_OFF + 1024, 6 * 16384 + 1024>(c.rs, g.v16, g.hsrc5, g.ldsw); }
      vm_wait<0>();
      u32 m = 0;
#pragma unroll
      for (int ks = 0; ks < NK; ++ks) { B[2 * ks] = lds16(g2_frag<K>(g, 0, ks)); m = umax3(umax3(m, B[2 * ks].x, B[2 * ks].y), B[2 * ks].z, B[2 * ks].w); }
      if (!__builtin_amdgcn_ballot_w64(m == 0xffffffffu)) break;
      __builtin_amdgcn_s_sleep(2);
    }
  }
  FENCE();
}
template <int HALF, int C>
DEV void g2_chunk(const Ctx& c, const G1& g, int layer, f32x4 (&acc)[4], u32x4 (&Bc)[8], u32& td, ConvIn& inA) {
  if constexpr (HALF == 0 && C == 2) { vm_wait<8>(); BARRIER(); FENCE(); }
  static_assert(SP_H >= 32 && SP_H + 8 * SP_D >= 48, "ring slots");
  if constexpr (HALF == 0 && C == 3) { vm_wait<(SP_H < 48 ? (47 - SP_H) / SP_D + 1 : 0)>(); BARRIER(); FENCE(); }
  constexpr int NK = C < 5 ? 4 : 2, NKN = C < 4 ? 4 : 2;
  u32x4 Bn[8];
  u32 mx = 0;
  sfor<NK * 4>([&](auto M) {
    constexpr int m = M, ks = (m >> 3) * 2 + ((m >> 1) & 1), ct = (m >> 2) & 1, dt = m & 1, n = (C * 4 + ks) * 2 + dt;
    if constexpr (HALF == 0) {
      if constexpr (C < 5 && m < NKN * 2) Bn[m] = lds16(g2_frag<C + 1>(g, m & 1, m >> 1));
      if constexpr (C == 0 && m % 3 == 0) {
        if constexpr (m < 12) dmap<4 * 8192, 5 * 16384, m / 3>(c.rs, g.v16, g.hsrc, g.ldsw, td);
        else dmap<0, 6 * 16384, m / 3 - 4>(c.rs, g.v16, g.hsrc5, g.ldsw, td);
      }
      constexpr int x = C * 16 + m - SP_H;
      if constexpr (x >= 0 && x % SP_D == 0 && x / SP_D < 12) dmap<HB_OFF + (x / SP_D / 4) * 8192, (x / SP_D / 4 + 1) * 16384, (x / SP_D) & 3>(c.rs, g.v16, g.hsrc, g.ldsw, td);
    } else if constexpr (C < 5) {
      if constexpr (C == 0 && (m == 4 || m >= 11)) {
        constexpr int j = m == 4 ? 0 : m - 10;
        if constexpr (j < 4) dma1<HB_OFF + 4 * 8192 + j * 1024, 5 * 16384 + j * 1024>(c.rs, g.v16, g.hsrc, g.ldsw);
        else dma1<HB_OFF + (j - 4) * 1024, 6 * 16384 + (j - 4) * 1024>(c.rs, g.v16, g.hsrc5, g.ldsw);
      }
      if constexpr (m == 0) vm_wait<C == 0 ? 9 : (C == 1 ? 11 : (C == 2 ? 7 : (C == 3 ? 2 : 0)))>();
      if constexpr (m < NKN) Bn[2 * m] = lds16(g2_frag<C + 1>(g, 0, m));
      if constexpr (m >= 5 && m < 5 + NKN) { mx = umax3(umax3(mx, Bn[2 * (m - 5)].x, Bn[2 * (m - 5)].y), Bn[2 * (m - 5)].z, Bn[2 * (m - 5)].w); }
      if constexpr (m == 9) {
        g2_verify<C + 1>(c, g, Bn, mx);
        BARRIER(); FENCE();
      }
      if constexpr (m >= 9 && m < 9 + NKN) Bn[2 * (m - 9) + 1] = lds16(g2_frag<C + 1>(g, 1, m - 9));
    }
    if constexpr (HALF == 1 && C * 16 + m == LD_A) conv_load<1, 0>(c, inA, layer);
    if constexpr (n < NWDA) mm_av<C == 0 && ks == 0 && dt == 1, 192 + 4 * n>(acc[dt * 2 + ct], Bc[ks * 2 + ct]);
    else mm_wv<VWD + 4 * (n - NWDA)>(acc[dt * 2 + ct], Bc[ks * 2 + ct]);
    PIN();
  });
  if constexpr (C < 5) {
#pragma unroll
    for (int m = 0; m < NKN * 2; ++m) Bc[m] = Bn[m];
    g2_chunk<HALF, C + 1>(c, g, layer, acc, Bc, td, inA);
  }
}
template <int HALF> DEV void g2_res(const G1& g, u32x2 (&r)[2]) {
#pragma unroll
  for (int ct = 0; ct < 2; ++ct) r[ct] = tr_read((ct ? g.prs1 : g.prs) + HALF * 4096);
}
DEV void g2_init(const u32x2 (&r)[2], f32x4 (&acc)[4]) {
#pragma unroll
  for (int ct = 0; ct < 2; ++ct) acc[ct] = f32x4{bf_lo(r[ct].x), bf_hi(r[ct].x), bf_lo(r[ct].y), bf_hi(r[ct].y)};
  asm volatile("s_nop 1" : "+v"(acc[0]), "+v"(acc[1]));
}
DEV void g2_first(const G1& g, u32x4 (&Bc)[8]) {
#pragma unroll
  for (int m = 0; m < 8; ++m) Bc[m] = lds16(g2_frag<0>(g, m & 1, m >> 1));
}
template <int HALF>
DEV void g2_unit(const Ctx& c, const G1& g, const Params& p, int layer, f32x4 (&acc)[4], u32x4 (&Bc)[8], ConvIn& inA) {
  u32 td;
  g2_chunk<HALF, 0>(c, g, layer, acc, Bc, td, inA);
  constexpr int NSP = (87 - SP_H) / SP_D + 1 < 12 ? (87 - SP_H) / SP_D + 1 : 12;
  MM_DONE4(acc);
  STAMP(16 + 2 * HALF + layer * 20);
#pragma unroll
  for (int ct = 0; ct < 2; ++ct) *(LAS f32x4*)(g.exw + HALF * 8192 + ct * 1024) = acc[2 + ct];
  if constexpr (HALF == 0) sfor<12 - NSP>([&](auto X) { dmap<HB_OFF + ((NSP + X) / 4) * 8192, ((NSP + X) / 4 + 1) * 16384, (NSP + X) & 3>(c.rs, g.v16, g.hsrc, g.ldsw, td); });
  SYNC_LDS();
  f32x4 ex[2];
#pragma unroll
  for (int ct = 0; ct < 2; ++ct) ex[ct] = *(LAS f32x4*)(g.exr + HALF * 8192 + ct * 1024);
  if constexpr (HALF == 0) dma4<HB_OFF + 3 * 8192, 4 * 16384>(c.rs, g.v16, g.hsrc, g.ldsw);
  if constexpr (HALF == 1 && LD_A == 89) conv_load<1, 0>(c, inA, layer);
  u32 v[4];
#pragma unroll
  for (int ct = 0; ct < 2; ++ct) {
    const f32x4 o = acc[ct] + ex[ct];
    v[2 * ct] = pk_bf16(o[0], o[1]); v[2 * ct + 1] = pk_bf16(o[2], o[3]);
  }
  if (layer == 0) {
    float st[4];
#pragma unroll
    for (int ct = 0; ct < 2; ++ct) {
      st[2 * ct] = dot2(v[2 * ct], 0x3F803F80u, dot2(v[2 * ct + 1], 0x3F803F80u, 0.f));
      st[2 * ct + 1] = dot2(v[2 * ct], v[2 * ct], dot2(v[2 * ct + 1], v[2 * ct + 1], 0.f));
    }
    swap32f(st[0], st[2]); swap32f(st[1], st[3]);
    float sA = st[0] + st[2], sB = st[1] + st[3];
    swap16f(sA, sB);
    *(LAS float*)g.stw = sA + sB;
    u32 t0 = dpp_u<0xB1>(selu(M_B0, v[1], v[0])), t1 = dpp_u<0xB1>(selu(M_B0, v[3], v[2]));
    const u32 a0 = selu(M_B0, v[0], t0), a1 = selu(M_B0, t0, v[1]), a2 = selu(M_B0, v[2], t1), a3 = selu(M_B0, t1, v[3]);
    t0 = dpp_u<0x4E>(selu(M_B1, a2, a0)); t1 = dpp_u<0x4E>(selu(M_B1, a3, a1));
    st16(c.rs, g.vo, HALF ? g.soh : g.so, u32x4{selu(M_B1, a0, t0), selu(M_B1, a1, t1), selu(M_B1, t0, a2), selu(M_B1, t1, a3)});
    SYNC_LDS();
    const f32x4 s4 = *(LAS f32x4*)g.sts;
    const float y = ((s4.x + s4.y) + (s4.z + s4.w)) * 0.0625f, fy = __builtin_floorf(y);
    const bool fits = fabsf(y) < 0x1p27f;
    const u32x2 sv = {fits ? ((u32)((y - fy) * 0x1p32f) & ~0xffu) | 1u : 0x11u, fits ? (u32)(int)fy : 0u};
    asm volatile("buffer_atomic_add_x2 %0, %1, %2, %3 offen" ::"v"(sv), "v"(g.va), "s"(c.rs), "s"(g.sa + HALF * 16384) : "memory");
  } else {
    swap16(v[0], v[2]); swap16(v[1], v[3]);
    st16<0>(mk_rsrc(p.out, SEQ * DM * 2), g.vo, HALF ? g.soh : g.so, u32x4{v[0], v[1], v[2], v[3]});
  }
  STAMP(17 + 2 * HALF + layer * 20);
}

__global__ void __launch_bounds__(256, 1) __attribute__((amdgpu_num_vgpr(140))) mega_kernel(Params p) {
  asm volatile("" ::: "v255", "a255");
  __shared__ __attribute__((aligned(16))) char smem[LDS_BYTES];
  const int w0 = __builtin_amdgcn_readfirstlane(threadIdx.x >> 6);
  Ctx c;
  c.ws = p.ws; c.lds = (LAS char*)smem; c.lds0 = (u32)(size_t)c.lds;
  c.rs = mk_rsrc(p.ws, WS_BYTES);
  refresh(c, w0);
  STAMP(0);
  u32 dsk;
  ConvIn inA, inB;
  ConvF cf;
  u32 pf_sink;
  {
    u64 pc; u32 len;
    asm volatile("s_getpc_b64 %0\n1:\n\ts_mov_b32 %1, .Lmega_tail-1b" : "=s"(pc), "=s"(len));
    const u32 off = ((c.b >> 3) * 16 + (c.tid & 15)) * 128;
    pf_sink = 0;
    if (c.tid < 16 && off + 4 <= len) pf_sink = *(const GAS u32*)(pc + off);
  }

  {
    const int b = c.b, tid = c.tid, lane = c.lane, w = c.w, q = c.q, l15 = c.l15;
    LAS char* lds = c.lds;
    const int kch = w >> 1, khalf = w & 1;
    const rsrc_t rx = mk_rsrc(p.x0, SEQ * DM * 2), rg = mk_rsrc(p.wg, DM * DF * 2), ru = mk_rsrc(p.wu, DM * DF * 2), rd = mk_rsrc(p.wd, DM * DF * 2);
    const u32x4 r0 = ld16p(rx, lane * 16, (8 * b + 2 * w) * 1024), r1 = ld16p(rx, lane * 16 + 1024, (8 * b + 2 * w) * 1024);
    u32x4 xt[2];
#pragma unroll
    for (int i = 0; i < 2; ++i) xt[i] = ld16p(rx, (tid >> 3) * 1024 + (tid & 7) * 16 + i * 128, (b >> 2) * 32768 + (b & 3) * 256);
    const int dch = 2 * c.cp + kch, pn = dch * NS + 64 * khalf + lane;
    const u16 pA = p.logA[pn], pI = p.Aim[pn], pCr = p.Cre[pn], pCi = p.Cim[pn], pdt = p.logdt[dch];
    const u16 pCr2 = p.Cre[pn ^ 64], pCi2 = p.Cim[pn ^ 64];
    dsk = *(const u32*)(p.Dskip + 2 * c.cp);
    const u32 ep = __hip_atomic_load((GAS u32*)(p.ws + OFF_EPOCH), RLX_AGENT);
    const int cbR = b >> 4, sR = b & 15;
    u32x4 wg[2], wu[2];
    int wrow[2], wtau[2];
#pragma unroll
    for (int i = 0; i < 2; ++i) {
      const int id = tid + 256 * i;
      wrow[i] = (id * 5958) >> 16; wtau[i] = id - wrow[i] * 11;
      const u32 vo = wrow[i] * (DF * 2) + wtau[i] * 16 + (id < 352 ? 0u : 0x40000000u) + sR * (32 * DF * 2) + cbR * 172;
      wg[i] = ld16p(rg, vo, 0); wu[i] = ld16p(ru, vo, 0);
    }
    u32x4 dv[2];
    const int s2 = b >> 2, dq = b & 3;
#pragma unroll
    for (int i = 0; i < 2; ++i) {
      const int krow = (tid >> 4) + 16 * i, U = 4 * s2 + (krow >> 3), cbU = (U * 5958) >> 16, fl = 8 * (U - cbU * 11) + (krow & 7);
      dv[i] = ld16p(rd, (86 * cbU + fl) * 1024 + (tid & 15) * 16 + dq * 256 + (fl < 86 ? 0u : 0x40000000u), 0);
    }
    LAS char* wxt = lds + L_XT + (tid >> 3) * 272 + (tid & 7) * 16;
    LAS char* rxt = lds + L_XT + (tid & 7) * (4 * 272) + (tid >> 3) * 4;
    u32 vst = selu(0x0001000100010001ull, selu(0x0006000600060006ull, 0x7f000000u, OFF_ST3 + b * 128 + w * 32 - 16 + l15 * 16 + q * 4),
                   OFF_ST1 + (b >> 7) * 8192 + (w & 1) * 4096 + ((2 * b + (w >> 1)) & 255) * 16 + q * 4);
    u32 vxp = (tid >> 3) * 8192 + (tid & 7) * 16;
    u32 sxp = OFF_XP + (b & 3) * (64 * 8192) + (b >> 2) * 128;
    LAS char* wi1[2] = {lds + L_IMG1 + wtau[0] * 1056 + wrow[0] * 32, lds + selu(__builtin_amdgcn_ballot_w64(tid < 96), L_DUMMY + tid * 16, L_IMG1 + wtau[1] * 1056 + wrow[1] * 32)};
    LAS char* wi2 = lds + L_IMG2 + ((tid & 15) >> 1) * 1056 + (4 * (tid >> 7) + ((tid >> 4) & 3) + ((tid & 64) ? 16 : 0)) * 32 + (tid & 1) * 16;
    asm volatile("" : "+v"(wxt), "+v"(rxt), "+v"(vst), "+v"(vxp), "+v"(wi1[0]), "+v"(wi1[1]), "+v"(wi2));
    asm volatile("" : "+s"(sxp));
    {
      const u32x4 z = {0u, 0u, 0u, 0u};
      *(LAS u32x4*)(lds + L_KC + (l15 >> 1) * KC_COPY + 4096 + (l15 & 1) * 16) = z;
      *(LAS u32x4*)(lds + L_KT - 16 + (l15 & 1) * KT_CH) = z;
    }
    flag_setup(c);
    conv_pad(c);
    LAS char* ci[11];
#pragma unroll
    for (int i = 0; i < 11; i += 2) {
      const int l0 = 2047 - 8 * tid - i, l1 = l0 - 1;
      const u32 pb = pk_bf16((float)(i >= 8 ? max(l0, 0) : l0), (float)(i >= 8 ? max(l1, 0) : l1));
      const int b0 = (int)(pb & 0xffffu) - (134 << 7) + 128, b1 = (int)(pb >> 16) - (134 << 7) + 128;
      ci[i] = lds + L_KT + 4 * max(b0, min(l0, 128));
      asm volatile("" : "+v"(ci[i]));
      if (i < 10) { ci[i + 1] = lds + L_KT + 4 * max(b1, min(l1, 128)); asm volatile("" : "+v"(ci[i + 1])); }
    }
    PIN();
    STAMP(42);
    asm volatile("" :: "v"(pf_sink));
#pragma unroll
    for (int i = 0; i < 2; ++i) *(LAS u32x4*)(wxt + i * 128) = xt[i];
    {
      float s0 = 0.f, q0 = 0.f, s1 = 0.f, q1 = 0.f;
#pragma unroll
      for (int i = 0; i < 4; ++i) {
        s0 = dot2(r0[i], 0x3F803F80u, s0); q0 = dot2(r0[i], r0[i], q0);
        s1 = dot2(r1[i], 0x3F803F80u, s1); q1 = dot2(r1[i], r1[i], q1);
      }
      swap32f(s0, s1); swap32f(q0, q1);
      float sA = s0 + s1, sB = q0 + q1;
      swap16f(sA, sB);
      float sv = sA + sB;
      sv += dpp_f<0xB1>(sv); sv += dpp_f<0x4E>(sv); sv += dpp_f<0x141>(sv); sv += dpp_f<0x140>(sv);
      __builtin_amdgcn_raw_buffer_store_b32(selu(0x0001000100010001ull, 0u, __builtin_bit_cast(u32, sv)), c.rs, vst, 0, 16);
    }
    SYNC_LDS();
    {
      u32x4 o[2];
#pragma unroll
      for (int i = 0; i < 2; ++i) o[i] = u32x4{*(LAS u32*)(rxt + i * 128), *(LAS u32*)(rxt + i * 128 + 272), *(LAS u32*)(rxt + i * 128 + 544), *(LAS u32*)(rxt + i * 128 + 816)};
      PIN();
      st16(c.rs, vxp, sxp, o[0]); st16(c.rs, vxp, sxp + 32 * 8192, o[1]);
    }
    STAMP(43);
    c.epoch = ep + 1u;

    const float dt = rnd_bf16(expf(bf2f(pdt)));
    const float a2 = -rnd_bf16(expf(bf2f(pA))) * dt * 1.44269504f;
    const float ai = bf2f(pI) * dt;
    const float i2p_hi = 0.15915494f, i2p_lo = (float)(0.15915494309189535 - (double)0.15915494f);
    const float fhi = ai * i2p_hi, flo = __builtin_fmaf(ai, i2p_hi, -fhi) + ai * i2p_lo;
    f32x2 wp[3];
#pragma unroll
    for (int i = 0; i < 3; ++i) {
      const float ps = i == 0 ? 1.f : (i == 1 ? 8.f : 128.f);
      const float mag = __builtin_amdgcn_exp2f(a2 * ps);
      const float rev = __builtin_fmaf(flo, ps, __builtin_amdgcn_fractf(fhi * ps));
      wp[i] = f32x2{mag * __builtin_amdgcn_cosf(rev), mag * __builtin_amdgcn_sinf(rev)};
    }
    const float cr = bf2f(pCr), cim = bf2f(pCi);
    int ce = 14 - __builtin_amdgcn_frexp_expf(wave_max(fmaxf(fmaxf(fabsf(cr), fabsf(cim)), fmaxf(fabsf(bf2f(pCr2)), fabsf(bf2f(pCi2))))));
    ce = ce > 100 ? 100 : (ce < -100 ? -100 : ce);
    LAS char* tb = lds + L_TB + w * TB_WAVE + lane * 4;
    auto put2 = [&](int ra, f32x2 va, int rb2, f32x2 vb) {
      *(LAS u32*)(tb + ra * TB_ROW) = __builtin_bit_cast(u32, __builtin_convertvector(va, f16x2));
      *(LAS u32*)(tb + rb2 * TB_ROW) = __builtin_bit_cast(u32, __builtin_convertvector(vb, f16x2));
    };
    {
      const f32x2 w8c = {wp[1].x, -wp[1].y}, w16c = cmul(w8c, w8c);
      f32x2 y0 = {ldexpf(cr, ce), -ldexpf(cim, ce)}, y1 = cmul(y0, w8c);
#pragma unroll
      for (int i = 0; i < 8; ++i) {
        put2(41 + 2 * i, y0, 42 + 2 * i, y1);
        if (i < 7) { y0 = cmul(y0, w16c); y1 = cmul(y1, w16c); }
      }
    }
    FENCE();
#pragma unroll
    for (int i = 0; i < 2; ++i) {
      const bool last = wtau[i] == 10;
      *(LAS u32x4*)wi1[i] = u32x4{wg[i].x, wu[i].x, wg[i].y, wu[i].y};
      *(LAS u32x4*)(wi1[i] + 16) = u32x4{wg[i].z, wu[i].z, last ? 0x00003F80u : wg[i].w, last ? 0u : wu[i].w};
      *(LAS u32x4*)(wi2 + i * 256) = dv[i];
    }
    vm_wait<0>();
    SYNC_LDS();
    flag_st<E_P0>(c, c.sflag);
    STAMP(1);
    {
      LAS char* ri1 = lds + L_IMG1 + 3 * w * 1056 + lane * 8;
      LAS char* ri2 = lds + L_IMG2 + 2 * w * 1056 + lane * 8;
      const u32 sgu = OFF_WGU + ((cbR * 11 + 3 * w) * 16 + sR) * 1024, sgu2 = w == 3 ? 0x7f000000u : sgu + 32768;
      const u32 swd = b < 176 ? OFF_WD + ((dq * 8 + 2 * w) * KS2 + s2) * 1024 : 0x7f000000u;
      u32x4 o[5];
#pragma unroll
      for (int i = 0; i < 3; ++i) o[i] = tr_frag(ri1 + i * 1056);
#pragma unroll
      for (int i = 0; i < 2; ++i) o[3 + i] = tr_frag(ri2 + i * 1056);
      PIN();
      st16(c.rs, lane * 16, sgu, o[0]); st16(c.rs, lane * 16, sgu + 16384, o[1]); st16(c.rs, lane * 16, sgu2, o[2]);
      STAMP(2);
      f32x2 e[8];
      e[1] = wp[0]; e[2] = cmul(e[1], e[1]); e[3] = cmul(e[2], e[1]); e[4] = cmul(e[2], e[2]);
      e[5] = cmul(e[4], e[1]); e[6] = cmul(e[4], e[2]); e[7] = cmul(e[4], e[3]);
      const f32x2 w256 = cmul(wp[2], wp[2]);
      f32x2 ta = {16384.f, 0.f}, tc = wp[2] * 16384.f;
      put2(0, ta, 8, tc);
#pragma unroll
      for (int i = 1; i < 8; ++i) { put2(i, e[i] * 16384.f, 8 + i, cmul(e[i], tc)); }
      ta = w256 * 16384.f; tc = cmul(tc, w256);
      put2(16, ta, 20, tc);
#pragma unroll
      for (int i = 1; i < 4; ++i) put2(16 + i, cmul(ta, e[2 * i]), 20 + i, cmul(tc, e[2 * i]));
#pragma unroll
      for (int i = 0; i < 2; ++i) {
        ta = cmul(ta, w256); tc = cmul(tc, w256);
        put2(24 + 4 * i, ta, 26 + 4 * i, tc);
        put2(25 + 4 * i, cmul(ta, e[4]), 27 + 4 * i, cmul(tc, e[4]));
      }
#pragma unroll
      for (int i = 0; i < 4; ++i) {
        ta = cmul(ta, w256); tc = cmul(tc, w256);
        put2(32 + 2 * i, ta, 33 + 2 * i, tc);
      }
      ta = cmul(ta, w256);
      put2(40, ta, 40, ta);
      vm_wait<0>();
      SYNC_LDS();
      peek<false, E_P0>(c, c.speek);
      flag_st<E_W>(c, c.sflag);
      st16(c.rs, lane * 16, swd, o[3]); st16(c.rs, lane * 16, swd + KS2 * 1024, o[4]);
      st16(c.rs, (tid >> 2) * 16384 + (tid & 3) * 16, OFF_XC + c.cp * 64, all_ones());
    }
    LAS char* ta0 = lds + L_KT + kch * KT_CH + selu((u64)(long long)-khalf, 128 * (q >> 1) + 4 * (q & 1) + 8 * l15, 512 + 64 * q + l15) * 4;
    LAS char* ta1 = lds + L_KT + kch * KT_CH + (q < 2 ? 256 + 64 * q + 4 * l15 : 384 + 64 * (q - 2) + 2 * l15) * 4;
    LAS char* ta1b = ta1 + (q < 2 ? 2 : 32) * 4;
    {
      LAS char* tr = lds + L_TB + 2 * kch * TB_WAVE + l15 * TB_ROW + q * 16;
      LAS char* tr0 = tr + khalf * 32 * TB_ROW;
      f32x4 kk[2];
      sfor<2>([&](auto H) {
        u32x4 fa[4][2], fb[4];
#pragma unroll
        for (int G = 0; G < 4; ++G) {
          fb[G] = lds16(tr + H * TB_WAVE + 41 * TB_ROW + G * 64);
          fa[G][0] = lds16(tr0 + H * TB_WAVE + G * 64);
          fa[G][1] = lds16(tr + H * TB_WAVE + 16 * TB_ROW + G * 64);
        }
        sfor<8>([&](auto N) { mm_f16<(H == 0 && N < 2)>(kk[N & 1], fa[N >> 1][N & 1], fb[N >> 1]); });
      });
      const float ks = ldexpf(2.f * dt, -14 - ce);
      asm volatile("s_nop 7" : "+v"(kk[0]), "+v"(kk[1]));
      kk[0] *= ks; kk[1] *= ks;
      STAMP(40);
      if (khalf) {
#pragma unroll
        for (int j = 0; j < 4; ++j) *(LAS float*)(ta0 + j * 64) = kk[0][j];
      } else {
        *(LAS f32x4*)ta0 = kk[0];
        *(LAS f32x2*)ta1 = kk[1].xy; *(LAS f32x2*)ta1b = kk[1].zw;
      }
    }
    conv_setup(c, 0, cf);
    conv_park(c, cf);
    poll_edge<false, 4, E_P0>(c, c.speek);
    SYNC_LDS();
    STAMP(41);
    conv_load<0, 0>(c, inA); conv_load<0, 1>(c, inB);
    peek<false, E_W>(c, c.speek);
    PIN();
    {
      f32x2 V[11];
#pragma unroll
      for (int i = 0; i < 11; ++i) V[i] = f32x2{*(LAS float*)ci[i], *(LAS float*)(ci[i] + KT_CH)};
      PIN();
#pragma unroll
      for (int ch = 0; ch < 2; ++ch)
#pragma unroll
        for (int s = 0; s < 4; ++s) {
          const u32x4 o = {pk_bf16(V[3 - s][ch], V[4 - s][ch]), pk_bf16(V[5 - s][ch], V[6 - s][ch]), pk_bf16(V[7 - s][ch], V[8 - s][ch]), pk_bf16(V[9 - s][ch], V[10 - s][ch])};
          *(LAS u32x4*)(lds + L_KC + ch * KC_CH + s * KC_COPY + tid * 16) = o;
        }
    }
  }
  STAMP(3);

  G1 g;
  conv_phase<0>(c, cf, g, dsk, inA, inB);

#pragma unroll 1
  for (int layer = 0; layer < 2; ++layer) {
    refresh(c, w0);
    STAMP(10 + layer * 20);
    g.layer = layer;
    asm volatile("" : "+s"(g.lanes), "+s"(g.xa), "+s"(g.res), "+s"(g.sst), "+s"(g.sst2), "+s"(g.hsrc), "+s"(g.hsrc5));
    asm volatile("" : "+s"(g.so), "+s"(g.soh), "+s"(g.eoff), "+s"(g.fA), "+s"(g.fG), "+s"(g.pA), "+s"(g.pG));
    g.ring[0] = c.lds + L_RING + c.lane * 8;
    g.ring[1] = g.ring[0] + 65536;
    g.v16 = c.lane * 16;
    g.vst = c.q * (KS2 * 1024) + c.l15 * 16;
    g.pk = c.lds + L_ST + c.l15 * 16;
    g.pws = c.lds + L_WS + c.w * 192 + c.q * 16;
    g.rd[0] = c.lds + L_RING + c.w * 4096 + c.lane * 16;
    g.rd[1] = g.rd[0] + 65536;
    g.rd[2] = c.lds + L_RING + (c.w ^ 1) * 4096 + c.lane * 16;
    g.rd[3] = g.rd[2] + 65536;
    g.prs = c.lds + L_RES + (2 * (c.w & 1) + (c.w >> 1)) * 512 + (c.w & 1) * 2048 + c.lane * 8;
    g.prs1 = c.lds + L_RES + (2 * (c.w & 1) + (c.w >> 1)) * 512 + ((c.w & 1) ^ 1) * 2048 + c.lane * 8;
    g.exw = c.lds + L_RING + c.w * 2048 + c.lane * 16;
    g.exr = c.lds + L_RING + (c.w ^ 2) * 2048 + c.lane * 16;
    g.sts = c.lds + L_ST + 1024 + c.lane * 16;
    g.stw = c.lds + L_ST + 1024 + (c.lane ^ ((c.w & 1) * 32)) * 16 + c.w * 4;
    g.vo = selu((u64)((long long)g.lanes >> 1), (((c.q ^ c.w) & 1) * 16 + c.l15) * 1024 + (c.q >> 1) * 16, (2 * c.q + (c.lane & 1)) * 8192 + ((c.lane ^ (2 * c.w)) & 2) * 32 + (c.l15 >> 2) * 16);
    g.va = (c.l15 & 3) * 4096 + (c.l15 >> 2) * 16 + (c.q >> 1) * 64 + (c.q & 1) * 8;
    asm volatile("" : "+v"(g.ring[0]), "+v"(g.ring[1]), "+v"(g.vst), "+v"(g.pk), "+v"(g.v16), "+v"(g.pws), "+v"(g.rd[0]), "+v"(g.rd[1]), "+v"(g.rd[2]), "+v"(g.rd[3]));
    asm volatile("" : "+v"(g.prs), "+v"(g.prs1), "+v"(g.exw), "+v"(g.exr), "+v"(g.sts), "+v"(g.stw), "+v"(g.vo), "+v"(g.va));
    u32x4 Bc[4];
    if (layer == 0) {
      st16(c.rs, g.vo, g.so, all_ones()); st16(c.rs, g.vo, g.soh, all_ones());
      vm_wait<19>();
    } else vm_wait<5>();
    {
      u32 mx = 0;
#pragma unroll
      for (int i = 0; i < 4; ++i) mx = umax5(mx, lds16(g.rd[0] + i * 1024));
      g1_verify<0>(c, g, mx);
    }
    BARRIER(); FENCE();
#pragma unroll
    for (int ct = 0; ct < 4; ++ct) Bc[ct] = tr_frag(g.ring[0] + ct * 4096);
    STAMP(11 + layer * 20);
    g1_unit<0>(c, g, Bc, [&]() {
      vm_wait<11>();
      BARRIER(); FENCE();
#pragma unroll
      for (int ct = 0; ct < 4; ++ct) Bc[ct] = tr_frag(g.ring[1] + ct * 4096);
    });
    STAMP(13 + layer * 20);
    f32x4 acc2[4];
    u32x4 Bd[8];
    u32x2 rr[2];
    g1_unit<1>(c, g, Bc, [&]() {
      vm_wait<6>();
      BARRIER(); FENCE();
      g2_res<0>(g, rr);
      g2_first(g, Bd);
    });
    g2_init(rr, acc2);
    STAMP(15 + layer * 20);
    ConvIn nA, nB;
    ConvF c2;
    g2_unit<0>(c, g, p, layer, acc2, Bd, nA);
    g2_res<1>(g, rr);
    vm_wait<13>();
    {
      u32 mx = 0;
#pragma unroll
      for (int ks = 0; ks < 4; ++ks) Bd[2 * ks] = lds16(g2_frag<0>(g, 0, ks));
      PIN();
      g2_init(rr, acc2);
#pragma unroll
      for (int ks = 0; ks < 4; ++ks) mx = umax3(umax3(mx, Bd[2 * ks].x, Bd[2 * ks].y), Bd[2 * ks].z, Bd[2 * ks].w);
      g2_verify<0>(c, g, Bd, mx);
      BARRIER(); FENCE();
#pragma unroll
      for (int ks = 0; ks < 4; ++ks) Bd[2 * ks + 1] = lds16(g2_frag<0>(g, 1, ks));
    }
    g2_unit<1>(c, g, p, layer, acc2, Bd, nA);
    if (layer == 0) {
      if (LD_A == 90) conv_load<1, 0>(c, nA);
      conv_again(c, cf, g.v16, c2);
      conv_pad(c);
      ffn_next(g);
      conv_phase<1>(c, c2, g, dsk, nA, nB);
    }
  }
  if (blockIdx.x == 0 && threadIdx.x == 0) __hip_atomic_store((GAS u32*)(c.ws + OFF_EPOCH), c.epoch, RLX_AGENT);
  asm volatile(".Lmega_tail:");
}

}

extern "C" void kernel_launch(void* const* d_in, const int* in_sizes, int n_in, void* d_out, int out_size, void* d_ws,
                              size_t ws_size, hipStream_t stream) {
  Params p;
  p.x0 = (const u16*)d_in[0];
  p.logA = (const u16*)d_in[1];
  p.Aim = (const u16*)d_in[2];
  p.Cre = (const u16*)d_in[3];
  p.Cim = (const u16*)d_in[4];
  p.logdt = (const u16*)d_in[5];
  p.Dskip = (const u16*)d_in[6];
  p.wg = (const u16*)d_in[8];
  p.wu = (const u16*)d_in[9];
  p.wd = (const u16*)d_in[10];
  p.out = (u16*)d_out;
  p.ws = (char*)d_ws;
  mega_kernel<<<NBLK, 256, 0, stream>>>(p);
}
```

```cpp
#include <hip/hip_runtime.h>
#include <hip/hip_bf16.h>
#include <utility>

typedef unsigned int u32;
typedef unsigned short u16;
typedef unsigned long long u64;
typedef u32 u32x4 __attribute__((ext_vector_type(4)));
typedef u32 u32x2 __attribute__((ext_vector_type(2)));
typedef float f32x4 __attribute__((ext_vector_type(4)));
typedef float f32x2 __attribute__((ext_vector_type(2)));
typedef short s16x4 __attribute__((ext_vector_type(4)));
typedef __bf16 bf16x8 __attribute__((ext_vector_type(8)));
typedef __bf16 bf16x2 __attribute__((ext_vector_type(2)));
typedef _Float16 f16x8 __attribute__((ext_vector_type(8)));
typedef _Float16 f16x2 __attribute__((ext_vector_type(2)));
#define LAS __attribute__((address_space(3)))
#define GAS __attribute__((address_space(1)))
#define RLX_AGENT __ATOMIC_RELAXED, __HIP_MEMORY_SCOPE_AGENT
#define DEV __device__ __forceinline__

#define CODE_PREFETCH 1
#define MAP_ROWS 0
#define H_REFETCH 0
#define PK_B -10
#define PK_A 4
#define FL_G 2
#define PK_G 84
#define CK_B 24
#define SP_H 48
#define SP_D 4
#define FL_A 2
#define PK_D 89
#define CK_D 1
#define SP_X 4
#define LD_A 80
#define LD_B 20
#define WD_LATE 2
#define FL_B 8
#define STAMP_WAVE 0

namespace {

constexpr int SEQ = 2048, DM = 512, NS = 128, DF = 1376;
constexpr int NBLK = 256;
constexpr int KS2 = 44;

constexpr u32 OFF_EPOCH = 0;
constexpr u32 OFF_FLAG = 4096;
constexpr u32 OFF_GFLAG = OFF_FLAG + 8 * 8192;
constexpr u32 OFF_ST1 = OFF_GFLAG + 8192;
constexpr u32 OFF_ST3 = OFF_ST1 + 16384;
constexpr u32 OFF_WGU = OFF_ST3 + 32768;
constexpr u32 OFF_WD = OFF_WGU + 16 * 11 * 16 * 1024;
constexpr u32 OFF_XC = OFF_WD + 32 * KS2 * 1024;
constexpr u32 OFF_XP = OFF_XC + 2 * 2097152;
constexpr u32 OFF_H = OFF_XP + 2 * 2097152;
constexpr u32 H_BYTES = 128 * KS2 * 1024;
constexpr u32 OFF_DBG = OFF_H + 2 * H_BYTES;
constexpr u32 WS_BYTES = OFF_DBG + 256 * 64 * 16;
constexpr int E_P0 = 0, E_W = 7, E_W2 = 1, E_CONV = 1  , E_DOWN = 3  ;

constexpr int KC_COPY = 4160, KC_CH = 4 * KC_COPY;
constexpr int L_KC = 0;
constexpr int L_RING = 2 * KC_CH;
constexpr int L_RES = L_RING + 7 * 16384;
constexpr int L_ST = L_RES + 8192;
constexpr int L_WS = L_ST + 2048;
constexpr int L_CA = L_WS + 768;
constexpr int LDS_BYTES = 163840;
constexpr int L_U = L_RING + 5 * 16384;
constexpr int U_CH = 4608;
constexpr int L_P = L_RING + 6 * 16384;
constexpr int TB_ROW = 272, TB_WAVE = 57 * TB_ROW;
constexpr int L_TB = L_RING;
constexpr int L_IMG1 = L_TB + 4 * TB_WAVE;
constexpr int L_KT = L_IMG1 + 11 * 1056 + 16;
constexpr int KT_CH = 772 * 4;
constexpr int L_IMG2 = L_U + 2 * U_CH;
constexpr int L_XT = L_IMG2 + 8 * 1056;
constexpr int L_DUMMY = L_XT + 32 * 272;
static_assert(L_KT + 2 * KT_CH <= L_U && L_DUMMY + 4128 <= L_RES && L_CA + 4096 <= LDS_BYTES, "lds");

struct Params {
  const u16 *x0, *logA, *Aim, *Cre, *Cim, *logdt, *Dskip, *wg, *wu, *wd;
  u16* out;
  char* ws;
};

DEV float bf_lo(u32 v) { return __builtin_bit_cast(float, v << 16); }
DEV float bf_hi(u32 v) { return __builtin_bit_cast(float, v & 0xffff0000u); }
DEV float bf2f(u16 v) { return __builtin_bit_cast(float, (u32)v << 16); }
DEV u32 pk_bf16(float lo, float hi) {
  f32x2 f = {lo, hi};
  return __builtin_bit_cast(u32, __builtin_convertvector(f, bf16x2));
}
DEV float rnd_bf16(float v) { return bf_lo(pk_bf16(v, 0.f)); }
DEV u32x2 tr_read(LAS char* p) {
  return __builtin_bit_cast(u32x2, __builtin_amdgcn_ds_read_tr16_b64_v4i16((LAS s16x4*)p));
}
DEV u32x4 tr_frag(LAS char* p) {
  u32x2 a = tr_read(p), b = tr_read(p + 512);
  u32x4 r = {a.x, a.y, b.x, b.y};
  return r;
}
DEV u32x4 lds16(LAS char* p) { return *(LAS u32x4*)p; }
template <int CTRL> DEV float dpp_f(float v) {
  return __builtin_bit_cast(float, __builtin_amdgcn_mov_dpp(__builtin_bit_cast(int, v), CTRL, 0xf, 0xf, true));
}
template <int CTRL> DEV u32 dpp_u(u32 v) { return (u32)__builtin_amdgcn_mov_dpp((int)v, CTRL, 0xf, 0xf, true); }
DEV void swap16(u32& a, u32& b) { auto r = __builtin_amdgcn_permlane16_swap(a, b, false, false); a = r[0]; b = r[1]; }
DEV void swap32(u32& a, u32& b) { auto r = __builtin_amdgcn_permlane32_swap(a, b, false, false); a = r[0]; b = r[1]; }
DEV void swap16f(float& a, float& b) { u32 x = __builtin_bit_cast(u32, a), y = __builtin_bit_cast(u32, b); swap16(x, y); a = __builtin_bit_cast(float, x); b = __builtin_bit_cast(float, y); }
DEV void swap32f(float& a, float& b) { u32 x = __builtin_bit_cast(u32, a), y = __builtin_bit_cast(u32, b); swap32(x, y); a = __builtin_bit_cast(float, x); b = __builtin_bit_cast(float, y); }
DEV float wave_sum(float v) {
  v += dpp_f<0xB1>(v);
  v += dpp_f<0x4E>(v);
  v += dpp_f<0x141>(v);
  v += dpp_f<0x140>(v);
  float a = v, b = v;
  swap16f(a, b); v = a + b;
  a = v; b = v;
  swap32f(a, b); v = a + b;
  return v;
}
DEV float wave_max(float v) {
  v = fmaxf(v, dpp_f<0xB1>(v)); v = fmaxf(v, dpp_f<0x4E>(v)); v = fmaxf(v, dpp_f<0x141>(v)); v = fmaxf(v, dpp_f<0x140>(v));
  float a = v, b = v;
  swap16f(a, b); v = fmaxf(a, b);
  a = v; b = v;
  swap32f(a, b); v = fmaxf(a, b);
  return v;
}
DEV float sel(u64 m, float a, float b) { float r; asm("v_cndmask_b32_e64 %0, %1, %2, %3" : "=v"(r) : "v"(a), "v"(b), "s"(m)); return r; }
DEV u32 selu(u64 m, u32 a, u32 b) { u32 r; asm("v_cndmask_b32_e64 %0, %1, %2, %3" : "=v"(r) : "v"(a), "v"(b), "s"(m)); return r; }
constexpr u64 M_B0 = 0xAAAAAAAAAAAAAAAAull, M_B1 = 0xCCCCCCCCCCCCCCCCull, M_B2 = 0xF0F0F0F0F0F0F0F0ull, M_B3 = 0xFF00FF00FF00FF00ull;
DEV float dot2(u32 a, u32 b, float acc) {
  return __builtin_amdgcn_fdot2_f32_bf16(__builtin_bit_cast(bf16x2, a), __builtin_bit_cast(bf16x2, b), acc, false);
}
DEV f32x2 cmul(f32x2 a, f32x2 b) {
  const f32x2 t = a.yy * f32x2{-b.y, b.x};
  return __builtin_elementwise_fma(a.xx, b, t);
}
#define ACLOB "a0","a1","a2","a3","a4","a5","a6","a7","a8","a9","a10","a11","a12","a13","a14","a15","a16","a17","a18","a19","a20","a21","a22","a23","a24","a25","a26","a27","a28","a29","a30","a31","a32","a33","a34","a35","a36","a37","a38","a39","a40","a41","a42","a43","a44","a45","a46","a47","a48","a49","a50","a51","a52","a53","a54","a55","a56","a57","a58","a59","a60","a61","a62","a63","a64","a65","a66","a67","a68","a69","a70","a71","a72","a73","a74","a75","a76","a77","a78","a79","a80","a81","a82","a83","a84","a85","a86","a87","a88","a89","a90","a91","a92","a93","a94","a95","a96","a97","a98","a99","a100","a101","a102","a103","a104","a105","a106","a107","a108","a109","a110","a111","a112","a113","a114","a115","a116","a117","a118","a119","a120","a121","a122","a123","a124","a125","a126","a127","a128","a129","a130","a131","a132","a133","a134","a135","a136","a137","a138","a139","a140","a141","a142","a143","a144","a145","a146","a147","a148","a149","a150","a151","a152","a153","a154","a155","a156","a157","a158","a159","a160","a161","a162","a163","a164","a165","a166","a167","a168","a169","a170","a171","a172","a173","a174","a175","a176","a177","a178","a179","a180","a181","a182","a183","a184","a185","a186","a187","a188","a189","a190","a191","a192","a193","a194","a195","a196","a197","a198","a199","a200","a201","a202","a203","a204","a205","a206","a207","a208","a209","a210","a211","a212","a213","a214","a215","a216","a217","a218","a219","a220","a221","a222","a223","a224","a225","a226","a227","a228","a229","a230","a231","a232","a233","a234","a235","a236","a237","a238","a239","a240","a241","a242","a243","a244","a245","a246","a247","a248","a249","a250","a251","a252","a253","a254","a255"
#define BARRIER() __builtin_amdgcn_s_barrier()
#define FENCE() asm volatile("" ::: "memory")
#define PIN() __builtin_amdgcn_sched_barrier(0)
#define LGKM0() asm volatile("s_waitcnt lgkmcnt(0)" ::: "memory", ACLOB)
#define SYNC_LDS() do { LGKM0(); BARRIER(); FENCE(); } while (0)
template <int N> DEV void vm_wait() { asm volatile("s_waitcnt vmcnt(%0)" ::"i"(N) : "memory", ACLOB); }

template <int N0, int N1> DEV void vm_wait2(int layer) {
  asm volatile("s_cmp_eq_u32 %0, 0\n\ts_waitcnt vmcnt(%1)\n\ts_cbranch_scc1 1f\n\ts_waitcnt vmcnt(%2)\n1:" ::"s"(layer), "i"(N0), "i"(N1) : "memory", "scc");
}
typedef __amdgpu_buffer_rsrc_t rsrc_t;
DEV rsrc_t mk_rsrc(const void* p, u32 bytes) { return __builtin_amdgcn_make_buffer_rsrc((void*)p, 0, bytes, 0x00020000); }
DEV u32x4 ld16(rsrc_t r, u32 voff, u32 soff) { return __builtin_amdgcn_raw_buffer_load_b128(r, voff, soff, 16); }
DEV u32x4 ld16p(rsrc_t r, u32 voff, u32 soff) { return __builtin_amdgcn_raw_buffer_load_b128(r, voff, soff, 0); }
DEV u32x4 all_ones() { u32x4 v = {~0u, ~0u, ~0u, ~0u}; asm volatile("" : "+v"(v)); return v; }
template <int AUX = 16> DEV void st16(rsrc_t r, u32 voff, u32 soff, u32x4 v) {
  __builtin_amdgcn_raw_buffer_store_b128(v, r, voff, soff, AUX);
  asm volatile("s_nop 1" ::"v"(v) : "memory");
}

template <int... Is, class F> DEV void sfor_impl(std::integer_sequence<int, Is...>, F&& f) { (f(std::integral_constant<int, Is>{}), ...); }
template <int N, class F> DEV void sfor(F&& f) { sfor_impl(std::make_integer_sequence<int, N>{}, f); }
template <bool FIRST> DEV void mm_vv(f32x4& acc, const u32x4& a, const u32x4& b) {
  if constexpr (FIRST) asm volatile("v_mfma_f32_16x16x32_bf16 %0, %1, %2, 0" : "=&v"(acc) : "v"(a), "v"(b));
  else asm volatile("v_mfma_f32_16x16x32_bf16 %0, %1, %2, %0" : "+v"(acc) : "v"(a), "v"(b));
}
template <bool FIRST> DEV void mm_f16(f32x4& acc, const u32x4& a, const u32x4& b) {
  if constexpr (FIRST) asm volatile("v_mfma_f32_16x16x32_f16 %0, %1, %2, 0" : "=&v"(acc) : "v"(a), "v"(b));
  else asm volatile("v_mfma_f32_16x16x32_f16 %0, %1, %2, %0" : "+v"(acc) : "v"(a), "v"(b));
}
template <bool FIRST, int A> DEV void mm_av(f32x4& acc, const u32x4& b) {
  if constexpr (FIRST) asm volatile("v_mfma_f32_16x16x32_bf16 %0, a[%c2:%c3], %1, 0" : "=&v"(acc) : "v"(b), "i"(A), "i"(A + 3));
  else asm volatile("v_mfma_f32_16x16x32_bf16 %0, a[%c2:%c3], %1, %0" : "+v"(acc) : "v"(b), "i"(A), "i"(A + 3));
}
template <int A, int OFF> DEV void ld16_a(rsrc_t r, u32 voff, u32 soff) {
  asm volatile("buffer_load_dwordx4 a[%c3:%c4], %0, %1, %2 offen offset:%c5 sc1" ::"v"(voff), "s"(r), "s"(soff), "i"(A), "i"(A + 3), "i"(OFF) : "memory");
}
#define MM_DONE4(a) asm volatile("s_nop 7" : "+v"((a)[0]), "+v"((a)[1]), "+v"((a)[2]), "+v"((a)[3]))

template <int G, int L> DEV void dma1(rsrc_t r, u32 voff, u32 soff, u32 lds) {
  u32 t;
  asm volatile("s_add_u32 m0, %4, %6\n\ts_add_u32 %0, %3, %5\n\tbuffer_load_dwordx4 %1, %2, %0 offen sc1 lds"
               : "=&s"(t) : "v"(voff), "s"(r), "s"(soff), "s"(lds), "i"(G), "i"(L) : "memory", "scc");
}
template <int G, int L, int P> DEV void dmap(rsrc_t r, u32 voff, u32 soff, u32 lds, u32& t) {
  if constexpr (P == 0)
    asm volatile("s_add_u32 m0, %4, %6\n\ts_add_u32 %0, %3, %5\n\tbuffer_load_dwordx4 %1, %2, %0 offen sc1 lds"
                 : "=&s"(t) : "v"(voff), "s"(r), "s"(soff), "s"(lds), "i"(G), "i"(L) : "memory", "scc");
  else
    asm volatile("buffer_load_dwordx4 %0, %1, %2 offen offset:%c3 sc1 lds" ::"v"(voff), "s"(r), "s"(t), "i"(P * 1024) : "memory");
}
template <int G, int L> DEV void dma4(rsrc_t r, u32 voff, u32 soff, u32 lds) {
  u32 t;
  asm volatile("s_add_u32 m0, %4, %6\n\ts_add_u32 %0, %3, %5\n\tbuffer_load_dwordx4 %1, %2, %0 offen sc1 lds\n\t"
               "buffer_load_dwordx4 %1, %2, %0 offen offset:1024 sc1 lds\n\tbuffer_load_dwordx4 %1, %2, %0 offen offset:2048 sc1 lds\n\t"
               "buffer_load_dwordx4 %1, %2, %0 offen offset:3072 sc1 lds"
               : "=&s"(t) : "v"(voff), "s"(r), "s"(soff), "s"(lds), "i"(G), "i"(L) : "memory", "scc");
}

constexpr u32 SPIN_CAP = 1u << 16;
constexpr int SYNC_WAVE = 2;

struct Ctx {
  char* ws;
  rsrc_t rs;
  LAS char* lds;
  u32 lds0, epoch;
  int b, tid, lane, w, q, l15, cp, cb, rb, db, rt;
  mutable u32 junk;
  u32 vt16, sxp, vflag, vpeek, sflag, sflagg, speek, speekg;
};

DEV void flag_setup(Ctx& c) {
  c.vflag = c.tid == 192 ? 0u : 0x7f000000u;
  c.vpeek = c.lane * 64;
  c.vt16 = c.tid * 16;
  c.sxp = OFF_XP + c.cp * 8192;
  c.sflag = OFF_FLAG + (c.b >> 2) * 64 + (c.b & 3) * 4;
  c.sflagg = OFF_FLAG + 8 * 4096 + c.rb * 256 + (c.cb >> 2) * 64 + (c.cb & 3) * 4;
  c.speek = c.w == SYNC_WAVE ? OFF_FLAG : 0x7f000000u;
  c.speekg = c.w == SYNC_WAVE ? OFF_FLAG + 8 * 4096 + (c.rt >> 1) * 256 : 0x7f000000u;
  asm volatile("" : "+v"(c.vflag), "+v"(c.vpeek), "+v"(c.vt16));
  asm volatile("" : "+s"(c.sflag), "+s"(c.sflagg), "+s"(c.speek), "+s"(c.speekg), "+s"(c.sxp));
}
template <int E> DEV void flag_st(const Ctx& c, u32 base) {
  u32 t;
  asm volatile("s_add_u32 %0, %4, %5\n\tbuffer_store_dword %1, %2, %3, %0 offen sc1" : "=&s"(t) : "v"(c.epoch), "v"(c.vflag), "s"(c.rs), "s"(base), "i"(E * 4096) : "memory", "scc");
}
template <bool GRP, int E> DEV void peek(const Ctx& c, u32 base) {
  u32 t;
  if constexpr (GRP)
    asm volatile("s_mov_b64 exec, 15\n\ts_add_u32 %0, %3, %4\n\tbuffer_load_dwordx4 a[252:255], %1, %2, %0 offen sc1\n\ts_mov_b64 exec, -1"
                 : "=&s"(t) : "v"(c.vpeek), "s"(c.rs), "s"(base), "i"(E * 4096) : "memory", "scc");
  else
    asm volatile("s_add_u32 %0, %3, %4\n\tbuffer_load_dwordx4 a[252:255], %1, %2, %0 offen sc1"
                 : "=&s"(t) : "v"(c.vpeek), "s"(c.rs), "s"(base), "i"(E * 4096) : "memory", "scc");
}
template <bool GRP, int N> DEV u64 flags_miss(const Ctx& c) {
  u32 f0, f1, f2, f3;
  asm volatile("s_waitcnt vmcnt(%4)\n\tv_accvgpr_read_b32 %0, a252\n\tv_accvgpr_read_b32 %1, a253\n\tv_accvgpr_read_b32 %2, a254\n\tv_accvgpr_read_b32 %3, a255"
               : "=v"(f0), "=v"(f1), "=v"(f2), "=v"(f3) : "i"(N) : "memory");
  const u32 d = ((f0 ^ c.epoch) | (f1 ^ c.epoch)) | ((f2 ^ c.epoch) | (f3 ^ c.epoch));
  return __builtin_amdgcn_ballot_w64(d != 0u) & (GRP ? 15ull : ~0ull);
}
template <bool GRP, int N, int E, int WV = SYNC_WAVE> DEV void poll_edge(const Ctx& c, u32 base) {
  if (c.w == WV) {
    if (__builtin_expect(flags_miss<GRP, N>(c) != 0ull, 0)) {
#pragma unroll 1
      for (u32 sp = 0; sp < SPIN_CAP; ++sp) {
        peek<GRP, E>(c, base);
        if (flags_miss<GRP, 0>(c) == 0ull) break;
        __builtin_amdgcn_s_sleep(4);
      }
    }
  }
  FENCE();
}

#define STAMP(i) do { } while (0)

DEV void refresh(Ctx& c, int w0) {
  int tid = threadIdx.x, b = blockIdx.x, w = w0;
  asm volatile("" : "+v"(tid)); asm volatile("" : "+s"(b)); asm volatile("" : "+s"(w));
  c.tid = tid; c.b = b; c.w = w; c.lane = tid & 63; c.q = c.lane >> 4; c.l15 = tid & 15;
  const int xcd = b & 7, rr = b >> 3;
  c.cp = xcd * 32 + rr;
  c.cb = (xcd & 3) * 4 + (rr & 3); c.rb = (xcd >> 2) * 8 + (rr >> 2);
  c.db = (xcd & 1) * 4 + (rr & 3); c.rt = (xcd >> 1) * 8 + (rr >> 2);
}

constexpr int AW(int tile, int s) { return (tile * 16 + s) * 4; }
template <int N> DEV void ld_wgu(const Ctx& c) {
  constexpr int tl = N % 3, s = N / 3;
  const u32 v = c.lane * 16 + ((tl == 2 && c.w == 3) ? 0x40000000u : 0u);
  ld16_a<AW(tl, s), (s & 3) * 1024>(c.rs, v, OFF_WGU + (c.cb * 11 + 3 * c.w + tl) * 16384 + (s >> 2) * 4096);
}
constexpr int HB_OFF = 64 * KS2 * 1024;
constexpr int NWDA = 15, VWD = 140;
DEV u32 wd_ptr(const Ctx& c, int other) { return OFF_WD + (c.db * 4 + 2 * (c.w & 1) + ((c.w >> 1) ^ other)) * (KS2 * 1024) + (c.w >> 1) * 4096; }
template <int N> DEV void ld_wd(const Ctx& c, u32 v16, u32 wd0, u32 wd1, u64 lanes) {
  constexpr int OFF = ((N >> 1) & 3) * 1024;
  if constexpr (N < NWDA)
    asm volatile("s_mov_b64 exec, %3\n\tbuffer_load_dwordx4 a[%c5:%c6], %0, %1, %2 offen offset:%c4 sc1\n\ts_mov_b64 exec, -1"
                 :: "v"(v16), "s"(c.rs), "s"((N & 1) ? wd1 : wd0), "s"(lanes), "i"(OFF), "i"(192 + 4 * N), "i"(195 + 4 * N) : "memory");
  else
    asm volatile("s_mov_b64 exec, %3\n\tbuffer_load_dwordx4 v[%c5:%c6], %0, %1, %2 offen offset:%c4 sc1\n\ts_mov_b64 exec, -1"
                 :: "v"(v16), "s"(c.rs), "s"((N & 1) ? wd1 : wd0), "s"(lanes), "i"(OFF), "i"(VWD + 4 * (N - NWDA)), "i"(VWD + 3 + 4 * (N - NWDA)) : "memory");
}
constexpr int WS0 = 9;
template <int V> DEV void mm_wv(f32x4& acc, const u32x4& b) {
  asm volatile("v_mfma_f32_16x16x32_bf16 %0, v[%c2:%c3], %1, %0" : "+v"(acc) : "v"(b), "i"(V), "i"(V + 3));
}

DEV u32 umax3(u32 a, u32 b, u32 d) { return max(max(a, b), d); }
DEV u32 umax5(u32 m, const u32x4& v) { return umax3(umax3(m, v.x, v.y), v.z, v.w); }
struct ConvIn { u32x4 x; u32x4 st[4]; };
template <int LAYER, int HALF> DEV void conv_load(const Ctx& c, ConvIn& in, u32 skip = 0) {
  u32 t;
  if constexpr (LAYER == 0) {
    asm volatile("s_add_u32 %3, %6, %7\n\tbuffer_load_dwordx4 %0, %4, %5, %3 offen sc1\n\t"
                 "s_mov_b32 %3, %8\n\tbuffer_load_dwordx4 %1, %4, %5, %3 offen sc1\n\ts_mov_b32 %3, %9\n\tbuffer_load_dwordx4 %2, %4, %5, %3 offen sc1"
                 : "=&v"(in.x), "=&v"(in.st[0]), "=&v"(in.st[1]), "=&s"(t)
                 : "v"(c.vt16), "s"(c.rs), "s"(c.sxp), "i"(HALF * 4096), "i"(OFF_ST1 + HALF * 8192), "i"(OFF_ST1 + HALF * 8192 + 4096)
                 : "memory", "scc");
  } else {
    constexpr u32 so = OFF_ST3 + HALF * 16384;
    asm volatile("s_cmp_lg_u32 %9, 0\n\ts_cbranch_scc1 1f\n\t"
                 "s_mov_b32 %5, %11\n\tbuffer_load_dwordx4 %1, %6, %7, %5 offen sc1\n\ts_mov_b32 %5, %12\n\tbuffer_load_dwordx4 %2, %6, %7, %5 offen sc1\n\t"
                 "s_mov_b32 %5, %13\n\tbuffer_load_dwordx4 %3, %6, %7, %5 offen sc1\n\ts_mov_b32 %5, %14\n\tbuffer_load_dwordx4 %4, %6, %7, %5 offen sc1\n\t"
                 "s_add_u32 %5, %8, %10\n\tbuffer_load_dwordx4 %0, %6, %7, %5 offen sc1\n1:"
                 : "=&v"(in.x), "=&v"(in.st[0]), "=&v"(in.st[1]), "=&v"(in.st[2]), "=&v"(in.st[3]), "=&s"(t)
                 : "v"(c.vt16), "s"(c.rs), "s"(c.sxp), "s"(skip), "i"(2097152 + HALF * 4096), "i"(so), "i"(so + 4096), "i"(so + 8192), "i"(so + 12288)
                 : "memory", "scc");
  }
}
template <int LAYER, int N> DEV void conv_wait(ConvIn& in) {
  if constexpr (LAYER == 0) asm volatile("s_waitcnt vmcnt(%3)" : "+v"(in.x), "+v"(in.st[0]), "+v"(in.st[1]) : "i"(N) : "memory");
  else asm volatile("s_waitcnt vmcnt(%5)" : "+v"(in.x), "+v"(in.st[0]), "+v"(in.st[1]), "+v"(in.st[2]), "+v"(in.st[3]) : "i"(N) : "memory");
}
DEV u32 conv_pending(const ConvIn& in) {
  u32 t = 0;
#pragma unroll
  for (int i = 0; i < 4; ++i) t |= (in.st[i].x ^ 8u) | (in.st[i].z ^ 8u);
  return (t & 0xffu) | (umax5(0u, in.x) == 0xffffffffu ? 1u : 0u);
}
template <int HALF> DEV void conv_accept(const Ctx& c, ConvIn& in, f32x2 (&st)[4]) {
  u32 pend = conv_pending(in);
#pragma unroll
  for (int i = 0; i < 4; ++i)
    st[i] = __builtin_elementwise_fma(f32x2{(float)in.st[i].x, (float)in.st[i].z}, f32x2{0x1p-28f, 0x1p-28f}, f32x2{(float)(int)in.st[i].y, (float)(int)in.st[i].w} * 16.f);
  if (__builtin_expect(__builtin_amdgcn_ballot_w64(pend != 0u) != 0ull, 0)) {
#pragma unroll 1
    for (u32 sp = 0; sp < SPIN_CAP && __builtin_amdgcn_ballot_w64((pend & 15u) != 0u); ++sp) {
      __builtin_amdgcn_s_sleep(2);
      conv_load<1, HALF>(c, in);
      conv_wait<1, 0>(in);
      pend = conv_pending(in);
    }
#pragma unroll
    for (int i = 0; i < 4; ++i)
      st[i] = __builtin_elementwise_fma(f32x2{(float)in.st[i].x, (float)in.st[i].z}, f32x2{0x1p-28f, 0x1p-28f}, f32x2{(float)(int)in.st[i].y, (float)(int)in.st[i].w} * 16.f);
    if (__builtin_amdgcn_ballot_w64((pend & 0xf0u) != 0u)) {
#pragma unroll
      for (int i = 0; i < 4; ++i) st[i] = f32x2{0.f, 0.f};
      u32 budget = SPIN_CAP;
#pragma unroll 1
      for (int pr = 0; pr < 256;) {
        FENCE();
        const u32x4 v = ld16(c.rs, c.vt16, OFF_XP + 2097152 + pr * 8192 + HALF * 4096);
        if (__builtin_amdgcn_ballot_w64(umax5(0u, v) == 0xffffffffu) && budget) { --budget; continue; }
#pragma unroll
        for (int i = 0; i < 4; ++i) { const float a = bf_lo(v[i]), e = bf_hi(v[i]); st[i] += f32x2{a + e, a * a + e * e}; }
        ++pr;
      }
    }
  }
}
template <int LAYER, int HALF> DEV void conv_norm(const Ctx& c, LAS char* pu, ConvIn& in, u32 dsk, f32x4 (&base)[2]) {
  f32x2 st[4];
  if (LAYER == 0) {
#pragma unroll
    for (int i = 0; i < 2; ++i) { const f32x4 v = __builtin_bit_cast(f32x4, in.st[i]); st[2 * i] = v.xy; st[2 * i + 1] = v.zw; }
  } else {
    conv_accept<HALF>(c, in, st);
  }
  const f32x2 dd = {bf_lo(dsk), bf_hi(dsk)};
  f32x2 u[4], x[4];
#pragma unroll
  for (int i = 0; i < 4; ++i) {
    const f32x2 me = __builtin_elementwise_fma(st[i], f32x2{-1.f / DM, 1.f / DM}, f32x2{0.f, 1e-5f});
    const float r = __builtin_amdgcn_rsqf(fmaxf(__builtin_fmaf(-me.x, me.x, me.y), 1e-5f));
    x[i] = f32x2{bf_lo(in.x[i]), bf_hi(in.x[i])};
    u[i] = (x[i] + me.x) * r;
  }
  *(LAS u32x2*)pu = u32x2{pk_bf16(u[0].x, u[1].x), pk_bf16(u[2].x, u[3].x)};
  *(LAS u32x2*)(pu + U_CH) = u32x2{pk_bf16(u[0].y, u[1].y), pk_bf16(u[2].y, u[3].y)};
#pragma unroll
  for (int i = 0; i < 4; ++i) { const f32x2 bs = __builtin_elementwise_fma(u[i], dd, x[i]); base[0][i] = bs.x; base[1][i] = bs.y; }
  asm volatile("" : "+v"(base[0]), "+v"(base[1]));
}
template <int H> DEV void lds8h(u32x4& d, LAS char* p) {
  const u32x2 r = *(LAS u32x2*)(p + 8 * H);
  if constexpr (H == 0) { d.x = r.x; d.y = r.y; } else { d.z = r.x; d.w = r.y; }
}
DEV void ldsA(u32x4& d, LAS char* p) { lds8h<0>(d, p); PIN(); lds8h<1>(d, p); PIN(); }
constexpr int tri_j(int m) { return m < 4 ? 0 : (m < 7 ? 1 : (m < 9 ? 2 : 3)); }
constexpr int tri_i(int m) { return m < 4 ? m : (m < 7 ? m - 3 : (m < 9 ? m - 5 : 3)); }
template <int B0, bool ZERO, class F>
DEV void conv_tri(LAS char* pa, LAS char* pb, u32x4 (&Ac)[4], f32x4 (&acc)[4], F&& side) {
  u32x4 Bc[4];
#pragma unroll
  for (int j = 0; j < 4; ++j) Bc[j] = lds16(pb + (B0 + j) * 512);
  PIN();
  sfor<4>([&](auto IT) {
    constexpr int it = IT;
    u32x4 An[4], Bn[4];
    sfor<10>([&](auto M) {
      constexpr int m = M, i = tri_i(m), j = tri_j(m);
      if constexpr (it < 3) {
        if constexpr (m < 8) lds8h<m & 1>(An[m >> 1], pa - 64 * (it + 1) + (7 - (m >> 1)) * 512);
        if constexpr (m == 0 || m == 5) { PIN(); Bn[m / 5] = lds16(pb - 64 * (it + 1) + (B0 + m / 5) * 512); }
        if constexpr (m >= 8) Bn[m - 6] = lds16(pb - 64 * (it + 1) + (B0 + m - 6) * 512);
      }
      side(std::integral_constant<int, it * 10 + m>{});
      mm_vv<ZERO && it == 0 && j == 0>(acc[i], Ac[i - j], Bc[j]);
      PIN();
    });
    if constexpr (it < 3) {
#pragma unroll
      for (int j = 0; j < 4; ++j) { Ac[j] = An[j]; Bc[j] = Bn[j]; }
    }
  });
}
template <int R> DEV void cross_first(LAS char* pa, LAS char* pb, u32x4 (&Ac)[7], u32x4 (&Bc)[4]) {
  if constexpr (R == 0) Bc[0] = lds16(pb);
  else if constexpr (R < 9) lds8h<(R - 1) & 1>(Ac[3 + (R - 1) / 2], pa + (3 - (R - 1) / 2) * 512);
  else if constexpr ((R - 9) % 3 == 0) Bc[1 + (R - 9) / 3] = lds16(pb + (1 + (R - 9) / 3) * 512);
  else lds8h<(R - 9) % 3 - 1>(Ac[2 - (R - 9) / 3], pa + (4 + (R - 9) / 3) * 512);
}
template <class F>
DEV void conv_cross(LAS char* pa, LAS char* pb, f32x4 (&acc)[4], u32x4 (&Ac)[7], u32x4 (&Bc)[4], u32x4 (&At)[4], F&& side) {
  sfor<4>([&](auto IT) {
    constexpr int it = IT;
    u32x4 An[7], Bn[4];
    sfor<16>([&](auto M) {
      constexpr int m = M, j = m >> 2, i = m & 3;
      if constexpr (it < 3) {
        if constexpr (m < 14) lds8h<m & 1>(An[6 - (m >> 1)], pa - 64 * (it + 1) + (m >> 1) * 512);
        if constexpr (m == 0 || m == 8) { PIN(); Bn[m / 8] = lds16(pb - 64 * (it + 1) + (m / 8) * 512); }
        if constexpr (m >= 14) Bn[m - 12] = lds16(pb - 64 * (it + 1) + (m - 12) * 512);
      } else if constexpr (m < 8) {
        lds8h<m & 1>(At[m >> 1], pa + (7 - (m >> 1)) * 512);
      }
      side(std::integral_constant<int, it * 16 + m>{});
      mm_vv<it == 0 && j == 0>(acc[i], Ac[3 + i - j], Bc[j]);
      PIN();
    });
    if constexpr (it < 3) {
#pragma unroll
      for (int k = 0; k < 7; ++k) Ac[k] = An[k];
#pragma unroll
      for (int k = 0; k < 4; ++k) Bc[k] = Bn[k];
    }
  });
}
struct ConvF { LAS char *pa, *pb, *pu, *pw, *pr; u32 vo, so, xa, ldsw, v16; f32x4 a[2], b[2]; };
DEV u32 xa_ptr(const Ctx& c, int layer) { return OFF_XC + layer * 2097152 + (c.rb * 4 + c.w) * 16384; }
DEV void conv_setup(const Ctx& c, int layer, ConvF& r) {
  const int qq = c.tid & 3, ii = (c.tid >> 2) & 15, kch = c.w >> 1, khalf = c.w & 1;
  r.pa = c.lds + L_KC + kch * KC_CH + (c.l15 & 3) * KC_COPY + 2 * (2044 - 16 * (8 * khalf + 1) - (c.l15 & 12) + 8 * c.q) - 7 * 512 - 192;
  r.pb = c.lds + L_U + kch * U_CH + 2 * (256 + 16 * (c.l15 - 8 * khalf - 1) + 8 * c.q) - 192;
  r.pu = c.lds + L_U + 512 + c.tid * 8;
  r.pw = c.lds + L_P + c.w * 4096 + (c.q * 16 + (c.l15 ^ (c.q * 4))) * 16;
  r.pr = c.lds + L_P + c.w * 1024 + (qq * 16 + (ii ^ (qq * 4))) * 16;
  r.vo = (c.tid >> 2) * 16384 + (c.tid & 1) * 32 + (c.tid & 2) * 8;
  r.so = OFF_XC + layer * 2097152 + c.cp * 64;
  r.xa = xa_ptr(c, layer); r.ldsw = c.lds0 + L_RING + c.w * 4096; r.v16 = c.lane * 16;
  asm volatile("" : "+v"(r.pa), "+v"(r.pb), "+v"(r.pu), "+v"(r.pw), "+v"(r.pr), "+v"(r.vo), "+v"(r.v16));
  asm volatile("" : "+s"(r.so), "+s"(r.xa), "+s"(r.ldsw));
}
DEV void conv_park(const Ctx& c, const ConvF& r) {
  *(LAS u32x4*)(c.lds + L_CA + c.vt16) = u32x4{(u32)(size_t)r.pa | ((u32)(size_t)r.pb - L_U) << 16, (u32)(size_t)r.pw, (u32)(size_t)r.pr, r.vo};
}
DEV void conv_again(const Ctx& c, const ConvF& o, u32 v16, ConvF& r) {
  const u32x4 a = *(LAS u32x4*)(c.lds + L_CA + c.vt16);
  r.pa = (LAS char*)(size_t)(a.x & 0xffffu); r.pb = (LAS char*)(size_t)((a.x >> 16) + L_U); r.pw = (LAS char*)(size_t)a.y; r.pr = (LAS char*)(size_t)a.z; r.vo = a.w;
  r.pu = c.lds + L_U + 512 + (c.vt16 >> 1);
  r.so = o.so + 2097152; r.xa = o.xa + 2097152; r.ldsw = o.ldsw; r.v16 = v16;
  asm volatile("" : "+v"(r.pa), "+v"(r.pb), "+v"(r.pu), "+v"(r.pw), "+v"(r.pr), "+v"(r.vo));
}
DEV void conv_pad(const Ctx& c) {
  u32x2 z = {0u, 0u};
  asm volatile("" : "+v"(z));
  LAS char* d = c.lds + L_U + (c.vpeek >> 3);
  *(LAS u32x2*)d = z; *(LAS u32x2*)(d + U_CH) = z;
}
DEV void conv_finish2(ConvF& r) {
#pragma unroll
  for (int ch = 0; ch < 2; ++ch) { r.a[ch] = *(LAS f32x4*)(r.pr + ch * 8192); r.b[ch] = *(LAS f32x4*)(r.pr + ch * 8192 + 4096); }
}
DEV void conv_finish3(const Ctx& c, int half, const ConvF& r, const f32x4 (&base)[2]) {
  u32x2 o[2];
#pragma unroll
  for (int ch = 0; ch < 2; ++ch) {
    const f32x4 t = (r.a[ch] + base[ch]) + r.b[ch];
    o[ch].x = pk_bf16(t.x, t.y); o[ch].y = pk_bf16(t.z, t.w);
  }
  const u32 r0 = dpp_u<0xB1>(selu(M_B0, o[1].x, o[0].x)), r1 = dpp_u<0xB1>(selu(M_B0, o[1].y, o[0].y));
  const u32x4 v = {selu(M_B0, o[0].x, r0), selu(M_B0, o[0].y, r1), selu(M_B0, r0, o[1].x), selu(M_B0, r1, o[1].y)};
  st16(c.rs, r.vo, r.so + half * 1048576, v);
}
DEV u32 res_ptr(const Ctx& c, int layer) { return OFF_XC + layer * 2097152 + (c.rt * 2 + (c.w >> 1)) * 16384 + c.db * 2048 + (c.w & 1) * 1024; }

struct G1 {
  u32 xa;
  u32 res, ldsr;
  LAS char* ring[2];
  u32 ldsw;
  u32 v16;
  u64 lanes;
  u32 wd[6][2];
  u32 vst, sst;
  u32 hsrc, hsrc5;
  LAS char* pk;
  LAS char* pws;
  LAS char *rd[4], *prs, *prs1, *exw, *exr, *sts, *stw;
  u32 vo, so, soh, va, sa, eoff, fA, fG, pA, pG, sst2, so1;
  int layer;
};
DEV void ffn_scalars(const Ctx& c, G1& g) {
  const int dfin = 2 * (c.w & 1) + (c.w >> 1);
  g.xa = xa_ptr(c, 0);
  g.res = res_ptr(c, 0);
  g.ldsw = c.lds0 + L_RING + c.w * 4096;
  g.lanes = ~0ull;
#pragma unroll
  for (int i = 0; i < 6; ++i) {
    g.wd[i][0] = wd_ptr(c, 0) + i * 8192 - (i == 5 ? (c.w >> 1) * 2048 : 0); g.wd[i][1] = wd_ptr(c, 1) + i * 8192 - (i == 5 ? (c.w >> 1) * 2048 : 0);
    asm volatile("" : "+s"(g.wd[i][0]), "+s"(g.wd[i][1]));
  }
  g.sst = OFF_H + c.rb * 4 * (KS2 * 1024) + (c.cb * 11 + 3 * c.w) * 256;
  g.sst2 = c.w == 3 ? 0x7f000000u : g.sst;
  g.hsrc = OFF_H + (c.rt * 2 + (c.w & 1)) * (KS2 * 1024) + (c.w >> 1) * 4096;
  g.hsrc5 = g.hsrc + 5 * 8192 - (c.w >> 1) * 2048;
  g.ldsr = c.lds0 + L_RES + c.w * 1024;
  g.so = OFF_XP + 2097152 + (c.db * 32 + dfin * 8) * 8192 + c.rt * 128;
  g.soh = g.so + 4096;
  g.so1 = c.rt * 32768 + c.db * 128 + dfin * 32;
  g.sa = c.w == 0 ? OFF_ST3 + c.rt * 128 : 0x7f000000u;
  g.eoff = c.sflag; g.fA = c.sflag; g.fG = c.sflagg; g.pA = c.speek; g.pG = c.speekg;
  asm volatile("" : "+s"(g.ldsw), "+s"(g.ldsr), "+s"(g.so1), "+s"(g.sa));
}
DEV void ffn_next(G1& g) {
  g.xa += 2097152; g.res += 2097152; g.lanes = 1ull;
  g.sst += H_BYTES; g.sst2 += H_BYTES; g.hsrc += H_BYTES; g.hsrc5 += H_BYTES;
  g.so = g.so1; g.soh = g.so1 + 1048576; g.eoff = 0x7f000000u;
  g.fA += 16384; g.fG += 8192; g.pA += 16384; g.pG += 8192;
}
template <int S0, int S1, class F> DEV void wsum_part(LAS char* pws, F&& side) {
  f32x4 ws[3];
  u32x4 ones = {0x3F803F80u, 0x3F803F80u, 0x3F803F80u, 0x3F803F80u};
  if constexpr (S0 > 0) {
#pragma unroll
    for (int i = 0; i < 3; ++i) ws[i] = *(LAS f32x4*)(pws + i * 64);
  }
  asm volatile("s_nop 1" : "+v"(ones));
  sfor<3 * (S1 - S0)>([&](auto N) { mm_av<(S0 == 0 && N < 3), AW(N % 3, S0 + N / 3)>(ws[N % 3], ones); side(N); });
  asm volatile("s_nop 7" : "+v"(ws[0]), "+v"(ws[1]), "+v"(ws[2]));
#pragma unroll
  for (int i = 0; i < 3; ++i) *(LAS f32x4*)(pws + i * 64) = ws[i];
}
template <int LAYER> DEV void conv_phase(Ctx& c, ConvF& cf, G1& g, u32 dsk, ConvIn& inA, ConvIn& inB) {
  LAS char* const pa = cf.pa + 192;
  LAS char* const pb = cf.pb + 192;
  f32x4 baseA[2], baseB[2];
  u32x4 Ac[4];
  if constexpr (LAYER == 1) {
#pragma unroll
    for (int j = 0; j < 4; ++j) ldsA(Ac[j], pa + (7 - j) * 512);
  }
  conv_wait<LAYER, LAYER == 0 ? 4 : (LD_A < 90 ? 2 : 0)>(inA);
  STAMP(4 + LAYER * 20);
  conv_norm<LAYER, 0>(c, cf.pu, inA, dsk, baseA);
  if constexpr (LAYER == 0) poll_edge<false, 0, E_W>(c, c.speek);
  SYNC_LDS();
  if constexpr (LAYER == 0) {
    flag_st<E_W2>(c, c.sflag);
#pragma unroll
    for (int j = 0; j < 4; ++j) ldsA(Ac[j], pa + (7 - j) * 512);
    sfor<2>([&](auto N) { ld_wgu<N>(c); });
  }
  STAMP(5 + LAYER * 20);
  f32x4 accA[4], acc[4];
  u32x4 Ax[7], Bx[4];
  conv_tri<0, true>(pa, pb, Ac, accA, [&](auto K) {
    if constexpr (LAYER == 0 && K % 4 == 1) ld_wgu<2 + K / 4>(c);
    if constexpr (K >= 30 && K < 39) { cross_first<2 * (K - 30)>(pa, pb, Ax, Bx); PIN(); cross_first<2 * (K - 30) + 1>(pa, pb, Ax, Bx); }
  });
  STAMP(6 + LAYER * 20);
  conv_cross(pa, pb, acc, Ax, Bx, Ac, [&](auto K) {
    if constexpr (K == 3) asm volatile("s_nop 3" : "+v"(accA[0]), "+v"(accA[1]), "+v"(accA[2]), "+v"(accA[3]));
    if constexpr (K < 3) *(LAS f32x4*)(cf.pw + K * 1024) = accA[K];
    if constexpr (K == 3) *(LAS f32x4*)(cf.pw + K * 1024) = accA[K];
    if constexpr (K == 9) { SYNC_LDS(); conv_finish2(cf); }
    if constexpr (K == 14) conv_finish3(c, 0, cf, baseA);
    if constexpr (LAYER == 0) {
      if constexpr (K % 4 == 0) ld_wgu<12 + K / 4>(c);
      if constexpr (K == 30) peek<false, E_W2>(c, c.speek);
    } else {
      if constexpr (K == LD_B) conv_load<1, 1>(c, inB);
    }
  });
  STAMP(7 + LAYER * 20);
  if constexpr (LAYER == 0) sfor<4>([&](auto N) { ld_wgu<28 + N>(c); });
  conv_wait<LAYER, LAYER == 0 ? 30 : 0>(inB);
  conv_norm<LAYER, 1>(c, cf.pu + 2048, inB, dsk, baseB);
  f32x4 ws[3];
  u32x4 ones = {0x3F803F80u, 0x3F803F80u, 0x3F803F80u, 0x3F803F80u};
  if constexpr (LAYER == 0) {
    vm_wait<20>();
    asm volatile("s_nop 1" : "+v"(ones));
    sfor<6>([&](auto N) { mm_av<(N < 3), AW(N % 3, N / 3)>(ws[N % 3], ones); });
    poll_edge<false, 12, E_W2>(c, c.speek);
  }
  SYNC_LDS();
  STAMP(8 + LAYER * 20);
  u32 td;
  conv_tri<4, false>(pa, pb, Ac, acc, [&](auto K) {
    if constexpr (LAYER == 0 && K % 8 == 2) ld_wgu<32 + K / 8>(c);
    if constexpr (K >= SP_X && (K - SP_X) % 4 == 0 && (K - SP_X) / 4 < 8)
      dmap<((K - SP_X) / 16) * 4096, ((K - SP_X) / 16) * 16384, ((K - SP_X) / 4) & 3>(c.rs, cf.v16, cf.xa, cf.ldsw, td);
  });
  MM_DONE4(acc);
#pragma unroll
  for (int i = 0; i < 4; ++i) *(LAS f32x4*)(cf.pw + i * 1024) = acc[i];
  if constexpr (LAYER == 0) {
    vm_wait<17>();
    sfor<15>([&](auto N) { mm_av<false, AW(N % 3, 2 + N / 3)>(ws[N % 3], ones); if constexpr (N % 5 == 0) ld_wgu<37 + N / 5>(c); });
  }
  SYNC_LDS();
  conv_finish2(cf);
  if constexpr (LAYER == 0) { PIN(); sfor<3 * WS0 - 21>([&](auto N) { mm_av<false, AW(N % 3, 7 + N / 3)>(ws[N % 3], ones); }); PIN(); }
  conv_finish3(c, 1, cf, baseB);
  STAMP(9 + LAYER * 20);
  if constexpr (LAYER == 0) {
    st16(c.rs, cf.vo, cf.so + 2097152, all_ones());
    asm volatile("" : "+v"(ws[0]), "+v"(ws[1]), "+v"(ws[2]));
#pragma unroll
    for (int i = 0; i < 3; ++i) *(LAS f32x4*)(c.lds + L_WS + c.w * 192 + c.q * 16 + i * 64) = ws[i];
    ffn_scalars(c, g);
    asm volatile("" : "+s"(g.xa), "+s"(g.res), "+s"(g.sst), "+s"(g.sst2), "+s"(g.hsrc), "+s"(g.hsrc5), "+s"(g.so), "+s"(g.soh));
    {
      const u32 vx2 = (c.lane >> 3) * 8192 + (c.lane & 7) * 16;
      st16(c.rs, vx2, g.so, all_ones()); st16(c.rs, vx2, g.soh, all_ones());
    }
    sfor<8>([&](auto N) { ld_wgu<40 + N>(c); });
  }
}

DEV void h_refetch(const Ctx& c, u32 vst, u32 sst) {
}
template <int K> DEV void g1_verify(const Ctx& c, const G1& g, u32 mx) {
  if (__builtin_expect(__builtin_amdgcn_ballot_w64(mx == 0xffffffffu) != 0ull, 0)) {
#pragma unroll 1
    for (u32 sp = 0; sp < SPIN_CAP; ++sp) {
      dma4<K * 4096, K * 16384>(c.rs, g.v16, g.xa, g.ldsw);
      vm_wait<0>();
      u32 m = 0;
#pragma unroll
      for (int i = 0; i < 4; ++i) m = umax5(m, lds16(g.rd[0] + K * 16384 + i * 1024));
      if (!__builtin_amdgcn_ballot_w64(m == 0xffffffffu)) break;
      __builtin_amdgcn_s_sleep(2);
    }
  }
  FENCE();
}
template <int N> DEV void ld_wd(const Ctx& c, const G1& g) { ld_wd<N>(c, g.v16, g.wd[N >> 3][0], g.wd[N >> 3][1], g.lanes); }
template <int HALF, bool STATS, int S>
DEV void g1_step(const Ctx& c, const G1& g, f32x4 (&acc)[3][4], u32x4 (&Bc)[4], u32& td, u32& mx) {
  if constexpr (HALF == 0 && (S & 3) == 3 && S < 12) g1_verify<(S + 1) / 4>(c, g, mx);
  if constexpr (S == 3) {
    if constexpr (HALF == 0) vm_wait2<12, 4>(g.layer); else vm_wait<4>();
    BARRIER(); FENCE();
    if constexpr (STATS && HALF == 0) flag_st<E_CONV + 1>(c, g.fA);
    if constexpr (STATS && HALF == 1) flag_st<0>(c, g.fG);
  }
  if constexpr (HALF == 0 && (S == 7 || S == 11)) { BARRIER(); FENCE(); }
  if constexpr (HALF == 1 && S == 7) { vm_wait<9>(); BARRIER(); FENCE(); }
  if constexpr (HALF == 1 && S == 11) { if constexpr (!STATS) poll_edge<true, 6, 0>(c, g.pG); vm_wait<11>(); BARRIER(); FENCE(); }
  u32x4 Bn[4], ck[4];
  sfor<12>([&](auto M) {
    constexpr int m = M, ct = m / 3, k = m % 3;
    if constexpr (HALF == 0 && (S & 3) == 2 && S < 12) {
      if constexpr (m == 0) { if constexpr (S == 2) vm_wait2<16, 5>(g.layer); else vm_wait<S == 10 ? 13 : 5>(); mx = 0; }
      if constexpr (k == 1) ck[ct] = lds16(g.rd[0] + ((S + 2) / 4) * 16384 + ct * 1024);
      if constexpr (m == 5 || m == 8) mx = umax5(mx, ck[ct - 1]);
      if constexpr (m == 11) mx = umax5(mx, ck[2]);
    }
    if constexpr (S < 15 && k < 2) {
      constexpr int slot = (HALF * 4 + (S + 1) / 4) % 7;
      const u32x2 r = tr_read(g.ring[slot >> 2] + (slot & 3) * 16384 + ct * 4096 + ((S + 1) & 3) * 1024 + k * 512);
      if constexpr (k == 0) { Bn[ct].x = r.x; Bn[ct].y = r.y; } else { Bn[ct].z = r.x; Bn[ct].w = r.y; }
    }
    if constexpr (k == 2 && (ct & 1) == 0) {
      constexpr int n = 2 * (S - 1) + (ct >> 1);
      constexpr int nx = (HALF == 0 && S < 2) ? n + 2 : n;
      if constexpr (HALF == 0 ? (S <= 4 && S != 2) : (S >= 1 && S <= 4)) dmap<HALF * 1048576 + 8192 + (nx >> 2) * 4096, ((HALF * 4 + 2 + (nx >> 2)) % 7) * 16384, nx & 3>(c.rs, g.v16, g.xa, g.ldsw, td);
      if constexpr (HALF == 1 && S == 5 && ct == 0) dma1<0, 0>(c.rs, g.v16, g.res, g.ldsr);
      if constexpr (HALF == 1 && S >= 11) dmap<((n - 20) >> 2) * 8192, (((n - 20) >> 2) + 1) * 16384, (n - 20) & 3>(c.rs, g.v16, g.hsrc, g.ldsw, td);
      if constexpr (HALF == 0 && n >= 9) ld_wd<n - 9>(c, g);
      if constexpr (HALF == 1 && n >= 9 && n <= 19) ld_wd<24 + n>(c, g);
    }
    if constexpr (HALF == 0 && S >= 13 && m == 5) {
      u32 t;
      asm volatile("s_add_u32 %0, %4, %5\n\tbuffer_store_dwordx4 %1, %2, %3, %0 offen offset:%c6 sc1\n\ts_nop 1"
                   : "=&s"(t) : "v"(u32x4{~0u, ~0u, ~0u, ~0u}), "v"(g.vst), "s"(c.rs), "s"(S == 15 ? g.sst2 : g.sst), "i"(HB_OFF), "i"((S - 13) * 256) : "memory", "scc");
    }
    if constexpr (HALF == 1 && S == 5 && m == 5) dma1<1048576, 4096>(c.rs, g.v16, g.res, g.ldsr);
    if constexpr (HALF == 0 && m == 5 && S >= 6 && S <= 12) ld_wd<15 + S>(c, g);
    if constexpr (!STATS && HALF == 0 && S == 12 && m == 11) peek<false, E_CONV + 1>(c, g.pA);
    if constexpr (!STATS && HALF == 1 && S == 7 && m == 11) peek<true, 0>(c, g.pG);
    if constexpr (STATS && k == 2) mm_vv<S == 0>(acc[k][ct], Bc[ct], Bc[ct]);
    else if constexpr (!(STATS && k == 0 && S >= 12)) mm_av<S == 0, AW(k, S)>(acc[k][ct], Bc[ct]);
    PIN();
  });
  if constexpr (HALF == 0 && (S & 3) == 2 && S < 12) mx = umax5(mx, ck[3]);
  if constexpr (S < 15) {
#pragma unroll
    for (int ct = 0; ct < 4; ++ct) Bc[ct] = Bn[ct];
    g1_step<HALF, STATS, S + 1>(c, g, acc, Bc, td, mx);
  } else {
    MM_DONE4(acc[2]); asm volatile("" : "+v"(acc[0][3]), "+v"(acc[1][3]), "+v"(acc[0][2]), "+v"(acc[1][2]), "+v"(acc[0][1]), "+v"(acc[1][1]), "+v"(acc[1][0]));
  }
}
template <int HALF, int N> DEV void g1_stage(const Ctx& c, const G1& g, u32& td) {
  if constexpr (HALF == 0) {
    if constexpr (N < 4) dmap<1048576, 4 * 16384, N>(c.rs, g.v16, g.xa, g.ldsw, td);
    else if constexpr (N >= 6 && N < 10) dmap<1048576 + 4096, 5 * 16384, N - 6>(c.rs, g.v16, g.xa, g.ldsw, td);
    else if constexpr (N >= 10 && N < 15) ld_wd<18 + N>(c, g);
  } else {
    if constexpr (N == 0 || N == 3) dmap<2 * 8192 + 2048, 3 * 16384 + 2048, N / 3>(c.rs, g.v16, g.hsrc, g.ldsw, td);
    else if constexpr (N >= 6 && N < 12 && N % 3 != 2) dmap<3 * 8192, 4 * 16384, ((N - 6) / 3) * 2 + N % 3>(c.rs, g.v16, g.hsrc, g.ldsw, td);
  }
  PIN();
}
template <int HALF, int I, int POS, class F>
DEV void g1_tile(const Ctx& c, const G1& g, const f32x4 (&acc)[4], const f32x4 (&k4)[4], const f32x4& ws, F&& next) {
  u32 hv[4], td;
  sfor<2>([&](auto P) {
    constexpr int pi = P, n0 = 6 * POS + 3 * pi;
    f32x2 gg[2], gu[2], ex[2];
    if constexpr (n0 == 15) { next(); PIN(); }
    g1_stage<HALF, n0>(c, g, td);
#pragma unroll
    for (int e = 0; e < 2; ++e) {
      const f32x4 k = k4[2 * pi + e];
      gg[e] = __builtin_elementwise_fma(k.xx, ws.xy, acc[2 * pi + e].xy);
      const f32x2 ar = gg[e] * __builtin_elementwise_fma(gg[e] * gg[e], k.ww, k.zz);
      ex[e] = f32x2{__builtin_amdgcn_exp2f(ar.x), __builtin_amdgcn_exp2f(ar.y)};
    }
    PIN();
    g1_stage<HALF, n0 + 1>(c, g, td);
#pragma unroll
    for (int e = 0; e < 2; ++e) {
      const f32x4 k = k4[2 * pi + e];
      gu[e] = gg[e] * __builtin_elementwise_fma(k.xx, ws.zw, acc[2 * pi + e].zw);
      ex[e] = __builtin_elementwise_fma(ex[e], k.yy, k.yy);
    }
#pragma unroll
    for (int e = 0; e < 2; ++e) ex[e] = f32x2{__builtin_amdgcn_rcpf(ex[e].x), __builtin_amdgcn_rcpf(ex[e].y)};
#pragma unroll
    for (int e = 0; e < 2; ++e) gu[e] *= ex[e];
#pragma unroll
    for (int e = 0; e < 2; ++e) hv[2 * pi + e] = pk_bf16(gu[e].x, gu[e].y);
    PIN();
    g1_stage<HALF, n0 + 2>(c, g, td);
  });
  swap32(hv[0], hv[2]); swap32(hv[1], hv[3]);
  swap16(hv[0], hv[1]); swap16(hv[2], hv[3]);
  u32 t;
  asm volatile("s_add_u32 %0, %4, %5\n\tbuffer_store_dwordx4 %1, %2, %3, %0 offen offset:%c6 sc1\n\ts_nop 1"
               : "=&s"(t) : "v"(u32x4{hv[0], hv[1], hv[2], hv[3]}), "v"(g.vst), "s"(c.rs), "s"(g.sst), "i"(HALF * HB_OFF), "i"(I * 256) : "memory", "scc");
  PIN();
}
template <int HALF, class F>
DEV void g1_unit(const Ctx& c, const G1& g, u32x4 (&Bc)[4], F&& next) {
  f32x4 acc[3][4], k4[4], ws[3];
  u32 td, mx;
  if (c.w == 3) {
    g1_step<HALF, true, 0>(c, g, acc, Bc, td, mx);
    float sm[4], sq[4], s0[4], s1[4];
#pragma unroll
    for (int ct = 0; ct < 4; ++ct) { sm[ct] = acc[1][ct][0]; s0[ct] = sel(M_B0, acc[2][ct][0], acc[2][ct][1]); s1[ct] = sel(M_B0, acc[2][ct][2], acc[2][ct][3]); }
#pragma unroll
    for (int ct = 0; ct < 4; ++ct) sq[ct] = sel(M_B1, s0[ct], s1[ct]);
    swap32f(sm[0], sm[2]); swap32f(sm[1], sm[3]); swap32f(sq[0], sq[2]); swap32f(sq[1], sq[3]);
    swap16f(sm[2], sm[3]); swap16f(sq[0], sq[1]); swap16f(sq[2], sq[3]);
    const float mu = sm[3] * (-1.f / DM);
    const float e2 = sel(M_B3, sel(M_B2, sq[0], sq[1]), sel(M_B2, sq[2], sq[3]));
    const float vr = fmaxf(__builtin_fmaf(-mu, mu, __builtin_fmaf(e2, 1.f / DM, 1e-5f)), 1e-5f);
    const float r = __builtin_amdgcn_rsqf(vr);
    const float c1 = (-2.f * 0.7978845608f * 1.44269504f) * r;
    *(LAS f32x4*)(g.sts - 1024) = f32x4{mu, vr, c1, (c1 * 0.044715f) * (r * r)};
    SYNC_LDS();
    STAMP(12 + 2 * HALF + g.layer * 20);
    LAS char* last = g.ring[0] + (HALF ? 0 : 3 * 16384);
    u32x4 B[2][4];
    sfor<16>([&](auto N) {
      constexpr int s = N >> 2, ct = N & 3;
      if constexpr (s < 3) B[s & 1][ct] = tr_frag(last + ct * 4096 + s * 1024);
      else k4[ct] = *(LAS f32x4*)(g.pk + ct * 256);
      if constexpr (N % 3 == 0) g1_stage<HALF, N / 3>(c, g, td);
      if constexpr (s == 0) mm_av<false, AW(0, 15)>(acc[0][ct], Bc[ct]);
      else mm_av<false, AW(0, 11 + s)>(acc[0][ct], B[(s - 1) & 1][ct]);
      PIN();
    });
    if constexpr (HALF == 0) { if (g.layer == 0) wsum_part<WS0, 16>(g.pws, [](auto) {}); }
#pragma unroll
    for (int i = 0; i < 2; ++i) ws[i] = *(LAS f32x4*)(g.pws + i * 64);
    flag_st<0>(c, 0x7f000000u);
    asm volatile("s_nop 3" : "+v"(acc[0][0]), "+v"(acc[0][1]), "+v"(acc[0][2]), "+v"(acc[0][3]));
  } else {
    g1_step<HALF, false, 0>(c, g, acc, Bc, td, mx);
    if constexpr (HALF == 0) { if (g.layer == 0) wsum_part<WS0, 16>(g.pws, [](auto) {}); poll_edge<false, 9, E_CONV + 1>(c, g.pA); }
#pragma unroll
    for (int i = 0; i < 3; ++i) ws[i] = *(LAS f32x4*)(g.pws + i * 64);
    BARRIER(); FENCE();
    STAMP(12 + 2 * HALF + g.layer * 20);
#pragma unroll
    for (int ct = 0; ct < 4; ++ct) k4[ct] = *(LAS f32x4*)(g.pk + ct * 256);
    g1_tile<HALF, 2, 0>(c, g, acc[2], k4, ws[2], next);
  }
  asm volatile("" : "+v"(k4[0]), "+v"(k4[1]), "+v"(k4[2]), "+v"(k4[3]));
  g1_tile<HALF, 1, 1>(c, g, acc[1], k4, ws[1], next);
  g1_tile<HALF, 0, 2>(c, g, acc[0], k4, ws[0], next);
}

template <int K> DEV LAS char* g2_frag(const G1& g, int other, int ks) { return g.rd[other * 2 + ((K + 1) >> 2)] + ((K + 1) & 3) * 16384 + ks * 1024; }
template <int K> DEV void g2_verify(const Ctx& c, const G1& g, u32x4 (&B)[8], u32 mx) {
  constexpr int NK = K < 5 ? 4 : 2;
  if (__builtin_expect(__builtin_amdgcn_ballot_w64(mx == 0xffffffffu) != 0ull, 0)) {
#pragma unroll 1
    for (u32 sp = 0; sp < SPIN_CAP; ++sp) {
      LGKM0();
      if constexpr (K < 5) dma4<HB_OFF + K * 8192, (K + 1) * 16384>(c.rs, g.v16, g.hsrc, g.ldsw);
      else { dma1<HB_OFF, 6 * 16384>(c.rs, g.v16, g.hsrc5, g.ldsw); dma1<HB_OFF + 1024, 6 * 16384 + 1024>(c.rs, g.v16, g.hsrc5, g.ldsw); }
      vm_wait<0>();
      u32 m = 0;
#pragma unroll
      for (int ks = 0; ks < NK; ++ks) { B[2 * ks] = lds16(g2_frag<K>(g, 0, ks)); m = umax3(umax3(m, B[2 * ks].x, B[2 * ks].y), B[2 * ks].z, B[2 * ks].w); }
      if (!__builtin_amdgcn_ballot_w64(m == 0xffffffffu)) break;
      __builtin_amdgcn_s_sleep(2);
    }
  }
  FENCE();
}
template <int HALF, int C>
DEV void g2_chunk(const Ctx& c, const G1& g, int layer, f32x4 (&acc)[4], u32x4 (&Bc)[8], u32& td, ConvIn& inA) {
  if constexpr (HALF == 0 && C == 2) { vm_wait<8>(); BARRIER(); FENCE(); }
  static_assert(SP_H >= 32 && SP_H + 8 * SP_D >= 48, "ring slots");
  if constexpr (HALF == 0 && C == 3) { vm_wait<(SP_H < 48 ? (47 - SP_H) / SP_D + 1 : 0)>(); BARRIER(); FENCE(); }
  constexpr int NK = C < 5 ? 4 : 2, NKN = C < 4 ? 4 : 2;
  u32x4 Bn[8];
  u32 mx = 0;
  sfor<NK * 4>([&](auto M) {
    constexpr int m = M, ks = (m >> 3) * 2 + ((m >> 1) & 1), ct = (m >> 2) & 1, dt = m & 1, n = (C * 4 + ks) * 2 + dt;
    if constexpr (HALF == 0) {
      if constexpr (C < 5 && m < NKN * 2) Bn[m] = lds16(g2_frag<C + 1>(g, m & 1, m >> 1));
      if constexpr (C == 0 && m % 3 == 0) {
        if constexpr (m < 12) dmap<4 * 8192, 5 * 16384, m / 3>(c.rs, g.v16, g.hsrc, g.ldsw, td);
        else dmap<0, 6 * 16384, m / 3 - 4>(c.rs, g.v16, g.hsrc5, g.ldsw, td);
      }
      constexpr int x = C * 16 + m - SP_H;
      if constexpr (x >= 0 && x % SP_D == 0 && x / SP_D < 12) dmap<HB_OFF + (x / SP_D / 4) * 8192, (x / SP_D / 4 + 1) * 16384, (x / SP_D) & 3>(c.rs, g.v16, g.hsrc, g.ldsw, td);
    } else if constexpr (C < 5) {
      if constexpr (C == 0 && (m == 4 || m >= 11)) {
        constexpr int j = m == 4 ? 0 : m - 10;
        if constexpr (j < 4) dma1<HB_OFF + 4 * 8192 + j * 1024, 5 * 16384 + j * 1024>(c.rs, g.v16, g.hsrc, g.ldsw);
        else dma1<HB_OFF + (j - 4) * 1024, 6 * 16384 + (j - 4) * 1024>(c.rs, g.v16, g.hsrc5, g.ldsw);
      }
      if constexpr (m == 0) vm_wait<C == 0 ? 9 : (C == 1 ? 11 : (C == 2 ? 7 : (C == 3 ? 2 : 0)))>();
      if constexpr (m < NKN) Bn[2 * m] = lds16(g2_frag<C + 1>(g, 0, m));
      if constexpr (m >= 5 && m < 5 + NKN) { mx = umax3(umax3(mx, Bn[2 * (m - 5)].x, Bn[2 * (m - 5)].y), Bn[2 * (m - 5)].z, Bn[2 * (m - 5)].w); }
      if constexpr (m == 9) {
        g2_verify<C + 1>(c, g, Bn, mx);
        BARRIER(); FENCE();
      }
      if constexpr (m >= 9 && m < 9 + NKN) Bn[2 * (m - 9) + 1] = lds16(g2_frag<C + 1>(g, 1, m - 9));
    }
    if constexpr (HALF == 1 && C * 16 + m == LD_A) conv_load<1, 0>(c, inA, layer);
    if constexpr (n < NWDA) mm_av<C == 0 && ks == 0 && dt == 1, 192 + 4 * n>(acc[dt * 2 + ct], Bc[ks * 2 + ct]);
    else mm_wv<VWD + 4 * (n - NWDA)>(acc[dt * 2 + ct], Bc[ks * 2 + ct]);
    PIN();
  });
  if constexpr (C < 5) {
#pragma unroll
    for (int m = 0; m < NKN * 2; ++m) Bc[m] = Bn[m];
    g2_chunk<HALF, C + 1>(c, g, layer, acc, Bc, td, inA);
  }
}
template <int HALF> DEV void g2_res(const G1& g, u32x2 (&r)[2]) {
#pragma unroll
  for (int ct = 0; ct < 2; ++ct) r[ct] = tr_read((ct ? g.prs1 : g.prs) + HALF * 4096);
}
DEV void g2_init(const u32x2 (&r)[2], f32x4 (&acc)[4]) {
#pragma unroll
  for (int ct = 0; ct < 2; ++ct) acc[ct] = f32x4{bf_lo(r[ct].x), bf_hi(r[ct].x), bf_lo(r[ct].y), bf_hi(r[ct].y)};
  asm volatile("s_nop 1" : "+v"(acc[0]), "+v"(acc[1]));
}
DEV void g2_first(const G1& g, u32x4 (&Bc)[8]) {
#pragma unroll
  for (int m = 0; m < 8; ++m) Bc[m] = lds16(g2_frag<0>(g, m & 1, m >> 1));
}
template <int HALF>
DEV void g2_unit(const Ctx& c, const G1& g, const Params& p, int layer, f32x4 (&acc)[4], u32x4 (&Bc)[8], ConvIn& inA) {
  u32 td;
  g2_chunk<HALF, 0>(c, g, layer, acc, Bc, td, inA);
  constexpr int NSP = (87 - SP_H) / SP_D + 1 < 12 ? (87 - SP_H) / SP_D + 1 : 12;
  MM_DONE4(acc);
  STAMP(16 + 2 * HALF + layer * 20);
#pragma unroll
  for (int ct = 0; ct < 2; ++ct) *(LAS f32x4*)(g.exw + HALF * 8192 + ct * 1024) = acc[2 + ct];
  if constexpr (HALF == 0) sfor<12 - NSP>([&](auto X) { dmap<HB_OFF + ((NSP + X) / 4) * 8192, ((NSP + X) / 4 + 1) * 16384, (NSP + X) & 3>(c.rs, g.v16, g.hsrc, g.ldsw, td); });
  SYNC_LDS();
  f32x4 ex[2];
#pragma unroll
  for (int ct = 0; ct < 2; ++ct) ex[ct] = *(LAS f32x4*)(g.exr + HALF * 8192 + ct * 1024);
  if constexpr (HALF == 0) dma4<HB_OFF + 3 * 8192, 4 * 16384>(c.rs, g.v16, g.hsrc, g.ldsw);
  if constexpr (HALF == 1 && LD_A == 89) conv_load<1, 0>(c, inA, layer);
  u32 v[4];
#pragma unroll
  for (int ct = 0; ct < 2; ++ct) {
    const f32x4 o = acc[ct] + ex[ct];
    v[2 * ct] = pk_bf16(o[0], o[1]); v[2 * ct + 1] = pk_bf16(o[2], o[3]);
  }
  if (layer == 0) {
    float st[4];
#pragma unroll
    for (int ct = 0; ct < 2; ++ct) {
      st[2 * ct] = dot2(v[2 * ct], 0x3F803F80u, dot2(v[2 * ct + 1], 0x3F803F80u, 0.f));
      st[2 * ct + 1] = dot2(v[2 * ct], v[2 * ct], dot2(v[2 * ct + 1], v[2 * ct + 1], 0.f));
    }
    swap32f(st[0], st[2]); swap32f(st[1], st[3]);
    float sA = st[0] + st[2], sB = st[1] + st[3];
    swap16f(sA, sB);
    *(LAS float*)g.stw = sA + sB;
    u32 t0 = dpp_u<0xB1>(selu(M_B0, v[1], v[0])), t1 = dpp_u<0xB1>(selu(M_B0, v[3], v[2]));
    const u32 a0 = selu(M_B0, v[0], t0), a1 = selu(M_B0, t0, v[1]), a2 = selu(M_B0, v[2], t1), a3 = selu(M_B0, t1, v[3]);
    t0 = dpp_u<0x4E>(selu(M_B1, a2, a0)); t1 = dpp_u<0x4E>(selu(M_B1, a3, a1));
    st16(c.rs, g.vo, HALF ? g.soh : g.so, u32x4{selu(M_B1, a0, t0), selu(M_B1, a1, t1), selu(M_B1, t0, a2), selu(M_B1, t1, a3)});
    SYNC_LDS();
    const f32x4 s4 = *(LAS f32x4*)g.sts;
    const float y = ((s4.x + s4.y) + (s4.z + s4.w)) * 0.0625f, fy = __builtin_floorf(y);
    const bool fits = fabsf(y) < 0x1p27f;
    const u32x2 sv = {fits ? ((u32)((y - fy) * 0x1p32f) & ~0xffu) | 1u : 0x11u, fits ? (u32)(int)fy : 0u};
    asm volatile("buffer_atomic_add_x2 %0, %1, %2, %3 offen" ::"v"(sv), "v"(g.va), "s"(c.rs), "s"(g.sa + HALF * 16384) : "memory");
  } else {
    swap16(v[0], v[2]); swap16(v[1], v[3]);
    st16<0>(mk_rsrc(p.out, SEQ * DM * 2), g.vo, HALF ? g.soh : g.so, u32x4{v[0], v[1], v[2], v[3]});
  }
  STAMP(17 + 2 * HALF + layer * 20);
}

__global__ void __launch_bounds__(256, 1) __attribute__((amdgpu_num_vgpr(140))) mega_kernel(Params p) {
  asm volatile("" ::: "v255", "a255");
  __shared__ __attribute__((aligned(16))) char smem[LDS_BYTES];
  const int w0 = __builtin_amdgcn_readfirstlane(threadIdx.x >> 6);
  Ctx c;
  c.ws = p.ws; c.lds = (LAS char*)smem; c.lds0 = (u32)(size_t)c.lds;
  c.rs = mk_rsrc(p.ws, WS_BYTES);
  refresh(c, w0);
  STAMP(0);
  u32 dsk;
  ConvIn inA, inB;
  ConvF cf;
  u32 pf_sink;
  {
    u64 pc; u32 len;
    asm volatile("s_getpc_b64 %0\n1:\n\ts_mov_b32 %1, .Lmega_tail-1b" : "=s"(pc), "=s"(len));
    const u32 off = ((c.b >> 3) * 16 + (c.tid & 15)) * 128;
    pf_sink = 0;
    if (c.tid < 16 && off + 4 <= len) pf_sink = *(const GAS u32*)(pc + off);
  }

  {
    const int b = c.b, tid = c.tid, lane = c.lane, w = c.w, q = c.q, l15 = c.l15;
    LAS char* lds = c.lds;
    const int kch = w >> 1, khalf = w & 1;
    const rsrc_t rx = mk_rsrc(p.x0, SEQ * DM * 2), rg = mk_rsrc(p.wg, DM * DF * 2), ru = mk_rsrc(p.wu, DM * DF * 2), rd = mk_rsrc(p.wd, DM * DF * 2);
    const u32x4 r0 = ld16p(rx, lane * 16, (8 * b + 2 * w) * 1024), r1 = ld16p(rx, lane * 16 + 1024, (8 * b + 2 * w) * 1024);
    u32x4 xt[2];
#pragma unroll
    for (int i = 0; i < 2; ++i) xt[i] = ld16p(rx, (tid >> 3) * 1024 + (tid & 7) * 16 + i * 128, (b >> 2) * 32768 + (b & 3) * 256);
    const int dch = 2 * c.cp + kch, pn = dch * NS + 64 * khalf + lane;
    const u16 pA = p.logA[pn], pI = p.Aim[pn], pCr = p.Cre[pn], pCi = p.Cim[pn], pdt = p.logdt[dch];
    const u16 pCr2 = p.Cre[pn ^ 64], pCi2 = p.Cim[pn ^ 64];
    dsk = *(const u32*)(p.Dskip + 2 * c.cp);
    const u32 ep = __hip_atomic_load((GAS u32*)(p.ws + OFF_EPOCH), RLX_AGENT);
    const int cbR = b >> 4, sR = b & 15;
    u32x4 wg[2], wu[2];
    int wrow[2], wtau[2];
#pragma unroll
    for (int i = 0; i < 2; ++i) {
      const int id = tid + 256 * i;
      wrow[i] = (id * 5958) >> 16; wtau[i] = id - wrow[i] * 11;
      const u32 vo = wrow[i] * (DF * 2) + wtau[i] * 16 + (id < 352 ? 0u : 0x40000000u) + sR * (32 * DF * 2) + cbR * 172;
      wg[i] = ld16p(rg, vo, 0); wu[i] = ld16p(ru, vo, 0);
    }
    u32x4 dv[2];
    u32 vdw[2];
    const int s2 = b >> 2, dq = b & 3;
#pragma unroll
    for (int i = 0; i < 2; ++i) {
      const int krow = (tid >> 4) + 16 * i, U = 4 * s2 + (krow >> 3), cbU = (U * 5958) >> 16, fl = 8 * (U - cbU * 11) + (krow & 7);
      vdw[i] = (86 * cbU + fl) * 1024 + (tid & 15) * 16 + dq * 256 + (fl < 86 ? 0u : 0x40000000u);
      asm volatile("" : "+v"(vdw[i]));
    }
    LAS char* wxt = lds + L_XT + (tid >> 3) * 272 + (tid & 7) * 16;
    LAS char* rxt = lds + L_XT + (tid & 7) * (4 * 272) + (tid >> 3) * 4;
    u32 vst = selu(0x0001000100010001ull, selu(0x0006000600060006ull, 0x7f000000u, OFF_ST3 + b * 128 + w * 32 - 16 + l15 * 16 + q * 4),
                   OFF_ST1 + (b >> 7) * 8192 + (w & 1) * 4096 + ((2 * b + (w >> 1)) & 255) * 16 + q * 4);
    u32 vxp = (tid >> 3) * 8192 + (tid & 7) * 16;
    u32 sxp = OFF_XP + (b & 3) * (64 * 8192) + (b >> 2) * 128;
    LAS char* wi1[2] = {lds + L_IMG1 + wtau[0] * 1056 + wrow[0] * 32, lds + selu(__builtin_amdgcn_ballot_w64(tid < 96), L_DUMMY + tid * 16, L_IMG1 + wtau[1] * 1056 + wrow[1] * 32)};
    LAS char* wi2 = lds + L_IMG2 + ((tid & 15) >> 1) * 1056 + (4 * (tid >> 7) + ((tid >> 4) & 3) + ((tid & 64) ? 16 : 0)) * 32 + (tid & 1) * 16;
    asm volatile("" : "+v"(wxt), "+v"(rxt), "+v"(vst), "+v"(vxp), "+v"(wi1[0]), "+v"(wi1[1]), "+v"(wi2));
    asm volatile("" : "+s"(sxp));
    {
      const u32x4 z = {0u, 0u, 0u, 0u};
      *(LAS u32x4*)(lds + L_KC + (l15 >> 1) * KC_COPY + 4096 + (l15 & 1) * 16) = z;
      *(LAS u32x4*)(lds + L_KT - 16 + (l15 & 1) * KT_CH) = z;
    }
    flag_setup(c);
    conv_pad(c);
    LAS char* ci[11];
#pragma unroll
    for (int i = 0; i < 11; i += 2) {
      const int l0 = 2047 - 8 * tid - i, l1 = l0 - 1;
      const u32 pb = pk_bf16((float)(i >= 8 ? max(l0, 0) : l0), (float)(i >= 8 ? max(l1, 0) : l1));
      const int b0 = (int)(pb & 0xffffu) - (134 << 7) + 128, b1 = (int)(pb >> 16) - (134 << 7) + 128;
      ci[i] = lds + L_KT + 4 * max(b0, min(l0, 128));
      asm volatile("" : "+v"(ci[i]));
      if (i < 10) { ci[i + 1] = lds + L_KT + 4 * max(b1, min(l1, 128)); asm volatile("" : "+v"(ci[i + 1])); }
    }
    PIN();
    STAMP(42);
    asm volatile("" :: "v"(pf_sink));
#pragma unroll
    for (int i = 0; i < 2; ++i) *(LAS u32x4*)(wxt + i * 128) = xt[i];
    {
      float s0 = 0.f, q0 = 0.f, s1 = 0.f, q1 = 0.f;
#pragma unroll
      for (int i = 0; i < 4; ++i) {
        s0 = dot2(r0[i], 0x3F803F80u, s0); q0 = dot2(r0[i], r0[i], q0);
        s1 = dot2(r1[i], 0x3F803F80u, s1); q1 = dot2(r1[i], r1[i], q1);
      }
      swap32f(s0, s1); swap32f(q0, q1);
      float sA = s0 + s1, sB = q0 + q1;
      swap16f(sA, sB);
      float sv = sA + sB;
      sv += dpp_f<0xB1>(sv); sv += dpp_f<0x4E>(sv); sv += dpp_f<0x141>(sv); sv += dpp_f<0x140>(sv);
      __builtin_amdgcn_raw_buffer_store_b32(selu(0x0001000100010001ull, 0u, __builtin_bit_cast(u32, sv)), c.rs, vst, 0, 16);
    }
    SYNC_LDS();
    {
      u32x4 o[2];
#pragma unroll
      for (int i = 0; i < 2; ++i) o[i] = u32x4{*(LAS u32*)(rxt + i * 128), *(LAS u32*)(rxt + i * 128 + 272), *(LAS u32*)(rxt + i * 128 + 544), *(LAS u32*)(rxt + i * 128 + 816)};
      PIN();
      st16(c.rs, vxp, sxp, o[0]); st16(c.rs, vxp, sxp + 32 * 8192, o[1]);
    }
    STAMP(43);
    if (WD_LATE == 1) { dv[0] = ld16p(rd, vdw[0], 0); dv[1] = ld16p(rd, vdw[1], 0); }
    c.epoch = ep + 1u;

    const float dt = rnd_bf16(expf(bf2f(pdt)));
    const float a2 = -rnd_bf16(expf(bf2f(pA))) * dt * 1.44269504f;
    const float ai = bf2f(pI) * dt;
    const float i2p_hi = 0.15915494f, i2p_lo = (float)(0.15915494309189535 - (double)0.15915494f);
    const float fhi = ai * i2p_hi, flo = __builtin_fmaf(ai, i2p_hi, -fhi) + ai * i2p_lo;
    f32x2 wp[3];
#pragma unroll
    for (int i = 0; i < 3; ++i) {
      const float ps = i == 0 ? 1.f : (i == 1 ? 8.f : 128.f);
      const float mag = __builtin_amdgcn_exp2f(a2 * ps);
      const float rev = __builtin_fmaf(flo, ps, __builtin_amdgcn_fractf(fhi * ps));
      wp[i] = f32x2{mag * __builtin_amdgcn_cosf(rev), mag * __builtin_amdgcn_sinf(rev)};
    }
    const float cr = bf2f(pCr), cim = bf2f(pCi);
    int ce = 14 - __builtin_amdgcn_frexp_expf(wave_max(fmaxf(fmaxf(fabsf(cr), fabsf(cim)), fmaxf(fabsf(bf2f(pCr2)), fabsf(bf2f(pCi2))))));
    ce = ce > 100 ? 100 : (ce < -100 ? -100 : ce);
    LAS char* tb = lds + L_TB + w * TB_WAVE + lane * 4;
    auto put2 = [&](int ra, f32x2 va, int rb2, f32x2 vb) {
      *(LAS u32*)(tb + ra * TB_ROW) = __builtin_bit_cast(u32, __builtin_convertvector(va, f16x2));
      *(LAS u32*)(tb + rb2 * TB_ROW) = __builtin_bit_cast(u32, __builtin_convertvector(vb, f16x2));
    };
    {
      const f32x2 w8c = {wp[1].x, -wp[1].y}, w16c = cmul(w8c, w8c);
      f32x2 y0 = {ldexpf(cr, ce), -ldexpf(cim, ce)}, y1 = cmul(y0, w8c);
#pragma unroll
      for (int i = 0; i < 8; ++i) {
        put2(41 + 2 * i, y0, 42 + 2 * i, y1);
        if (i < 7) { y0 = cmul(y0, w16c); y1 = cmul(y1, w16c); }
      }
    }
    FENCE();
#pragma unroll
    for (int i = 0; i < 2; ++i) {
      const bool last = wtau[i] == 10;
      *(LAS u32x4*)wi1[i] = u32x4{wg[i].x, wu[i].x, wg[i].y, wu[i].y};
      *(LAS u32x4*)(wi1[i] + 16) = u32x4{wg[i].z, wu[i].z, last ? 0x00003F80u : wg[i].w, last ? 0u : wu[i].w};
    }
    vm_wait<WD_LATE == 1 ? 2 : 0>();
    SYNC_LDS();
    flag_st<E_P0>(c, c.sflag);
    STAMP(1);
    {
      LAS char* ri1 = lds + L_IMG1 + 3 * w * 1056 + lane * 8;
      LAS char* ri2 = lds + L_IMG2 + 2 * w * 1056 + lane * 8;
      const u32 sgu = OFF_WGU + ((cbR * 11 + 3 * w) * 16 + sR) * 1024, sgu2 = w == 3 ? 0x7f000000u : sgu + 32768;
      const u32 swd = b < 176 ? OFF_WD + ((dq * 8 + 2 * w) * KS2 + s2) * 1024 : 0x7f000000u;
      u32x4 o[5];
#pragma unroll
      for (int i = 0; i < 3; ++i) o[i] = tr_frag(ri1 + i * 1056);
      if (WD_LATE == 2) { dv[0] = ld16p(rd, vdw[0], 0); dv[1] = ld16p(rd, vdw[1], 0); }
      PIN();
      st16(c.rs, lane * 16, sgu, o[0]); st16(c.rs, lane * 16, sgu + 16384, o[1]); st16(c.rs, lane * 16, sgu2, o[2]);
      STAMP(2);
      f32x2 e[8];
      e[1] = wp[0]; e[2] = cmul(e[1], e[1]); e[3] = cmul(e[2], e[1]); e[4] = cmul(e[2], e[2]);
      e[5] = cmul(e[4], e[1]); e[6] = cmul(e[4], e[2]); e[7] = cmul(e[4], e[3]);
      const f32x2 w256 = cmul(wp[2], wp[2]);
      f32x2 ta = {16384.f, 0.f}, tc = wp[2] * 16384.f;
      put2(0, ta, 8, tc);
#pragma unroll
      for (int i = 1; i < 8; ++i) { put2(i, e[i] * 16384.f, 8 + i, cmul(e[i], tc)); }
      ta = w256 * 16384.f; tc = cmul(tc, w256);
      put2(16, ta, 20, tc);
#pragma unroll
      for (int i = 1; i < 4; ++i) put2(16 + i, cmul(ta, e[2 * i]), 20 + i, cmul(tc, e[2 * i]));
#pragma unroll
      for (int i = 0; i < 2; ++i) {
        ta = cmul(ta, w256); tc = cmul(tc, w256);
        put2(24 + 4 * i, ta, 26 + 4 * i, tc);
        put2(25 + 4 * i, cmul(ta, e[4]), 27 + 4 * i, cmul(tc, e[4]));
      }
#pragma unroll
      for (int i = 0; i < 4; ++i) {
        ta = cmul(ta, w256); tc = cmul(tc, w256);
        put2(32 + 2 * i, ta, 33 + 2 * i, tc);
      }
      ta = cmul(ta, w256);
      put2(40, ta, 40, ta);
#pragma unroll
      for (int i = 0; i < 2; ++i) *(LAS u32x4*)(wi2 + i * 256) = dv[i];
      vm_wait<0>();
      SYNC_LDS();
#pragma unroll
      for (int i = 0; i < 2; ++i) o[3 + i] = tr_frag(ri2 + i * 1056);
      peek<false, E_P0>(c, c.speek);
      flag_st<E_W>(c, c.sflag);
      st16(c.rs, lane * 16, swd, o[3]); st16(c.rs, lane * 16, swd + KS2 * 1024, o[4]);
      st16(c.rs, (tid >> 2) * 16384 + (tid & 3) * 16, OFF_XC + c.cp * 64, all_ones());
    }
    LAS char* ta0 = lds + L_KT + kch * KT_CH + selu((u64)(long long)-khalf, 128 * (q >> 1) + 4 * (q & 1) + 8 * l15, 512 + 64 * q + l15) * 4;
    LAS char* ta1 = lds + L_KT + kch * KT_CH + (q < 2 ? 256 + 64 * q + 4 * l15 : 384 + 64 * (q - 2) + 2 * l15) * 4;
    LAS char* ta1b = ta1 + (q < 2 ? 2 : 32) * 4;
    {
      LAS char* tr = lds + L_TB + 2 * kch * TB_WAVE + l15 * TB_ROW + q * 16;
      LAS char* tr0 = tr + khalf * 32 * TB_ROW;
      f32x4 kk[2];
      sfor<2>([&](auto H) {
        u32x4 fa[4][2], fb[4];
#pragma unroll
        for (int G = 0; G < 4; ++G) {
          fb[G] = lds16(tr + H * TB_WAVE + 41 * TB_ROW + G * 64);
          fa[G][0] = lds16(tr0 + H * TB_WAVE + G * 64);
          fa[G][1] = lds16(tr + H * TB_WAVE + 16 * TB_ROW + G * 64);
        }
        sfor<8>([&](auto N) { mm_f16<(H == 0 && N < 2)>(kk[N & 1], fa[N >> 1][N & 1], fb[N >> 1]); });
      });
      const float ks = ldexpf(2.f * dt, -14 - ce);
      asm volatile("s_nop 7" : "+v"(kk[0]), "+v"(kk[1]));
      kk[0] *= ks; kk[1] *= ks;
      STAMP(40);
      if (khalf) {
#pragma unroll
        for (int j = 0; j < 4; ++j) *(LAS float*)(ta0 + j * 64) = kk[0][j];
      } else {
        *(LAS f32x4*)ta0 = kk[0];
        *(LAS f32x2*)ta1 = kk[1].xy; *(LAS f32x2*)ta1b = kk[1].zw;
      }
    }
    conv_setup(c, 0, cf);
    conv_park(c, cf);
    poll_edge<false, 4, E_P0>(c, c.speek);
    SYNC_LDS();
    STAMP(41);
    conv_load<0, 0>(c, inA); conv_load<0, 1>(c, inB);
    peek<false, E_W>(c, c.speek);
    PIN();
    {
      f32x2 V[11];
#pragma unroll
      for (int i = 0; i < 11; ++i) V[i] = f32x2{*(LAS float*)ci[i], *(LAS float*)(ci[i] + KT_CH)};
      PIN();
#pragma unroll
      for (int ch = 0; ch < 2; ++ch)
#pragma unroll
        for (int s = 0; s < 4; ++s) {
          const u32x4 o = {pk_bf16(V[3 - s][ch], V[4 - s][ch]), pk_bf16(V[5 - s][ch], V[6 - s][ch]), pk_bf16(V[7 - s][ch], V[8 - s][ch]), pk_bf16(V[9 - s][ch], V[10 - s][ch])};
          *(LAS u32x4*)(lds + L_KC + ch * KC_CH + s * KC_COPY + tid * 16) = o;
        }
    }
  }
  STAMP(3);

  G1 g;
  conv_phase<0>(c, cf, g, dsk, inA, inB);

#pragma unroll 1
  for (int layer = 0; layer < 2; ++layer) {
    refresh(c, w0);
    STAMP(10 + layer * 20);
    g.layer = layer;
    asm volatile("" : "+s"(g.lanes), "+s"(g.xa), "+s"(g.res), "+s"(g.sst), "+s"(g.sst2), "+s"(g.hsrc), "+s"(g.hsrc5));
    asm volatile("" : "+s"(g.so), "+s"(g.soh), "+s"(g.eoff), "+s"(g.fA), "+s"(g.fG), "+s"(g.pA), "+s"(g.pG));
    g.ring[0] = c.lds + L_RING + c.lane * 8;
    g.ring[1] = g.ring[0] + 65536;
    g.v16 = c.lane * 16;
    g.vst = c.q * (KS2 * 1024) + c.l15 * 16;
    g.pk = c.lds + L_ST + c.l15 * 16;
    g.pws = c.lds + L_WS + c.w * 192 + c.q * 16;
    g.rd[0] = c.lds + L_RING + c.w * 4096 + c.lane * 16;
    g.rd[1] = g.rd[0] + 65536;
    g.rd[2] = c.lds + L_RING + (c.w ^ 1) * 4096 + c.lane * 16;
    g.rd[3] = g.rd[2] + 65536;
    g.prs = c.lds + L_RES + (2 * (c.w & 1) + (c.w >> 1)) * 512 + (c.w & 1) * 2048 + c.lane * 8;
    g.prs1 = c.lds + L_RES + (2 * (c.w & 1) + (c.w >> 1)) * 512 + ((c.w & 1) ^ 1) * 2048 + c.lane * 8;
    g.exw = c.lds + L_RING + c.w * 2048 + c.lane * 16;
    g.exr = c.lds + L_RING + (c.w ^ 2) * 2048 + c.lane * 16;
    g.sts = c.lds + L_ST + 1024 + c.lane * 16;
    g.stw = c.lds + L_ST + 1024 + (c.lane ^ ((c.w & 1) * 32)) * 16 + c.w * 4;
    g.vo = selu((u64)((long long)g.lanes >> 1), (((c.q ^ c.w) & 1) * 16 + c.l15) * 1024 + (c.q >> 1) * 16, (2 * c.q + (c.lane & 1)) * 8192 + ((c.lane ^ (2 * c.w)) & 2) * 32 + (c.l15 >> 2) * 16);
    g.va = (c.l15 & 3) * 4096 + (c.l15 >> 2) * 16 + (c.q >> 1) * 64 + (c.q & 1) * 8;
    asm volatile("" : "+v"(g.ring[0]), "+v"(g.ring[1]), "+v"(g.vst), "+v"(g.pk), "+v"(g.v16), "+v"(g.pws), "+v"(g.rd[0]), "+v"(g.rd[1]), "+v"(g.rd[2]), "+v"(g.rd[3]));
    asm volatile("" : "+v"(g.prs), "+v"(g.prs1), "+v"(g.exw), "+v"(g.exr), "+v"(g.sts), "+v"(g.stw), "+v"(g.vo), "+v"(g.va));
    u32x4 Bc[4];
    vm_wait2<19, 5>(layer);
    {
      u32 mx = 0;
#pragma unroll
      for (int i = 0; i < 4; ++i) mx = umax5(mx, lds16(g.rd[0] + i * 1024));
      g1_verify<0>(c, g, mx);
    }
    BARRIER(); FENCE();
#pragma unroll
    for (int ct = 0; ct < 4; ++ct) Bc[ct] = tr_frag(g.ring[0] + ct * 4096);
    STAMP(11 + layer * 20);
    g1_unit<0>(c, g, Bc, [&]() {
      vm_wait<11>();
      BARRIER(); FENCE();
#pragma unroll
      for (int ct = 0; ct < 4; ++ct) Bc[ct] = tr_frag(g.ring[1] + ct * 4096);
    });
    STAMP(13 + layer * 20);
    f32x4 acc2[4];
    u32x4 Bd[8];
    u32x2 rr[2];
    g1_unit<1>(c, g, Bc, [&]() {
      vm_wait<6>();
      BARRIER(); FENCE();
      g2_res<0>(g, rr);
      g2_first(g, Bd);
    });
    g2_init(rr, acc2);
    STAMP(15 + layer * 20);
    ConvIn nA, nB;
    ConvF c2;
    g2_unit<0>(c, g, p, layer, acc2, Bd, nA);
    g2_res<1>(g, rr);
    vm_wait<13>();
    {
      u32 mx = 0;
#pragma unroll
      for (int ks = 0; ks < 4; ++ks) Bd[2 * ks] = lds16(g2_frag<0>(g, 0, ks));
      PIN();
      g2_init(rr, acc2);
#pragma unroll
      for (int ks = 0; ks < 4; ++ks) mx = umax3(umax3(mx, Bd[2 * ks].x, Bd[2 * ks].y), Bd[2 * ks].z, Bd[2 * ks].w);
      g2_verify<0>(c, g, Bd, mx);
      BARRIER(); FENCE();
#pragma unroll
      for (int ks = 0; ks < 4; ++ks) Bd[2 * ks + 1] = lds16(g2_frag<0>(g, 1, ks));
    }
    g2_unit<1>(c, g, p, layer, acc2, Bd, nA);
    if (layer == 0) {
      if (LD_A == 90) conv_load<1, 0>(c, nA);
      conv_again(c, cf, g.v16, c2);
      conv_pad(c);
      ffn_next(g);
      conv_phase<1>(c, c2, g, dsk, nA, nB);
    }
  }
  if (blockIdx.x == 0 && threadIdx.x == 0) __hip_atomic_store((GAS u32*)(c.ws + OFF_EPOCH), c.epoch, RLX_AGENT);
  asm volatile(".Lmega_tail:");
}

}

extern "C" void kernel_launch(void* const* d_in, const int* in_sizes, int n_in, void* d_out, int out_size, void* d_ws,
                              size_t ws_size, hipStream_t stream) {
  Params p;
  p.x0 = (const u16*)d_in[0];
  p.logA = (const u16*)d_in[1];
  p.Aim = (const u16*)d_in[2];
  p.Cre = (const u16*)d_in[3];
  p.Cim = (const u16*)d_in[4];
  p.logdt = (const u16*)d_in[5];
  p.Dskip = (const u16*)d_in[6];
  p.wg = (const u16*)d_in[8];
  p.wu = (const u16*)d_in[9];
  p.wd = (const u16*)d_in[10];
  p.out = (u16*)d_out;
  p.ws = (char*)d_ws;
  mega_kernel<<<NBLK, 256, 0, stream>>>(p);
}
```
